```python
import torch

def _softmax(x, axis=-1):
    return torch.softmax(x, dim=axis)
def _gelu(x):
    return torch.nn.functional.gelu(x, approximate="tanh")
def _silu(x):
    return torch.nn.functional.silu(x)
def _sigmoid(x):
    return torch.sigmoid(x)
def _layernorm(x, axis=-1, eps=1e-5):
    m = x.mean(dim=axis, keepdim=True)
    v = x.var(dim=axis, keepdim=True, unbiased=False)
    return (x - m) / torch.sqrt(v + eps)
def _scatter_add(tgt, idx, src, axis=0):
    return tgt.clone().index_add_(axis, idx, src)
def _segment_sum(src, seg, n):
    out = torch.zeros((n,) + src.shape[1:], dtype=src.dtype)
    return out.index_add_(0, seg, src)
def _pad(x, pw):
    flat = tuple(v for pair in reversed(pw) for v in pair)
    return torch.nn.functional.pad(x, flat)

kernel_name = 'recurrent_scan__ssm_conv_s5__mixer_s_mlp__prompt'

def setup_inputs():

    _w = (lambda a: (lambda t: t.to(torch.bfloat16) if t.is_floating_point() else t)(torch.as_tensor(a)))
    def _A_setup():
        B, S, D = 1, 2048, 640
        P = D
        Lambda_re = -0.5 * (lambda _x: torch.exp(torch.as_tensor(_x)))((lambda *s: torch.randn(*s, dtype=torch.bfloat16) * 0.2)(P))
        Lambda_im = (lambda *s: torch.randn(*s, dtype=torch.bfloat16) * 0.4)(P) * 1.0
        B_re = (lambda *s: torch.randn(*s, dtype=torch.bfloat16) * 0.2)(P, D) * (D ** -0.5)
        B_im = (lambda *s: torch.randn(*s, dtype=torch.bfloat16) * 0.2)(P, D) * (D ** -0.5)
        C_re = (lambda *s: torch.randn(*s, dtype=torch.bfloat16) * 0.2)(D, P) * (P ** -0.5)
        C_im = (lambda *s: torch.randn(*s, dtype=torch.bfloat16) * 0.2)(D, P) * (P ** -0.5)
        log_dt = (lambda *s: torch.randn(*s, dtype=torch.bfloat16) * 0.2)(P) * 0.3 - 2.0
        Dskip = (lambda *s: torch.randn(*s, dtype=torch.bfloat16) * 0.05)(D)
        return {
            "x": _w((lambda *s: torch.randn(*s, dtype=torch.bfloat16) * 0.05)(B, S, D)),
            "Lambda_re": _w(Lambda_re),
            "Lambda_im": _w(Lambda_im),
            "B_re": _w(B_re),
            "B_im": _w(B_im),
            "C_re": _w(C_re),
            "C_im": _w(C_im),
            "log_dt": _w(log_dt),
            "Dskip": _w(Dskip),
        }
    _A_in = _A_setup()

    def _B_setup():
        B, S, D, F = 1, 2048, 640, 1536
        return {
            "x":  _w((lambda *s: torch.randn(*s, dtype=torch.bfloat16) * 0.05)(B, S, D)),
            "w1": _w((lambda *s: torch.randn(*s, dtype=torch.bfloat16) * 3.2)(D, D) * (D ** -0.5)),
            "w2": _w((lambda *s: torch.randn(*s, dtype=torch.bfloat16) * 3.2)(D, D) * (D ** -0.5)),
            "w3": _w((lambda *s: torch.randn(*s, dtype=torch.bfloat16) * 3.2)(D, F) * (D ** -0.5)),
            "w4": _w((lambda *s: torch.randn(*s, dtype=torch.bfloat16) * 1.6)(F, D) * (F ** -0.5)),
        }
    _B_in = _B_setup()

    return {
        "A_x": _A_in["x"],
        "A_Lambda_re": _A_in["Lambda_re"],
        "A_Lambda_im": _A_in["Lambda_im"],
        "A_B_re": _A_in["B_re"],
        "A_B_im": _A_in["B_im"],
        "A_C_re": _A_in["C_re"],
        "A_C_im": _A_in["C_im"],
        "A_log_dt": _A_in["log_dt"],
        "A_Dskip": _A_in["Dskip"],
        "B_x": _B_in["x"],
        "B_w1": _B_in["w1"],
        "B_w2": _B_in["w2"],
        "B_w3": _B_in["w3"],
        "B_w4": _B_in["w4"],
    }

def reference(A_x, A_Lambda_re, A_Lambda_im, A_B_re, A_B_im, A_C_re, A_C_im, A_log_dt, A_Dskip, B_x, B_w1, B_w2, B_w3, B_w4):
    def _A_ref(x, Lambda_re, Lambda_im, B_re, B_im, C_re, C_im, log_dt, Dskip):
        Bsz, L, H = x.shape
        Lam = Lambda_re + 1j * Lambda_im
        Bmat = B_re + 1j * B_im
        Cmat = C_re + 1j * C_im
        dt = torch.exp(log_dt)
        Lam_bar = torch.exp(dt * Lam)
        B_bar = ((Lam_bar - 1.0) / Lam)[:, None] * Bmat
        Bu = torch.einsum("bld,pd->blp", x + 0j, B_bar)
        h = torch.zeros_like(Bu[:, 0, :])
        hs = []
        for t in range(L):
            h = Lam_bar[None, :] * h + Bu[:, t, :]
            hs.append(h)
        hseq = torch.stack(hs, 1)
        y = 2.0 * torch.einsum("blp,dp->bld", hseq, Cmat).real + x * Dskip[None, None, :]
        return y

    def _B_ref(x, w1, w2, w3, w4):
        B, S, D = x.shape
        h = _gelu(_layernorm(x, axis=-1) @ w1)
        c = D // 4
        g0 = _pad(h[:, :, 0*c:1*c], ((0, 0), (1, 0), (0, 0)))[:, :S, :]
        g1 = _pad(h[:, :, 1*c:2*c], ((0, 0), (0, 1), (0, 0)))[:, 1:, :]
        g2 = _pad(h[:, :, 2*c:3*c], ((0, 0), (2, 0), (0, 0)))[:, :S, :]
        g3 = _pad(h[:, :, 3*c:   ], ((0, 0), (0, 2), (0, 0)))[:, 2:, :]
        hs = torch.concatenate([g0, g1, g2, g3], axis=-1) @ w2
        x1 = x + hs
        h2 = _gelu(_layernorm(x1, axis=-1) @ w3) @ w4
        return x1 + h2

    x = A_x
    xn = _layernorm(x, axis=-1)
    s = _A_ref(xn, A_Lambda_re, A_Lambda_im, A_B_re, A_B_im, A_C_re, A_C_im, A_log_dt, A_Dskip)
    g = _silu(_B_ref(xn, B_w1, B_w2, B_w3, B_w4))
    return x + s * g
```

```cpp
#include <hip/hip_runtime.h>
#include <hip/hip_bf16.h>
#include <stdint.h>

#define DI __device__ __forceinline__
#define LAS __attribute__((address_space(3)))
#define AINL __attribute__((always_inline))
typedef unsigned u32;
typedef unsigned long long u64;
typedef u32 u32x4 __attribute__((ext_vector_type(4)));
typedef u32 u32x2 __attribute__((ext_vector_type(2)));
typedef float f32x4 __attribute__((ext_vector_type(4)));
typedef float f32x2 __attribute__((ext_vector_type(2)));
typedef __bf16 bf16x8 __attribute__((ext_vector_type(8)));
typedef __bf16 bf16x2 __attribute__((ext_vector_type(2)));
typedef short s16x4 __attribute__((ext_vector_type(4)));
typedef short s16x8 __attribute__((ext_vector_type(8)));

#define XMAP 1
#define ABL 0
#define PSLEEP 2
#define STAMPS 0

constexpr u32 WS_EPOCH = 0;
constexpr u32 WS_FLAG = 0x1000;
constexpr u32 FL_1 = 0, FL_2 = 160, FL_3 = 320, FL_A = 480, FL_S = 560, FL_C = 640;
constexpr u32 WS_XS = 0x20000;
constexpr u32 WS_CS3 = 0x2A000;
constexpr u32 WS_EG = 0x30000;
constexpr u32 WS_ST1 = 0x60000;
constexpr u32 WS_SS = 0xC0000;
constexpr u32 WS_H1 = 0x400000;
constexpr u32 WS_X1 = 0x680000;
constexpr u32 WS_HH = 0x900000;
constexpr u32 WS_H2 = 0xE00000;
constexpr u32 WS_STAMP = 0x1400000;
constexpr u32 WS_BYTES = 0x1500000;
constexpr u32 OOB = 0x7FFFFFF0u;
constexpr float LN_EPS = 1e-5f;
constexpr int SPIN_CAP = 1 << 17;

template <int I> struct IC { static constexpr int v = I; };
template <int B, int E, class F> DI void sfor(F&& f) {
  if constexpr (B < E) { f(IC<B>{}); sfor<B + 1, E>(f); }
}

#if defined(__HIP_DEVICE_COMPILE__) && __HIP_DEVICE_COMPILE__
typedef __amdgpu_buffer_rsrc_t rsrc_t;
DI rsrc_t mk_rsrc(const void* p, u32 bytes) { return __builtin_amdgcn_make_buffer_rsrc((void*)p, 0, bytes, 0x00020000); }

template <int SC1> DI void dma1(u32 lds, u32 voff, rsrc_t r, u32 soff) {
  if (ABL == 2) return;
  if constexpr (SC1)
    asm volatile("s_mov_b32 m0, %0\n\ts_nop 0\n\tbuffer_load_dwordx4 %1, %2, %3 offen sc1 lds" ::"s"(lds), "v"(voff), "s"(r), "s"(soff) : "memory");
  else
    asm volatile("s_mov_b32 m0, %0\n\ts_nop 0\n\tbuffer_load_dwordx4 %1, %2, %3 offen lds" ::"s"(lds), "v"(voff), "s"(r), "s"(soff) : "memory");
}
template <int SC1> DI void dma2(u32 lds, u32 v0, u32 v1, rsrc_t r, u32 soff) {
  if (ABL == 2) return;
  if constexpr (SC1)
    asm volatile("s_mov_b32 m0, %0\n\ts_nop 0\n\tbuffer_load_dwordx4 %1, %3, %4 offen sc1 lds\n\tbuffer_load_dwordx4 %2, %3, %4 offen offset:1024 sc1 lds"
                 ::"s"(lds), "v"(v0), "v"(v1), "s"(r), "s"(soff) : "memory");
  else
    asm volatile("s_mov_b32 m0, %0\n\ts_nop 0\n\tbuffer_load_dwordx4 %1, %3, %4 offen lds\n\tbuffer_load_dwordx4 %2, %3, %4 offen offset:1024 lds"
                 ::"s"(lds), "v"(v0), "v"(v1), "s"(r), "s"(soff) : "memory");
}
template <int SC1> DI void dma4(u32 lds, u32 v, rsrc_t r, u32 soff) {
  if (ABL == 2) return;
  if constexpr (SC1)
    asm volatile("s_mov_b32 m0, %0\n\ts_nop 0\n\tbuffer_load_dwordx4 %1, %2, %3 offen sc1 lds\n\tbuffer_load_dwordx4 %1, %2, %3 offen offset:1024 sc1 lds\n\t"
                 "buffer_load_dwordx4 %1, %2, %3 offen offset:2048 sc1 lds\n\tbuffer_load_dwordx4 %1, %2, %3 offen offset:3072 sc1 lds"
                 ::"s"(lds), "v"(v), "s"(r), "s"(soff) : "memory");
  else
    asm volatile("s_mov_b32 m0, %0\n\ts_nop 0\n\tbuffer_load_dwordx4 %1, %2, %3 offen lds\n\tbuffer_load_dwordx4 %1, %2, %3 offen offset:1024 lds\n\t"
                 "buffer_load_dwordx4 %1, %2, %3 offen offset:2048 lds\n\tbuffer_load_dwordx4 %1, %2, %3 offen offset:3072 lds"
                 ::"s"(lds), "v"(v), "s"(r), "s"(soff) : "memory");
}
constexpr int VC(int n) { return ABL == 2 ? 0 : n; }
template <int N> DI void waitv() { asm volatile("s_waitcnt vmcnt(%0)" ::"i"(VC(N)) : "memory"); }
DI void bar() { asm volatile("s_waitcnt lgkmcnt(0)\n\ts_barrier" ::: "memory"); }
template <int N> DI void waitbar() { asm volatile("s_waitcnt vmcnt(%0) lgkmcnt(0)\n\ts_barrier" ::"i"(VC(N)) : "memory"); }

DI void touch(u32& d, u32 voff, rsrc_t r) { asm volatile("buffer_load_dword %0, %1, %2, 0 offen" : "+v"(d) : "v"(voff), "s"(r) : "memory"); }

DI u32x4 bld128(u32 voff, rsrc_t r, u32 soff) { return __builtin_amdgcn_raw_buffer_load_b128(r, voff, soff, 0); }
DI u32x2 bld64(u32 voff, rsrc_t r, u32 soff) { return __builtin_amdgcn_raw_buffer_load_b64(r, voff, soff, 0); }
DI u32x2 bld64_sc1(u32 voff, rsrc_t r, u32 soff) { return __builtin_amdgcn_raw_buffer_load_b64(r, voff, soff, 16); }
DI u32 bld32(u32 voff, rsrc_t r, u32 soff) { return __builtin_amdgcn_raw_buffer_load_b32(r, voff, soff, 0); }
DI u32 bld16(u32 voff, rsrc_t r) { return __builtin_amdgcn_raw_buffer_load_b16(r, voff, 0, 0); }
DI void st128_sc1(u32x4 v, u32 voff, rsrc_t r) { __builtin_amdgcn_raw_buffer_store_b128(v, r, voff, 0, 16); }
DI void st64_sc1(u32x2 v, u32 voff, rsrc_t r) { __builtin_amdgcn_raw_buffer_store_b64(v, r, voff, 0, 16); }
DI void st32_sc1(u32 v, u32 voff, rsrc_t r) { __builtin_amdgcn_raw_buffer_store_b32(v, r, voff, 0, 16); }

DI void poll_flags(u32 voff, bool active, rsrc_t ws, u32 ta, u32 tb) {
  _Pragma("nounroll") for (int it = 0; it < SPIN_CAP; ++it) {
    u32x2 v;
    asm volatile("buffer_load_dwordx2 %0, %1, %2, 0 offen sc1\n\ts_waitcnt vmcnt(0)" : "=&v"(v) : "v"(voff), "s"(ws) : "memory");
    const bool ok = !active || (v.x == ta && v.y == tb);
    if (__all(ok)) break;
    __builtin_amdgcn_s_sleep(1);
  }
}
DI u32x4 poll_granule(u32 voff, bool active, rsrc_t ws, u32 ta, u32 tb) {
  u32x4 v;
  _Pragma("nounroll") for (int it = 0; it < SPIN_CAP; ++it) {
    asm volatile("buffer_load_dwordx4 %0, %1, %2, 0 offen sc1\n\ts_waitcnt vmcnt(0)" : "=&v"(v) : "v"(voff), "s"(ws) : "memory");
    const bool ok = !active || (v.y == ta && v.w == tb);
    if (__all(ok)) break;
    __builtin_amdgcn_s_sleep(1);
  }
  return v;
}

DI float asf(u32 x) { return __uint_as_float(x); }
DI u32 asu(float x) { return __float_as_uint(x); }
DI float bflo(u32 x) { return asf(x << 16); }
DI float bfhi(u32 x) { return asf(x & 0xffff0000u); }
DI u32 pk2(float a, float b) { f32x2 p = {a, b}; bf16x2 q = __builtin_convertvector(p, bf16x2); return __builtin_bit_cast(u32, q); }
DI float sum16(float x) { const u32 u = asu(x); auto r = __builtin_amdgcn_permlane16_swap(u, u, false, false); const u32 a = r[0], b = r[1]; return asf(a) + asf(b); }
DI float sum32(float x) { const u32 u = asu(x); auto r = __builtin_amdgcn_permlane32_swap(u, u, false, false); const u32 a = r[0], b = r[1]; return asf(a) + asf(b); }
template <int C> DI float dppadd(float x) { return x + asf(__builtin_amdgcn_update_dpp(0u, asu(x), C, 0xf, 0xf, true)); }
DI float wavesum(float x) { x = dppadd<0xB1>(x); x = dppadd<0x4E>(x); x = dppadd<0x141>(x); x = dppadd<0x140>(x); return sum32(sum16(x)); }
DI void gather4(float x, float (&e)[4]) {
  const u32 u = asu(x);
  auto p = __builtin_amdgcn_permlane16_swap(u, u, false, false);
  const u32 a = p[0], b = p[1];
  auto pa = __builtin_amdgcn_permlane32_swap(a, a, false, false);
  auto pb = __builtin_amdgcn_permlane32_swap(b, b, false, false);
  const u32 e0 = pa[0], e2 = pa[1], e1 = pb[0], e3 = pb[1];
  e[0] = asf(e0); e[1] = asf(e1); e[2] = asf(e2); e[3] = asf(e3);
}
DI float gelu_f(float v) {
  const float c1 = -2.302208198f, c2 = -0.1029432397f;
  const float e = __builtin_amdgcn_exp2f(v * __builtin_fmaf(v * v, c2, c1));
  return v * __builtin_amdgcn_rcpf(1.0f + e);
}
DI float silu_f(float z) { return z * __builtin_amdgcn_rcpf(1.0f + __builtin_amdgcn_exp2f(z * -1.4426950409f)); }
DI f32x4 mfma(bf16x8 a, bf16x8 b, f32x4 c) { if (ABL == 1 || ABL == 4) { asm volatile("" : "+v"(c) : "v"(a), "v"(b)); return c; } return __builtin_amdgcn_mfma_f32_16x16x32_bf16(a, b, c, 0, 0, 0); }
DI bf16x8 lds128(LAS char* p) { if (ABL == 1 || ABL == 3) { bf16x8 r; asm volatile("" : "=v"(r)); return r; } return *(LAS bf16x8*)p; }
DI bf16x8 ldstr(LAS char* p0, LAS char* p1) {
  if (ABL == 1 || ABL == 3) { bf16x8 r; asm volatile("" : "=v"(r)); return r; }
  const s16x4 a = __builtin_amdgcn_ds_read_tr16_b64_v4i16((LAS s16x4*)p0);
  const s16x4 b = __builtin_amdgcn_ds_read_tr16_b64_v4i16((LAS s16x4*)p1);
  const s16x8 w = __builtin_shufflevector(a, b, 0, 1, 2, 3, 4, 5, 6, 7);
  return __builtin_bit_cast(bf16x8, w);
}
DI u32x4 pack8(f32x4 a, f32x4 b) { u32x4 r; r.x = pk2(a.x, a.y); r.y = pk2(a.z, a.w); r.z = pk2(b.x, b.y); r.w = pk2(b.z, b.w); return r; }
DI u32x2 pack4(f32x4 a) { u32x2 r; r.x = pk2(a.x, a.y); r.y = pk2(a.z, a.w); return r; }
DI int wrapk(int k, int n) { return k >= n ? k - n : k; }
#define SB() __builtin_amdgcn_sched_barrier(0)
DI f32x4 keep(f32x4 v) { asm volatile("" : "+v"(v)); return v; }
DI LAS char* pin(LAS char* p) { asm volatile("" : "+v"(p)); return p; }
#endif

#define STAMP(i) do { } while (0)
#define CSTAMP(i) do { } while (0)

#define ROT 0
__global__ __launch_bounds__(768, 3) void s5_mixer_kernel(
    const void* __restrict__ x, const void* __restrict__ lam_re, const void* __restrict__ lam_im,
    const void* __restrict__ b_re, const void* __restrict__ b_im, const void* __restrict__ c_re,
    const void* __restrict__ c_im, const void* __restrict__ log_dt, const void* __restrict__ dskip,
    const void* __restrict__ w1, const void* __restrict__ w2, const void* __restrict__ w3,
    const void* __restrict__ w4, void* __restrict__ out, void* __restrict__ ws) {
#if defined(__HIP_DEVICE_COMPILE__) && __HIP_DEVICE_COMPILE__
  __shared__ __attribute__((aligned(16))) char smem_[163840];
  LAS char* const sm = (LAS char*)smem_;
  const u32 L0 = (u32)(size_t)sm;
  const int tid = threadIdx.x, l = tid & 63;
  const int w = __builtin_amdgcn_readfirstlane(tid >> 6);
  const int bid = blockIdx.x, xcd = bid & 7, rr = bid >> 3;
  const int m = (xcd >> 1) * 4 + (rr < 20 ? rr / 5 : (rr - 20) / 3);
  const int role = rr < 20 ? rr % 5 + 5 * (xcd & 1) : 10 + (rr - 20) % 3 + 3 * (xcd & 1);
  const int g = l >> 4, li = l & 15, r8 = l >> 3, tq = w & 3, cq = w >> 2, L = w - 8;
  const rsrc_t rws = mk_rsrc(ws, WS_BYTES);
  const rsrc_t rx = mk_rsrc(x, 2048 * 1280);
  STAMP(0); CSTAMP(30);
  const int fm = (5 * (m & 3) + 1) >> 1;
  const int rot1 = (2 * (role % 5) + fm) % 10, rotA1 = (2 * (role % 3) + 1 + fm) % 10;
  const int rot10 = 0, rot24 = 0, rotA3 = 0;

  u32 ta, tb;
  auto row_stats = [&]() AINL {
    const int stok = m * 128 + role * 8 + w;
    const u32x2 ep = bld64_sc1(0, rws, 0);
    const u32x4 ra = bld128(l * 16, rx, stok * 1280);
    const u32 rb_ = bld32(1024 + l * 4, rx, stok * 1280);
    const u64 e = (((u64)__builtin_amdgcn_readfirstlane(ep.y) << 32) | (u32)__builtin_amdgcn_readfirstlane(ep.x)) + 0x9E3779B97F4A7C15ull;
    ta = (u32)e; tb = (u32)(e >> 32);
    float v[10] = {bflo(ra.x), bfhi(ra.x), bflo(ra.y), bfhi(ra.y), bflo(ra.z), bfhi(ra.z), bflo(ra.w), bfhi(ra.w), bflo(rb_), bfhi(rb_)};
    float s = 0.f;
#pragma unroll
    for (int i = 0; i < 10; ++i) s += v[i];
    const float mu = wavesum(s) * (1.0f / 640.0f);
    float q = 0.f;
#pragma unroll
    for (int i = 0; i < 10; ++i) { const float d = v[i] - mu; q = __builtin_fmaf(d, d, q); }
    const float rs = 1.0f / sqrtf(wavesum(q) * (1.0f / 640.0f) + LN_EPS);
    u32x4 gr = {asu(mu), ta, asu(rs), tb};
    st128_sc1(gr, l == 0 ? WS_XS + stok * 16 : OOB, rws);
  };
  auto put_tag = [&](u32 off) AINL { if (tid == 0) *(LAS u32x2*)(sm + off) = (u32x2){ta, tb}; };
  auto get_tag = [&](u32 off) AINL {
    const u32x2 t = *(volatile LAS u32x2*)(sm + off);
    ta = __builtin_amdgcn_readfirstlane(t.x); tb = __builtin_amdgcn_readfirstlane(t.y);
  };
  auto set_flag = [&](u32 idx) AINL { st64_sc1((u32x2){ta, tb}, l == 0 ? WS_FLAG + idx * 128 : OOB, rws); };
  auto await = [&](u32 own, int i, bool act, auto&& first3) AINL {
    bar();
    if (L == 0) { set_flag(own); poll_flags(WS_FLAG + i * 128, act, rws, ta, tb); }
    bar();
    first3();
  };

  const u32 rb0 = li * 128 + ((g ^ (li >> 1)) << 4);
  const bf16x8 ones = __builtin_bit_cast(bf16x8, (u32x4){0x3F803F80u, 0x3F803F80u, 0x3F803F80u, 0x3F803F80u});
  const f32x4 zero4 = {0.f, 0.f, 0.f, 0.f};

  if (role < 10) {
    const int j = role;
    constexpr u32 ACT = 0, W = 65536;
    if (w >= 8) {
      const rsrc_t rw1 = mk_rsrc(w1, 640 * 1280), rw2 = mk_rsrc(w2, 640 * 1280);
      const rsrc_t rw3 = mk_rsrc(w3, 640 * 3072), rw4 = mk_rsrc(w4, 1536 * 1280);
      const u32 vx0 = (m * 128 + L * 32 + r8) * 1280 + (((l & 7) ^ g) << 4);
      const u32 vx1 = (m * 128 + L * 32 + 8 + r8) * 1280 + (((l & 7) ^ (4 + g)) << 4) - 1024;
      const u32 wch = j * 128 + (((l & 7) ^ (2 * g)) << 4);
      const u32 vq0 = ((L >> 1) * 32 + (r8 >> 2) * 8 + (L & 1) * 4 + (r8 & 3)) * 1280 + wch, vq1 = vq0 + 16 * 1280 - 1024;
      const u32 ldsA = L0 + ACT + L * 4096, ldsW = L0 + W + L * 2048;
      auto issue_1 = [&](int s) AINL {
        const int kt = wrapk(s + rot1, 10);
        dma2<0>(ldsA + (s & 3) * 16384, vx0, vx1, rx, kt * 128);
        dma2<0>(ldsW + 65536 + (s & 3) * 8192, vq0, vq1, rw1, kt * 81920);
        dma2<0>(ldsA + (s & 3) * 16384 + 2048, vx0, vx1, rx, kt * 128 + 20480);
      };
      issue_1(0); issue_1(1); issue_1(2);
      const u32 vw0 = (L * 16 + r8) * 1280 + wch, vw1 = vw0 + 10240 - 1024;
      const u32 va = L * 4096 + l * 16, vl = L * 1024 + l * 16;
      u32 v3[5];
#pragma unroll
      for (int i = 0; i < 5; ++i) {
        const int c = 64 * i + l, r = c / 20, pos = c - 20 * r;
        v3[i] = (L * 16 + r) * 3072 + j * 320 + ((pos ^ (2 * ((r >> 2) & 1))) << 4) - 1024 * (i & 1);
      }
      auto issue_w2 = [&](int s) AINL { dma2<0>(ldsW + s * 8192, vw0, vw1, rw2, wrapk(s + rot10, 10) * 81920); };
      auto issue_w3 = [&](int s) AINL {
        const u32 d = L0 + W + (s & 3) * 20480 + L * 5120, so = wrapk(s + rot10, 10) * 196608;
        dma2<0>(d, v3[0], v3[1], rw3, so); dma2<0>(d + 2048, v3[2], v3[3], rw3, so); dma1<0>(d + 4096, v3[4], rw3, so);
      };
      auto issue_w4 = [&](int s) AINL { dma2<0>(ldsW + ((s + 5) % 12) * 8192, vw0, vw1, rw4, wrapk(s + rot24, 24) * 81920); };
      auto issue_a = [&](int s, u32 base) AINL { dma4<1>(ldsA + (s & 3) * 16384, va, rws, base); };
      sfor<0, 10>([&](auto ic) AINL {
        constexpr int s = ic.v;
        constexpr int gs[13] = {6, 6, 6, 6, 6, 6, 6, 6, 6, 6, 5, 6, 6};
        waitbar<gs[s + 1] + gs[s + 2]>();
        if constexpr (s + 3 < 10) issue_1(s + 3);
        else if constexpr (s == 7) { dma1<1>(L0 + ACT + 2 * 16384 + L * 1024, vl, rws, WS_XS + m * 2048); issue_w2(0); issue_w2(1); }
        else { issue_w2(3 * s - 22); issue_w2(3 * s - 21); issue_w2(3 * s - 20); }
        if constexpr (s == 0) get_tag(W);
      });
      waitbar<16>();
      issue_w2(8); issue_w2(9);
      await(FL_1 + m * 10 + j, FL_1 + m * 10 + (l < 10 ? l : l < 16 ? -10 + (l - 10) + (l >= 13 ? 2 : 0) : 12 + (l - 16) + (l >= 19 ? 2 : 0)),
            l < 10 || (l < 16 && m > 0) || (l >= 16 && l < 22 && m < 15), [&]() AINL {
        sfor<0, 3>([&](auto ic) AINL { constexpr int s = ic.v; issue_a(s, WS_H1 + (m * 10 + wrapk(s + rot10, 10)) * 16384); });
      });
      sfor<0, 10>([&](auto ic) AINL {
        constexpr int s = ic.v;
        constexpr int gs[13] = {4, 4, 4, 4, 4, 4, 4, 4, 4, 4, 5, 5, 5};
        waitbar<gs[s + 1] + gs[s + 2]>();
        if constexpr (s + 3 < 10) issue_a(s + 3, WS_H1 + (m * 10 + wrapk(s + 3 + rot10, 10)) * 16384);
        else issue_w3(s - 7);
      });
      bar();
      issue_w3(3);
      await(FL_2 + m * 10 + j, l < 10 ? FL_2 + m * 10 + l : FL_C + ((5 * j) >> 2) + l - 10, l < (j < 9 ? 12 : 11), [&]() AINL {
        sfor<0, 3>([&](auto ic) AINL { constexpr int s = ic.v; issue_a(s, WS_X1 + (m * 10 + wrapk(s + rot10, 10)) * 16384); });
      });
      sfor<0, 10>([&](auto ic) AINL {
        constexpr int s = ic.v;
        constexpr int gs[13] = {4, 4, 4, 4, 9, 9, 9, 9, 9, 9, 8, 8, 8};
        waitbar<gs[s + 1] + gs[s + 2]>();
        if constexpr (s + 3 < 10) {
          issue_a(s + 3, WS_X1 + (m * 10 + wrapk(s + 3 + rot10, 10)) * 16384);
          if constexpr (s >= 1) issue_w3(s + 3);
        } else if constexpr (s == 7) {
          issue_a(2, WS_ST1 + m * 20480);
          issue_w4(0); issue_w4(1);
        } else if constexpr (s == 8) {
          dma1<1>(L0 + ACT + 3 * 16384 + L * 1024, vl, rws, WS_ST1 + m * 20480 + 16384);
          dma1<1>(L0 + ACT + 3 * 16384 + 4096 + L * 1024, vl, rws, WS_CS3 + j * 640);
          issue_w4(2); issue_w4(3); issue_w4(4);
        } else { issue_w4(5); issue_w4(6); issue_w4(7); issue_w4(8); }
      });
      waitbar<14>();
      issue_w4(9); issue_w4(10); issue_w4(11);
      await(FL_3 + m * 10 + j, FL_3 + m * 10 + l, l < 10, [&]() AINL {
        sfor<0, 3>([&](auto ic) AINL { constexpr int s = ic.v; issue_a(s, WS_H2 + (m * 24 + wrapk(s + rot24, 24)) * 16384); });
      });
      u32x2 fs;
      const u32 fso = WS_FLAG + (FL_S + m * 5 + (j >> 1)) * 128;
      sfor<0, 24>([&](auto ic) AINL {
        constexpr int s = ic.v;
        constexpr int gs[27] = {4, 4, 4, 4, 6, 6, 6, 6, 6, 6, 6, 6, 6, 6, 6, 6, 4, 4, 4, 4, 5, 4, 4, 4, 4, 0, 0};
        if constexpr (s == 20) { asm volatile("s_waitcnt vmcnt(%1) lgkmcnt(0)\n\ts_barrier" : "+v"(fs) : "i"(VC(gs[s + 1] + gs[s + 2])) : "memory"); }
        else waitbar<gs[s + 1] + gs[s + 2]>();
        if constexpr (s == 17) asm volatile("buffer_load_dwordx2 %0, %1, %2, 0 offen sc1" : "=v"(fs) : "v"(fso), "s"(rws) : "memory");
        if constexpr (s + 3 < 24) issue_a(s + 3, WS_H2 + (m * 24 + wrapk(s + 3 + rot24, 24)) * 16384);
        if constexpr (s >= 1 && s <= 12) issue_w4(s + 11);
        if constexpr (s == 21) {
          if (!__all(fs.x == ta && fs.y == tb)) poll_flags(fso, true, rws, ta, tb);
          issue_a(0, WS_SS + (m * 10 + j) * 16384);
        }
      });
      waitbar<0>();
    } else {
      row_stats();
      put_tag(W);
      const int q4 = li >> 2, p4 = l & 3;
      LAS char* const wb0 = pin(sm + W + (4 * g + q4) * 128 + ((((2 * cq) ^ ((2 * g + (q4 >> 1)) & 3))) << 5) + 8 * p4);
      LAS char* const wb1 = pin(sm + W + (4 * g + q4) * 128 + ((((2 * cq + 1) ^ ((2 * g + (q4 >> 1)) & 3))) << 5) + 8 * p4);
      LAS char* const wh0 = pin(wb0 + 65536);
      LAS char* const wh1 = pin(wb1 + 65536);
      LAS char* const ab = pin(sm + ACT + tq * 4096 + l * 16);
      struct F64 { bf16x8 a0, a1, b0, b1; };
      F64 F0, F1;
      f32x4 acc[2][2], cs[2];
      u32x2 xs[2][2];
      auto step64 = [&](auto mf, auto cs_on, F64& R, LAS char* pb, u32 wo, const F64& M) AINL {
        LAS char* const p0 = wo >= 65536 ? wh0 + (wo - 65536) : wb0 + wo;
        LAS char* const p1 = wo >= 65536 ? wh1 + (wo - 65536) : wb1 + wo;
        R.a0 = ldstr(p0, p0 + 2048); SB();
        if constexpr (mf.v) acc[0][0] = mfma(M.a0, M.b0, acc[0][0]);
        SB(); R.b0 = lds128(pb); R.b1 = lds128(pb + 2048); SB();
        if constexpr (mf.v) acc[1][0] = mfma(M.a1, M.b0, acc[1][0]);
        SB(); R.a1 = ldstr(p1, p1 + 2048); SB();
        if constexpr (mf.v) { acc[0][1] = mfma(M.a0, M.b1, acc[0][1]); acc[1][1] = mfma(M.a1, M.b1, acc[1][1]); }
        if constexpr (mf.v && cs_on.v) { cs[0] = mfma(M.a0, ones, keep(cs[0])); cs[1] = mfma(M.a1, ones, keep(cs[1])); }
        SB();
      };
      auto mm = [&](const F64& M) AINL {
        acc[0][0] = mfma(M.a0, M.b0, acc[0][0]); acc[1][0] = mfma(M.a1, M.b0, acc[1][0]);
        acc[0][1] = mfma(M.a0, M.b1, acc[0][1]); acc[1][1] = mfma(M.a1, M.b1, acc[1][1]);
      };
      float mu[2], rs[2];
      {
        LAS char* const xb0 = pin(sm + ACT + tq * 4096 + rb0);
        LAS char* const xb1 = pin(sm + ACT + tq * 4096 + (rb0 ^ 64));
        const int s_own = wrapk(j - rot1 + 10, 10);
#pragma unroll
        for (int a = 0; a < 2; ++a) { cs[a] = zero4; acc[a][0] = zero4; acc[a][1] = zero4; }
        STAMP(1);
        sfor<0, 10>([&](auto ic) AINL {
          constexpr int s = ic.v;
          constexpr u32 so = (s & 3) * 16384, wo = 65536 + (s & 3) * 8192;
          bar();
          if constexpr (s == 0) STAMP(16);
          step64(IC<(s > 0)>{}, IC<0>{}, F0, xb0 + so, wo, F1);
          if (s > 0 && tq == ((2 * s - 1) & 3)) { cs[0] = mfma(F1.a0, ones, keep(cs[0])); cs[1] = mfma(F1.a1, ones, keep(cs[1])); }
          step64(IC<1>{}, IC<0>{}, F1, xb1 + so, wo + 4096, F0);
          if (tq == ((2 * s) & 3)) { cs[0] = mfma(F0.a0, ones, keep(cs[0])); cs[1] = mfma(F0.a1, ones, keep(cs[1])); }
          if (s == s_own) {
#pragma unroll
            for (int tt = 0; tt < 2; ++tt)
#pragma unroll
              for (int ct = 0; ct < 2; ++ct)
                xs[ct][tt] = *(LAS u32x2*)(sm + ACT + so + tq * 4096 + tt * 2048 + li * 128 + (((4 * cq + 2 * ct + (g >> 1)) ^ (li >> 1)) << 4) + 8 * (g & 1));
          }
        });
        mm(F1);
        if (tq == 3) { cs[0] = mfma(F1.a0, ones, cs[0]); cs[1] = mfma(F1.a1, ones, cs[1]); }
        if (li == 0) { *(LAS f32x4*)(sm + ACT + 2 * 16384 + 8192 + w * 128 + g * 16) = cs[0]; *(LAS f32x4*)(sm + ACT + 2 * 16384 + 8192 + w * 128 + 64 + g * 16) = cs[1]; }
        bar();
        STAMP(2);
#pragma unroll
        for (int ct = 0; ct < 2; ++ct) {
          LAS char* const pc = sm + ACT + 2 * 16384 + 8192 + cq * 512 + ct * 64 + g * 16;
          cs[ct] = (*(LAS f32x4*)pc + *(LAS f32x4*)(pc + 128)) + (*(LAS f32x4*)(pc + 256) + *(LAS f32x4*)(pc + 384));
        }
        u32x4 g0 = *(LAS u32x4*)(sm + ACT + 2 * 16384 + (tq * 32 + li) * 16);
        u32x4 g1 = *(LAS u32x4*)(sm + ACT + 2 * 16384 + (tq * 32 + 16 + li) * 16);
        if (!__all(g0.y == ta && g0.w == tb && g1.y == ta && g1.w == tb)) {
          g0 = poll_granule(WS_XS + (m * 128 + tq * 32 + li) * 16, true, rws, ta, tb);
          g1 = poll_granule(WS_XS + (m * 128 + tq * 32 + 16 + li) * 16, true, rws, ta, tb);
        }
        mu[0] = asf(g0.x); rs[0] = asf(g0.z); mu[1] = asf(g1.x); rs[1] = asf(g1.z);
        const int blk = 2 * j + cq, grp = blk / 5;
        const int shift = (grp & 1) ? -(1 + (grp >> 1)) : (1 + (grp >> 1));
        if ((m == 0 && tq == 0 && shift > 0) || (m == 15 && tq == 3 && shift < 0)) {
          const int t = shift > 0 ? li : 2032 + li;
          const bool z = shift > 0 ? t < shift : t >= 2048 + shift;
          const u32 off = WS_H1 + ((t >> 7) * 10 + j) * 16384 + ((t >> 4) & 7) * 2048 + cq * 1024 + l * 16;
          st128_sc1((u32x4){0u, 0u, 0u, 0u}, z ? off : OOB, rws);
        }
#pragma unroll
        for (int tt = 0; tt < 2; ++tt) {
          const float r2 = rs[tt] * __builtin_amdgcn_rsqf(1.0f - LN_EPS * rs[tt] * rs[tt] + LN_EPS);
          f32x4 h[2];
#pragma unroll
          for (int ct = 0; ct < 2; ++ct)
#pragma unroll
            for (int e = 0; e < 4; ++e) h[ct][e] = gelu_f(r2 * __builtin_fmaf(-mu[tt], cs[ct][e], acc[ct][tt][e]));
          const int t = m * 128 + tq * 32 + tt * 16 + li, tp = t + shift;
          const u32 off = WS_H1 + ((tp >> 7) * 10 + j) * 16384 + ((tp >> 4) & 7) * 2048 + cq * 1024 + (g * 16 + (tp & 15)) * 16;
          st128_sc1(pack8(h[0], h[1]), (u32)tp < 2048u ? off : OOB, rws);
          SB();
        }
      }
      STAMP(3);
      waitbar<0>();
      STAMP(4);
      bar();
      STAMP(5);
#pragma unroll
      for (int a = 0; a < 2; ++a) { acc[a][0] = zero4; acc[a][1] = zero4; }
      sfor<0, 10>([&](auto ic) AINL {
        constexpr int s = ic.v;
        bar();
        if constexpr (s == 0) STAMP(17);
        step64(IC<(s > 0)>{}, IC<0>{}, F0, ab + (s & 3) * 16384, s * 8192, F1);
        step64(IC<1>{}, IC<0>{}, F1, ab + (s & 3) * 16384 + 1024, s * 8192 + 4096, F0);
      });
      bar();
      mm(F1);
      STAMP(6);
      u32x4 x1p[2];
      {
        float sm1[2], sq1[2];
#pragma unroll
        for (int tt = 0; tt < 2; ++tt) {
          float s1 = 0.f, s2 = 0.f;
          f32x4 x1[2];
#pragma unroll
          for (int ct = 0; ct < 2; ++ct) {
            const float xv[4] = {bflo(xs[ct][tt].x), bfhi(xs[ct][tt].x), bflo(xs[ct][tt].y), bfhi(xs[ct][tt].y)};
#pragma unroll
            for (int e = 0; e < 4; ++e) {
              const float v = __builtin_fmaf(xv[e] - mu[tt], rs[tt], acc[ct][tt][e]);
              x1[ct][e] = v; s1 += v; s2 = __builtin_fmaf(v, v, s2);
            }
          }
          x1p[tt] = pack8(x1[0], x1[1]);
          st128_sc1(x1p[tt], WS_X1 + (m * 10 + j) * 16384 + (2 * tq + tt) * 2048 + cq * 1024 + l * 16, rws);
          sm1[tt] = sum32(sum16(s1)); sq1[tt] = sum32(sum16(s2));
        }
        const u32x2 st = {asu(g & 1 ? sm1[1] : sm1[0]), asu(g & 1 ? sq1[1] : sq1[0])};
        st64_sc1(st, l < 32 ? WS_ST1 + m * 20480 + (2 * j + cq) * 1024 + (tq * 32 + l) * 8 : OOB, rws);
      }
      STAMP(7);
      waitbar<0>();
      STAMP(8);
      bar();
      STAMP(9);
      {
        const int sg = (g & 1) * 32;
        LAS char* const bA = pin(sm + W + (4 * g + q4) * 320 + 8 * p4 + 160 * cq + (cq ? -sg : sg));
        LAS char* const bB = pin(sm + W + (4 * g + q4) * 320 + 8 * p4 + 160 * cq + (cq ? sg : -sg));
        LAS char* const bA2 = pin(bA + 40960);
        LAS char* const bB2 = pin(bB + 40960);
        struct F3 { bf16x8 a[5], b0, b1; };
        F3 G0, G1;
        f32x4 a3[5][2];
        auto step3 = [&](auto mf, F3& R, u32 so, u32 wo, const F3& M) AINL {
          LAS char* const pA = wo >= 40960 ? bA2 + (wo - 40960) : bA + wo;
          LAS char* const pB = wo >= 40960 ? bB2 + (wo - 40960) : bB + wo;
          R.a[0] = ldstr(pA, pA + 5120); SB();
          if constexpr (mf.v) a3[0][0] = mfma(M.a[0], M.b0, a3[0][0]);
          SB(); R.b0 = lds128(ab + so); R.b1 = lds128(ab + so + 2048); SB();
          if constexpr (mf.v) a3[0][1] = mfma(M.a[0], M.b1, a3[0][1]);
          SB();
          sfor<1, 5>([&](auto ii) AINL {
            constexpr int i = ii.v;
            LAS char* const pw = ((i & 1) ? pB : pA) + 32 * i;
            R.a[i] = ldstr(pw, pw + 5120); SB();
            if constexpr (mf.v) a3[(i + 1) >> 1][(i + 1) & 1] = mfma(M.a[(i + 1) >> 1], ((i + 1) & 1) ? M.b1 : M.b0, a3[(i + 1) >> 1][(i + 1) & 1]);
            SB();
          });
          if constexpr (mf.v) {
            a3[3][0] = mfma(M.a[3], M.b0, a3[3][0]); a3[3][1] = mfma(M.a[3], M.b1, a3[3][1]);
            a3[4][0] = mfma(M.a[4], M.b0, a3[4][0]); a3[4][1] = mfma(M.a[4], M.b1, a3[4][1]);
          }
          SB();
        };
#pragma unroll
        for (int i = 0; i < 5; ++i) { a3[i][0] = zero4; a3[i][1] = zero4; }
        sfor<0, 10>([&](auto ic) AINL {
          constexpr int s = ic.v;
          constexpr u32 so = (s & 3) * 16384, wo = (s & 3) * 20480;
          bar();
          if constexpr (s == 0) STAMP(18);
          step3(IC<(s > 0)>{}, G0, so, wo, G1);
          step3(IC<1>{}, G1, so + 1024, wo + 10240, G0);
        });
#pragma unroll
        for (int i = 0; i < 5; ++i) { a3[i][0] = mfma(G1.a[i], G1.b0, a3[i][0]); a3[i][1] = mfma(G1.a[i], G1.b1, a3[i][1]); }
        bar();
        STAMP(10);
        float m1[2], r1[2];
#pragma unroll
        for (int tt = 0; tt < 2; ++tt) {
          float s1 = 0.f, s2 = 0.f;
#pragma unroll
          for (int i = 0; i < 5; ++i) {
            const f32x2 v = *(LAS f32x2*)(sm + ACT + 2 * 16384 + (g + 4 * i) * 1024 + (tq * 32 + tt * 16 + li) * 8);
            s1 += v.x; s2 += v.y;
          }
          s1 = sum32(sum16(s1)) * (1.0f / 640.0f); s2 = sum32(sum16(s2)) * (1.0f / 640.0f);
          m1[tt] = s1; r1[tt] = __builtin_amdgcn_rsqf(fmaxf(__builtin_fmaf(-s1, s1, s2), 0.f) + LN_EPS);
        }
        f32x4 c3[5];
#pragma unroll
        for (int i = 0; i < 5; ++i) c3[i] = *(LAS f32x4*)(sm + ACT + 3 * 16384 + 4096 + (80 * cq + 16 * i + 4 * g) * 4);
#pragma unroll
        for (int tt = 0; tt < 2; ++tt) {
          u32x2 pk[5];
#pragma unroll
          for (int i = 0; i < 5; ++i) {
            f32x4 h;
#pragma unroll
            for (int e = 0; e < 4; ++e) h[e] = gelu_f(r1[tt] * __builtin_fmaf(-m1[tt], c3[i][e], a3[i][tt][e]));
            pk[i] = pack4(h);
          }
          const u32 base = WS_H2 + m * 24 * 16384 + (2 * tq + tt) * 2048 + l * 16;
          auto boff = [&](int b) AINL { return b < 48 ? base + (b >> 1) * 16384 + (b & 1) * 1024 : OOB; };
          if (cq == 0) {
            st128_sc1((u32x4){pk[0].x, pk[0].y, pk[1].x, pk[1].y}, boff(5 * j), rws);
            st128_sc1((u32x4){pk[2].x, pk[2].y, pk[3].x, pk[3].y}, boff(5 * j + 1), rws);
            st64_sc1(pk[4], boff(5 * j + 2), rws);
          } else {
            st64_sc1(pk[0], boff(5 * j + 2) + 8, rws);
            st128_sc1((u32x4){pk[1].x, pk[1].y, pk[2].x, pk[2].y}, boff(5 * j + 3), rws);
            st128_sc1((u32x4){pk[3].x, pk[3].y, pk[4].x, pk[4].y}, boff(5 * j + 4), rws);
          }
          SB();
        }
      }
      STAMP(11);
      waitbar<0>();
      STAMP(12);
      const rsrc_t rdk = mk_rsrc(dskip, 1280);
      const u32x2 dk0 = bld64((cq * 32 + 4 * g) * 2, rdk, j * 128), dk1 = bld64((cq * 32 + 16 + 4 * g) * 2, rdk, j * 128);
      bar();
      STAMP(13);
#pragma unroll
      for (int a = 0; a < 2; ++a) { acc[a][0] = zero4; acc[a][1] = zero4; }
      sfor<0, 24>([&](auto ic) AINL {
        constexpr int s = ic.v;
        bar();
        if constexpr (s == 0) STAMP(19);
        step64(IC<(s > 0)>{}, IC<0>{}, F0, ab + (s & 3) * 16384, ((s + 5) % 12) * 8192, F1);
        step64(IC<1>{}, IC<0>{}, F1, ab + (s & 3) * 16384 + 1024, ((s + 5) % 12) * 8192 + 4096, F0);
      });
      mm(F1);
      bar();
      STAMP(14);
      const rsrc_t rout = mk_rsrc(out, 2048 * 2560);
#pragma unroll
      for (int tt = 0; tt < 2; ++tt) {
        const u32x4 sv = *(LAS u32x4*)(sm + ACT + w * 2048 + tt * 1024 + l * 16);
#pragma unroll
        for (int ct = 0; ct < 2; ++ct) {
          const float xv[4] = {bflo(xs[ct][tt].x), bfhi(xs[ct][tt].x), bflo(xs[ct][tt].y), bfhi(xs[ct][tt].y)};
          const u32 s0 = ct ? sv.z : sv.x, s1 = ct ? sv.w : sv.y;
          const float sy[4] = {bflo(s0), bfhi(s0), bflo(s1), bfhi(s1)};
          const u32 y0 = ct ? x1p[tt].z : x1p[tt].x, y1 = ct ? x1p[tt].w : x1p[tt].y;
          const float x1[4] = {bflo(y0), bfhi(y0), bflo(y1), bfhi(y1)};
          const u32x2 dk = ct ? dk1 : dk0;
          const float dv[4] = {bflo(dk.x), bfhi(dk.x), bflo(dk.y), bfhi(dk.y)};
          u32x4 o;
#pragma unroll
          for (int e = 0; e < 4; ++e) {
            const float sfull = __builtin_fmaf((xv[e] - mu[tt]) * rs[tt], dv[e], sy[e]);
            o[e] = asu(__builtin_fmaf(sfull, silu_f(x1[e] + acc[ct][tt][e]), xv[e]));
          }
          __builtin_amdgcn_raw_buffer_store_b128(o, rout, (m * 128 + tq * 32 + tt * 16 + li) * 2560 + (j * 64 + cq * 32 + ct * 16 + 4 * g) * 4, 0, 0);
        }
      }
      STAMP(15); CSTAMP(31);
    }
  } else if (role < 15) {
    const int a = role - 10;
    constexpr u32 MISC = 147456, CST = 157696, TAG = 159744;
    if (w >= 8) {
      const rsrc_t rbr = mk_rsrc(b_re, 640 * 1280), rbi = mk_rsrc(b_im, 640 * 1280);
      const rsrc_t rcr = mk_rsrc(c_re, 640 * 1280), rci = mk_rsrc(c_im, 640 * 1280);
      const u32 vx0 = (m * 128 + 32 * (r8 >> 2) + 8 * L + (r8 & 3)) * 1280 + (((l & 7) ^ g) << 4);
      const u32 vx1 = (m * 128 + 32 * (2 + (r8 >> 2)) + 8 * L + (r8 & 3)) * 1280 + (((l & 7) ^ (4 + g)) << 4) - 1024;
      const u32 vb0 = (a * 128 + L * 32 + r8) * 1280 + (((l & 7) ^ g) << 4);
      const u32 vb1 = (a * 128 + L * 32 + 8 + r8) * 1280 + (((l & 7) ^ (4 + g)) << 4) - 1024;
      const u32 ldsw = L0 + L * 4096;
      auto issue_nk = [&](u32 d, rsrc_t r, u32 so) AINL { dma2<0>(d, vb0, vb1, r, so); dma2<0>(d + 2048, vb0, vb1, r, so + 20480); };
      auto issue1 = [&](int s) AINL {
        const int kt = wrapk(s + rotA1, 10);
        const u32 d = ldsw + (s % 3) * 49152;
        dma2<0>(d, vx0, vx1, rx, kt * 128); dma2<0>(d + 2048, vx0, vx1, rx, kt * 128 + 5120);
        issue_nk(d + 16384, rbr, kt * 128); issue_nk(d + 32768, rbi, kt * 128);
      };
      auto issue_c = [&](int s) AINL {
        const int kt = wrapk(s + rotA3, 20);
        issue_nk(ldsw + 49152 + (s % 6) * 16384, kt < 10 ? rcr : rci, wrapk(kt, 10) * 128);
      };
      const u32 va = L * 4096 + l * 16;
      auto issue_h = [&](int s) AINL {
        dma4<1>(ldsw + ((s & 3) == 3 ? MISC : (s & 3) * 16384), va, rws, WS_HH + (m * 20 + wrapk(s + rotA3, 20)) * 16384);
      };
      issue1(0); issue1(1);
      sfor<0, 10>([&](auto ic) AINL {
        constexpr int s = ic.v;
        waitbar<(s == 9 ? 13 : 12)>();
        if constexpr (s + 2 < 10) issue1(s + 2);
        else if constexpr (s == 8) { dma1<1>(L0 + MISC + L * 1024, L * 1024 + l * 16, rws, WS_XS + m * 2048); issue_c(0); issue_c(1); issue_c(2); }
        else { issue_c(3); issue_c(4); issue_c(5); }
        if constexpr (s == 0) get_tag(TAG);
      });
      waitbar<24>();
      bar(); bar();
      await(FL_A + m * 5 + a, FL_A + m * 5 + l, l < 5, [&]() AINL { issue_h(0); issue_h(1); issue_h(2); });
      sfor<0, 20>([&](auto ic) AINL {
        constexpr int s = ic.v;
        constexpr int gs[23] = {4, 4, 4, 4, 8, 8, 8, 8, 8, 8, 8, 8, 8, 8, 8, 8, 8, 8, 4, 4, 0, 0, 0};
        waitbar<gs[s + 1] + gs[s + 2]>();
        if constexpr (s + 3 < 20) issue_h(s + 3);
        if constexpr (s >= 1 && s + 5 < 20) issue_c(s + 5);
      });
    } else {
      const int p = a * 128 + w * 16 + li;
      const u32 plr = bld16(p * 2, mk_rsrc(lam_re, 1280)), pli = bld16(p * 2, mk_rsrc(lam_im, 1280)), pdt = bld16(p * 2, mk_rsrc(log_dt, 1280));
      row_stats();
      put_tag(TAG);
      float lbr, lbi, kr, ki;
      {
        const float Lr = bflo(plr), Li = bflo(pli);
        const float dt = bflo(pk2(expf(bflo(pdt)), 0.f));
        const float ea = expf(dt * Lr);
        float sn, cn; sincosf(dt * Li, &sn, &cn);
        lbr = ea * cn; lbi = ea * sn;
        const float inv = 2.0f / (Lr * Lr + Li * Li);
        kr = ((lbr - 1.0f) * Lr + lbi * Li) * inv;
        ki = (lbi * Lr - (lbr - 1.0f) * Li) * inv;
        asm volatile("" : "+v"(lbr), "+v"(lbi), "+v"(kr), "+v"(ki));
      }
      f32x4 are[8], aim[8], sre = zero4, sim = zero4;
#pragma unroll
      for (int i = 0; i < 8; ++i) { are[i] = zero4; aim[i] = zero4; }
      {
        struct FW { bf16x8 br, bi; };
        struct FX { bf16x8 x[4]; };
        FW W0, W1; FX Xa, Xb;
        auto half = [&](auto mf, auto rw, auto hh, FW& RW, LAS char* pw, FX& RX, LAS char* px, const FW& MW, const FX& MX) AINL {
          constexpr int h = hh.v;
          if constexpr (rw.v) { RW.br = lds128(pw); RX.x[0] = lds128(px); } else { RX.x[0] = lds128(px); RX.x[1] = lds128(px + 2048); }
          SB();
          if constexpr (mf.v) are[4 * h] = mfma(MX.x[0], MW.br, are[4 * h]);
          SB();
          if constexpr (rw.v) { RW.bi = lds128(pw + 16384); RX.x[1] = lds128(px + 2048); } else { RX.x[2] = lds128(px + 4096); RX.x[3] = lds128(px + 6144); }
          SB();
          if constexpr (mf.v) aim[4 * h] = mfma(MX.x[0], MW.bi, aim[4 * h]);
          SB();
          if constexpr (rw.v) { RX.x[2] = lds128(px + 4096); RX.x[3] = lds128(px + 6144); }
          SB();
          if constexpr (mf.v) {
#pragma unroll
            for (int i = 1; i < 4; ++i) { are[4 * h + i] = mfma(MX.x[i], MW.br, are[4 * h + i]); aim[4 * h + i] = mfma(MX.x[i], MW.bi, aim[4 * h + i]); }
            if constexpr (h == 0) sre = mfma(ones, MW.br, keep(sre)); else sim = mfma(ones, MW.bi, keep(sim));
          }
          SB();
        };
        STAMP(1);
        sfor<0, 10>([&](auto ic) AINL {
          constexpr int s = ic.v;
          LAS char* const x0 = pin(sm + rb0 + (s % 3) * 49152);
          LAS char* const x1 = pin(sm + (rb0 ^ 64) + (s % 3) * 49152);
          LAS char* const w0 = pin(x0 + w * 2048 + 16384);
          LAS char* const w1_ = pin(x1 + w * 2048 + 16384);
          bar();
          if constexpr (s == 0) STAMP(16);
          half(IC<(s > 0)>{}, IC<1>{}, IC<1>{}, W0, w0, Xa, x0, W1, Xb);
          half(IC<1>{}, IC<0>{}, IC<0>{}, W0, w0, Xb, x0 + 8192, W0, Xa);
          half(IC<1>{}, IC<1>{}, IC<1>{}, W1, w1_, Xa, x1, W0, Xb);
          half(IC<1>{}, IC<0>{}, IC<0>{}, W1, w1_, Xb, x1 + 8192, W1, Xa);
        });
#pragma unroll
        for (int i = 0; i < 4; ++i) { are[4 + i] = mfma(Xb.x[i], W1.br, are[4 + i]); aim[4 + i] = mfma(Xb.x[i], W1.bi, aim[4 + i]); }
        sim = mfma(ones, W1.bi, sim);
      }
      bar();
      STAMP(2);
      if (w < 2) {
        u32x4 g0 = *(LAS u32x4*)(sm + MISC + tid * 16);
        if (!__all(g0.y == ta && g0.w == tb)) g0 = poll_granule(WS_XS + (m * 128 + tid) * 16, true, rws, ta, tb);
        const float rs = asf(g0.z);
        *(LAS f32x2*)(sm + CST + (tid >> 5) * 272 + (tid & 31) * 8) = (f32x2){rs, -rs * asf(g0.x)};
      }
      bar();
#pragma unroll
      for (int mt = 0; mt < 8; ++mt) {
        const f32x4 s0 = *(LAS f32x4*)(sm + CST + g * 272 + mt * 32), s1 = *(LAS f32x4*)(sm + CST + g * 272 + mt * 32 + 16);
        const float rv[4] = {s0.x, s0.z, s1.x, s1.z}, nv[4] = {s0.y, s0.w, s1.y, s1.w};
#pragma unroll
        for (int e = 0; e < 4; ++e) {
          are[mt][e] = __builtin_fmaf(rv[e], are[mt][e], nv[e] * sre.x);
          aim[mt][e] = __builtin_fmaf(rv[e], aim[mt][e], nv[e] * sim.x);
        }
      }
      bar();
      float hr = 0.f, hi = 0.f;
#pragma unroll
      for (int mt = 0; mt < 8; ++mt)
#pragma unroll
        for (int e = 0; e < 4; ++e) {
          const float nr = __builtin_fmaf(-lbi, hi, __builtin_fmaf(lbr, hr, are[mt][e]));
          const float ni = __builtin_fmaf(lbi, hr, __builtin_fmaf(lbr, hi, aim[mt][e]));
          hr = nr; hi = ni;
        }
      float er[4], ei[4];
      gather4(hr, er); gather4(hi, ei);
      float p32r = lbr, p32i = lbi;
#pragma unroll
      for (int i = 0; i < 5; ++i) { const float t = p32r * p32r - p32i * p32i; p32i = 2.0f * p32r * p32i; p32r = t; }
      float cr[5], ci[5];
      cr[0] = 0.f; ci[0] = 0.f;
#pragma unroll
      for (int q = 0; q < 4; ++q) {
        cr[q + 1] = __builtin_fmaf(-p32i, ci[q], __builtin_fmaf(p32r, cr[q], er[q]));
        ci[q + 1] = __builtin_fmaf(p32i, cr[q], __builtin_fmaf(p32r, ci[q], ei[q]));
      }
      st128_sc1((u32x4){asu(cr[4]), ta, asu(ci[4]), tb}, l < 16 ? WS_EG + (m * 640 + p) * 16 : OOB, rws);
      STAMP(3);
      const float p64r = p32r * p32r - p32i * p32i, p64i = 2.0f * p32r * p32i;
      const float Pr = p64r * p64r - p64i * p64i, Pi = 2.0f * p64r * p64i;
      float Hr = 0.f, Hi = 0.f;
      if (m > 0) {
        u32x4 e0, e1, e2, e3;
        const int m0 = m - 1 - 4 * g;
        const u32 o0 = WS_EG + (m0 * 640 + p) * 16;
        const bool v0 = m0 >= 0, v1 = m0 >= 1, v2 = m0 >= 2, v3 = m0 >= 3;
        _Pragma("nounroll") for (int it = 0; it < SPIN_CAP; ++it) {
          asm volatile("buffer_load_dwordx4 %0, %4, %8, 0 offen sc1\n\tbuffer_load_dwordx4 %1, %5, %8, 0 offen sc1\n\t"
                       "buffer_load_dwordx4 %2, %6, %8, 0 offen sc1\n\tbuffer_load_dwordx4 %3, %7, %8, 0 offen sc1\n\ts_waitcnt vmcnt(0)"
                       : "=&v"(e0), "=&v"(e1), "=&v"(e2), "=&v"(e3)
                       : "v"(v0 ? o0 : OOB), "v"(v1 ? o0 - 10240 : OOB), "v"(v2 ? o0 - 20480 : OOB), "v"(v3 ? o0 - 30720 : OOB), "s"(rws) : "memory");
          const bool ok = (!v0 || (e0.y == ta && e0.w == tb)) && (!v1 || (e1.y == ta && e1.w == tb)) &&
                          (!v2 || (e2.y == ta && e2.w == tb)) && (!v3 || (e3.y == ta && e3.w == tb));
          if (__all(ok)) break;
          __builtin_amdgcn_s_sleep(1);
        }
        float qr = asf(e3.x), qi = asf(e3.z), t;
        t = __builtin_fmaf(-Pi, qi, __builtin_fmaf(Pr, qr, asf(e2.x))); qi = __builtin_fmaf(Pi, qr, __builtin_fmaf(Pr, qi, asf(e2.z))); qr = t;
        t = __builtin_fmaf(-Pi, qi, __builtin_fmaf(Pr, qr, asf(e1.x))); qi = __builtin_fmaf(Pi, qr, __builtin_fmaf(Pr, qi, asf(e1.z))); qr = t;
        t = __builtin_fmaf(-Pi, qi, __builtin_fmaf(Pr, qr, asf(e0.x))); qi = __builtin_fmaf(Pi, qr, __builtin_fmaf(Pr, qi, asf(e0.z))); qr = t;
        float gr[4], gi[4];
        gather4(qr, gr); gather4(qi, gi);
        const float P2r = Pr * Pr - Pi * Pi, P2i = 2.0f * Pr * Pi;
        const float P4r = P2r * P2r - P2i * P2i, P4i = 2.0f * P2r * P2i;
        Hr = gr[3]; Hi = gi[3];
#pragma unroll
        for (int q = 2; q >= 0; --q) {
          t = __builtin_fmaf(-P4i, Hi, __builtin_fmaf(P4r, Hr, gr[q])); Hi = __builtin_fmaf(P4i, Hr, __builtin_fmaf(P4r, Hi, gi[q])); Hr = t;
        }
      }
      STAMP(4);
      {
        const float p96r = p64r * p32r - p64i * p32i, p96i = p64r * p32i + p64i * p32r;
        const float fr = g == 0 ? 1.f : g == 1 ? p32r : g == 2 ? p64r : p96r;
        const float fi = g == 0 ? 0.f : g == 1 ? p32i : g == 2 ? p64i : p96i;
        const float c0r = g == 0 ? cr[0] : g == 1 ? cr[1] : g == 2 ? cr[2] : cr[3];
        const float c0i = g == 0 ? ci[0] : g == 1 ? ci[1] : g == 2 ? ci[2] : ci[3];
        hr = __builtin_fmaf(-fi, Hi, __builtin_fmaf(fr, Hr, c0r));
        hi = __builtin_fmaf(fi, Hr, __builtin_fmaf(fr, Hi, c0i));
      }
#pragma unroll
      for (int mt = 0; mt < 8; ++mt)
#pragma unroll
        for (int e = 0; e < 4; ++e) {
          const float nr = __builtin_fmaf(-lbi, hi, __builtin_fmaf(lbr, hr, are[mt][e]));
          const float ni = __builtin_fmaf(lbi, hr, __builtin_fmaf(lbr, hi, aim[mt][e]));
          hr = nr; hi = ni;
          are[mt][e] = __builtin_fmaf(kr, hr, -ki * hi);
          aim[mt][e] = __builtin_fmaf(-kr, hi, -ki * hr);
        }
      {
        LAS char* const tb_ = sm + (w < 6 ? w * 8192 : MISC + (w - 6) * 8192);
        const int swc = ((li & 3) << 1) | ((li >> 2) & 1) | ((li >> 3) << 3);
#pragma unroll
        for (int i = 0; i < 4; ++i) {
          *(LAS u32x4*)(tb_ + li * 256 + (((4 * g + i) ^ swc) << 4)) = pack8(are[2 * i], are[2 * i + 1]);
          *(LAS u32x4*)(tb_ + 4096 + li * 256 + (((4 * g + i) ^ swc) << 4)) = pack8(aim[2 * i], aim[2 * i + 1]);
        }
        asm volatile("s_waitcnt lgkmcnt(0)" ::: "memory");
        const int q4 = li >> 2, p4 = l & 3;
        const int c0 = 8 * (g & 1) + q4, c1 = c0 + 4;
        const int sw0 = ((c0 & 3) << 1) | ((c0 >> 3) << 3), sw1 = sw0 | 1;
#pragma unroll
        for (int part = 0; part < 2; ++part)
#pragma unroll
          for (int u = 0; u < 4; ++u) {
            const int tt = 2 * u + (g >> 1), ch = 2 * tt + (p4 >> 1);
            const bf16x8 v = ldstr(tb_ + part * 4096 + c0 * 256 + ((ch ^ sw0) << 4) + 8 * (p4 & 1),
                                   tb_ + part * 4096 + c1 * 256 + ((ch ^ sw1) << 4) + 8 * (p4 & 1));
            const u32 off = WS_HH + (m * 20 + 10 * part + 2 * a + (w >> 2)) * 16384 + tt * 2048 + ((w >> 1) & 1) * 1024 + ((2 * (w & 1) + (g & 1)) * 16 + li) * 16;
            st128_sc1(__builtin_bit_cast(u32x4, v), off, rws);
          }
      }
      waitbar<0>();
      STAMP(5);
      bar();
      STAMP(6);
      f32x4 ac[4][2];
#pragma unroll
      for (int i = 0; i < 4; ++i) { ac[i][0] = zero4; ac[i][1] = zero4; }
      LAS char* const ab = pin(sm + tq * 4096 + l * 16);
      LAS char* const ab3 = pin(ab + MISC);
      LAS char* cb[2][2];
#pragma unroll
      for (int i = 0; i < 2; ++i) { cb[0][i] = pin(sm + 49152 + i * 49152 + cq * 8192 + rb0); cb[1][i] = pin(sm + 49152 + i * 49152 + cq * 8192 + (rb0 ^ 64)); }
      struct FC { bf16x8 c[4], b0, b1; };
      FC G0, G1;
      auto stepC = [&](auto mf, FC& R, int ks, int s, const FC& M) AINL {
        LAS char* const pa = ((s & 3) == 3 ? ab3 : ab + (s & 3) * 16384) + ks * 1024;
        LAS char* const pc = cb[ks][(s % 6) / 3] + ((s % 6) % 3) * 16384;
        R.b0 = lds128(pa); R.c[0] = lds128(pc); SB();
        if constexpr (mf.v) ac[0][0] = mfma(M.c[0], M.b0, ac[0][0]);
        SB(); R.b1 = lds128(pa + 2048); R.c[1] = lds128(pc + 2048); SB();
        if constexpr (mf.v) ac[0][1] = mfma(M.c[0], M.b1, ac[0][1]);
        SB(); R.c[2] = lds128(pc + 4096); R.c[3] = lds128(pc + 6144); SB();
        if constexpr (mf.v) {
#pragma unroll
          for (int ct = 1; ct < 4; ++ct) { ac[ct][0] = mfma(M.c[ct], M.b0, ac[ct][0]); ac[ct][1] = mfma(M.c[ct], M.b1, ac[ct][1]); }
        }
        SB();
      };
      sfor<0, 20>([&](auto ic) AINL {
        constexpr int s = ic.v;
        bar();
        if constexpr (s == 0) STAMP(17);
        stepC(IC<(s > 0)>{}, G0, 0, s, G1);
        stepC(IC<1>{}, G1, 1, s, G0);
      });
#pragma unroll
      for (int ct = 0; ct < 4; ++ct) { ac[ct][0] = mfma(G1.c[ct], G1.b0, ac[ct][0]); ac[ct][1] = mfma(G1.c[ct], G1.b1, ac[ct][1]); }
      STAMP(7);
#pragma unroll
      for (int cb_ = 0; cb_ < 2; ++cb_)
#pragma unroll
        for (int tt = 0; tt < 2; ++tt)
          st128_sc1(pack8(ac[2 * cb_][tt], ac[2 * cb_ + 1][tt]), WS_SS + ((m * 10 + 2 * a + cq) * 8 + tq + 4 * cb_) * 2048 + tt * 1024 + l * 16, rws);
      waitbar<0>();
      if (w == 0) set_flag(FL_S + m * 5 + a);
      STAMP(8);
    }
  } else if (w < 8) {
    row_stats();
    u32 dummy = 0;
    const u32 lo = tid < 400 ? (m * 400 + tid) * 128 : OOB;
    touch(dummy, lo, mk_rsrc(w2, 640 * 1280));
    touch(dummy, lo, mk_rsrc(c_re, 640 * 1280));
    touch(dummy, lo, mk_rsrc(c_im, 640 * 1280));
    const rsrc_t rw4 = mk_rsrc(w4, 1536 * 1280);
    if (m < 12) {
      const rsrc_t rw3 = mk_rsrc(w3, 640 * 3072);
      float cs[8];
#pragma unroll
      for (int e = 0; e < 8; ++e) cs[e] = 0.f;
      const u32 vo = (tid >> 4) * 3072 + m * 256 + (tid & 15) * 16;
      u32x4 t[20];
#pragma unroll
      for (int i = 0; i < 20; ++i) t[i] = bld128(vo, rw3, i * 32 * 3072);
      SB();
#pragma unroll
      for (int i = 0; i < 20; ++i) {
        cs[0] += bflo(t[i].x); cs[1] += bfhi(t[i].x); cs[2] += bflo(t[i].y); cs[3] += bfhi(t[i].y);
        cs[4] += bflo(t[i].z); cs[5] += bfhi(t[i].z); cs[6] += bflo(t[i].w); cs[7] += bfhi(t[i].w);
      }
#pragma unroll
      for (int e = 0; e < 8; ++e) cs[e] = sum32(sum16(cs[e]));
      if (l < 16) {
        *(LAS f32x4*)(sm + w * 512 + l * 32) = (f32x4){cs[0], cs[1], cs[2], cs[3]};
        *(LAS f32x4*)(sm + w * 512 + l * 32 + 16) = (f32x4){cs[4], cs[5], cs[6], cs[7]};
      }
      bar();
      if (w < 2) {
        float s = 0.f;
#pragma unroll
        for (int i = 0; i < 8; ++i) s += *(LAS float*)(sm + i * 512 + tid * 4);
        st32_sc1(asu(s), WS_CS3 + (m * 128 + tid) * 4, rws);
      }
      waitbar<0>();
      if (w == 0) set_flag(FL_C + m);
    }
    STAMP(1);
    touch(dummy, (m * 960 + tid) * 128, rw4);
    touch(dummy, tid < 448 ? (m * 960 + 512 + tid) * 128 : OOB, rw4);
    asm volatile("s_waitcnt vmcnt(0)" : "+v"(dummy)::"memory");
    STAMP(2);
    if (m == 0) {
#pragma unroll
      for (int i = 0; i < 4; ++i) (void)poll_granule(WS_XS + (i * 512 + tid) * 16, true, rws, ta, tb);
      bar();
      if (tid == 0) st64_sc1((u32x2){ta, tb}, WS_EPOCH, rws);
    }
  }
#endif
}

extern "C" void kernel_launch(void* const* d_in, const int* in_sizes, int n_in, void* d_out, int out_size,
                              void* d_ws, size_t ws_size, hipStream_t stream) {
  (void)in_sizes; (void)n_in; (void)out_size; (void)ws_size;
  s5_mixer_kernel<<<256, 768, 0, stream>>>(d_in[0], d_in[1], d_in[2], d_in[3], d_in[4], d_in[5], d_in[6], d_in[7],
                                           d_in[8], d_in[10], d_in[11], d_in[12], d_in[13], d_out, d_ws);
}
```

```cpp
#include <hip/hip_runtime.h>
#include <hip/hip_bf16.h>
#include <stdint.h>

#define DI __device__ __forceinline__
#define LAS __attribute__((address_space(3)))
#define AINL __attribute__((always_inline))
typedef unsigned u32;
typedef unsigned long long u64;
typedef u32 u32x4 __attribute__((ext_vector_type(4)));
typedef u32 u32x2 __attribute__((ext_vector_type(2)));
typedef float f32x4 __attribute__((ext_vector_type(4)));
typedef float f32x2 __attribute__((ext_vector_type(2)));
typedef __bf16 bf16x8 __attribute__((ext_vector_type(8)));
typedef __bf16 bf16x2 __attribute__((ext_vector_type(2)));
typedef short s16x4 __attribute__((ext_vector_type(4)));
typedef short s16x8 __attribute__((ext_vector_type(8)));

#define XMAP 1
#define ABL 0
#define PSLEEP 2
#define STAMPS 0

constexpr u32 WS_EPOCH = 0;
constexpr u32 WS_FLAG = 0x1000;
constexpr u32 FL_1 = 0, FL_2 = 160, FL_3 = 320, FL_A = 480, FL_S = 560, FL_C = 640;
constexpr u32 WS_XS = 0x20000;
constexpr u32 WS_CS3 = 0x2A000;
constexpr u32 WS_EG = 0x30000;
constexpr u32 WS_ST1 = 0x60000;
constexpr u32 WS_SS = 0xC0000;
constexpr u32 WS_H1 = 0x400000;
constexpr u32 WS_X1 = 0x680000;
constexpr u32 WS_HH = 0x900000;
constexpr u32 WS_H2 = 0xE00000;
constexpr u32 WS_STAMP = 0x1400000;
constexpr u32 WS_BYTES = 0x1500000;
constexpr u32 OOB = 0x7FFFFFF0u;
constexpr float LN_EPS = 1e-5f;
constexpr int SPIN_CAP = 1 << 17;

template <int I> struct IC { static constexpr int v = I; };
template <int B, int E, class F> DI void sfor(F&& f) {
  if constexpr (B < E) { f(IC<B>{}); sfor<B + 1, E>(f); }
}

#if defined(__HIP_DEVICE_COMPILE__) && __HIP_DEVICE_COMPILE__
typedef __amdgpu_buffer_rsrc_t rsrc_t;
DI rsrc_t mk_rsrc(const void* p, u32 bytes) { return __builtin_amdgcn_make_buffer_rsrc((void*)p, 0, bytes, 0x00020000); }

template <int SC1> DI void dma1(u32 lds, u32 voff, rsrc_t r, u32 soff) {
  if (ABL == 2) return;
  if constexpr (SC1)
    asm volatile("s_mov_b32 m0, %0\n\ts_nop 0\n\tbuffer_load_dwordx4 %1, %2, %3 offen sc1 lds" ::"s"(lds), "v"(voff), "s"(r), "s"(soff) : "memory");
  else
    asm volatile("s_mov_b32 m0, %0\n\ts_nop 0\n\tbuffer_load_dwordx4 %1, %2, %3 offen lds" ::"s"(lds), "v"(voff), "s"(r), "s"(soff) : "memory");
}
template <int SC1> DI void dma2(u32 lds, u32 v0, u32 v1, rsrc_t r, u32 soff) {
  if (ABL == 2) return;
  if constexpr (SC1)
    asm volatile("s_mov_b32 m0, %0\n\ts_nop 0\n\tbuffer_load_dwordx4 %1, %3, %4 offen sc1 lds\n\tbuffer_load_dwordx4 %2, %3, %4 offen offset:1024 sc1 lds"
                 ::"s"(lds), "v"(v0), "v"(v1), "s"(r), "s"(soff) : "memory");
  else
    asm volatile("s_mov_b32 m0, %0\n\ts_nop 0\n\tbuffer_load_dwordx4 %1, %3, %4 offen lds\n\tbuffer_load_dwordx4 %2, %3, %4 offen offset:1024 lds"
                 ::"s"(lds), "v"(v0), "v"(v1), "s"(r), "s"(soff) : "memory");
}
template <int SC1> DI void dma4(u32 lds, u32 v, rsrc_t r, u32 soff) {
  if (ABL == 2) return;
  if constexpr (SC1)
    asm volatile("s_mov_b32 m0, %0\n\ts_nop 0\n\tbuffer_load_dwordx4 %1, %2, %3 offen sc1 lds\n\tbuffer_load_dwordx4 %1, %2, %3 offen offset:1024 sc1 lds\n\t"
                 "buffer_load_dwordx4 %1, %2, %3 offen offset:2048 sc1 lds\n\tbuffer_load_dwordx4 %1, %2, %3 offen offset:3072 sc1 lds"
                 ::"s"(lds), "v"(v), "s"(r), "s"(soff) : "memory");
  else
    asm volatile("s_mov_b32 m0, %0\n\ts_nop 0\n\tbuffer_load_dwordx4 %1, %2, %3 offen lds\n\tbuffer_load_dwordx4 %1, %2, %3 offen offset:1024 lds\n\t"
                 "buffer_load_dwordx4 %1, %2, %3 offen offset:2048 lds\n\tbuffer_load_dwordx4 %1, %2, %3 offen offset:3072 lds"
                 ::"s"(lds), "v"(v), "s"(r), "s"(soff) : "memory");
}
constexpr int VC(int n) { return ABL == 2 ? 0 : n; }
template <int N> DI void waitv() { asm volatile("s_waitcnt vmcnt(%0)" ::"i"(VC(N)) : "memory"); }
DI void bar() { asm volatile("s_waitcnt lgkmcnt(0)\n\ts_barrier" ::: "memory"); }
template <int N> DI void waitbar() { asm volatile("s_waitcnt vmcnt(%0) lgkmcnt(0)\n\ts_barrier" ::"i"(VC(N)) : "memory"); }

DI void touch(u32& d, u32 voff, rsrc_t r) { asm volatile("buffer_load_dword %0, %1, %2, 0 offen" : "+v"(d) : "v"(voff), "s"(r) : "memory"); }

DI u32x4 bld128(u32 voff, rsrc_t r, u32 soff) { return __builtin_amdgcn_raw_buffer_load_b128(r, voff, soff, 0); }
DI u32x2 bld64(u32 voff, rsrc_t r, u32 soff) { return __builtin_amdgcn_raw_buffer_load_b64(r, voff, soff, 0); }
DI u32x2 bld64_sc1(u32 voff, rsrc_t r, u32 soff) { return __builtin_amdgcn_raw_buffer_load_b64(r, voff, soff, 16); }
DI u32 bld32(u32 voff, rsrc_t r, u32 soff) { return __builtin_amdgcn_raw_buffer_load_b32(r, voff, soff, 0); }
DI u32 bld16(u32 voff, rsrc_t r) { return __builtin_amdgcn_raw_buffer_load_b16(r, voff, 0, 0); }
DI void st128_sc1(u32x4 v, u32 voff, rsrc_t r) { __builtin_amdgcn_raw_buffer_store_b128(v, r, voff, 0, 16); }
DI void st64_sc1(u32x2 v, u32 voff, rsrc_t r) { __builtin_amdgcn_raw_buffer_store_b64(v, r, voff, 0, 16); }
DI void st32_sc1(u32 v, u32 voff, rsrc_t r) { __builtin_amdgcn_raw_buffer_store_b32(v, r, voff, 0, 16); }

DI void poll_flags(u32 voff, bool active, rsrc_t ws, u32 ta, u32 tb) {
  _Pragma("nounroll") for (int it = 0; it < SPIN_CAP; ++it) {
    u32x2 v;
    asm volatile("buffer_load_dwordx2 %0, %1, %2, 0 offen sc1\n\ts_waitcnt vmcnt(0)" : "=&v"(v) : "v"(voff), "s"(ws) : "memory");
    const bool ok = !active || (v.x == ta && v.y == tb);
    if (__all(ok)) break;
    __builtin_amdgcn_s_sleep(1);
  }
}
DI u32x4 poll_granule(u32 voff, bool active, rsrc_t ws, u32 ta, u32 tb) {
  u32x4 v;
  _Pragma("nounroll") for (int it = 0; it < SPIN_CAP; ++it) {
    asm volatile("buffer_load_dwordx4 %0, %1, %2, 0 offen sc1\n\ts_waitcnt vmcnt(0)" : "=&v"(v) : "v"(voff), "s"(ws) : "memory");
    const bool ok = !active || (v.y == ta && v.w == tb);
    if (__all(ok)) break;
    __builtin_amdgcn_s_sleep(1);
  }
  return v;
}

DI float asf(u32 x) { return __uint_as_float(x); }
DI u32 asu(float x) { return __float_as_uint(x); }
DI float bflo(u32 x) { return asf(x << 16); }
DI float bfhi(u32 x) { return asf(x & 0xffff0000u); }
DI u32 pk2(float a, float b) { f32x2 p = {a, b}; bf16x2 q = __builtin_convertvector(p, bf16x2); return __builtin_bit_cast(u32, q); }
DI float sum16(float x) { const u32 u = asu(x); auto r = __builtin_amdgcn_permlane16_swap(u, u, false, false); const u32 a = r[0], b = r[1]; return asf(a) + asf(b); }
DI float sum32(float x) { const u32 u = asu(x); auto r = __builtin_amdgcn_permlane32_swap(u, u, false, false); const u32 a = r[0], b = r[1]; return asf(a) + asf(b); }
template <int C> DI float dppadd(float x) { return x + asf(__builtin_amdgcn_update_dpp(0u, asu(x), C, 0xf, 0xf, true)); }
DI float wavesum(float x) { x = dppadd<0xB1>(x); x = dppadd<0x4E>(x); x = dppadd<0x141>(x); x = dppadd<0x140>(x); return sum32(sum16(x)); }
DI void gather4(float x, float (&e)[4]) {
  const u32 u = asu(x);
  auto p = __builtin_amdgcn_permlane16_swap(u, u, false, false);
  const u32 a = p[0], b = p[1];
  auto pa = __builtin_amdgcn_permlane32_swap(a, a, false, false);
  auto pb = __builtin_amdgcn_permlane32_swap(b, b, false, false);
  const u32 e0 = pa[0], e2 = pa[1], e1 = pb[0], e3 = pb[1];
  e[0] = asf(e0); e[1] = asf(e1); e[2] = asf(e2); e[3] = asf(e3);
}
DI float gelu_f(float v) {
  const float c1 = -2.302208198f, c2 = -0.1029432397f;
  const float e = __builtin_amdgcn_exp2f(v * __builtin_fmaf(v * v, c2, c1));
  return v * __builtin_amdgcn_rcpf(1.0f + e);
}
DI float silu_f(float z) { return z * __builtin_amdgcn_rcpf(1.0f + __builtin_amdgcn_exp2f(z * -1.4426950409f)); }
DI f32x4 mfma(bf16x8 a, bf16x8 b, f32x4 c) { if (ABL == 1 || ABL == 4) { asm volatile("" : "+v"(c) : "v"(a), "v"(b)); return c; } return __builtin_amdgcn_mfma_f32_16x16x32_bf16(a, b, c, 0, 0, 0); }
DI bf16x8 lds128(LAS char* p) { if (ABL == 1 || ABL == 3) { bf16x8 r; asm volatile("" : "=v"(r)); return r; } return *(LAS bf16x8*)p; }
DI bf16x8 ldstr(LAS char* p0, LAS char* p1) {
  if (ABL == 1 || ABL == 3) { bf16x8 r; asm volatile("" : "=v"(r)); return r; }
  const s16x4 a = __builtin_amdgcn_ds_read_tr16_b64_v4i16((LAS s16x4*)p0);
  const s16x4 b = __builtin_amdgcn_ds_read_tr16_b64_v4i16((LAS s16x4*)p1);
  const s16x8 w = __builtin_shufflevector(a, b, 0, 1, 2, 3, 4, 5, 6, 7);
  return __builtin_bit_cast(bf16x8, w);
}
DI u32x4 pack8(f32x4 a, f32x4 b) { u32x4 r; r.x = pk2(a.x, a.y); r.y = pk2(a.z, a.w); r.z = pk2(b.x, b.y); r.w = pk2(b.z, b.w); return r; }
DI u32x2 pack4(f32x4 a) { u32x2 r; r.x = pk2(a.x, a.y); r.y = pk2(a.z, a.w); return r; }
DI int wrapk(int k, int n) { return k >= n ? k - n : k; }
#define SB() __builtin_amdgcn_sched_barrier(0)
DI f32x4 keep(f32x4 v) { asm volatile("" : "+v"(v)); return v; }
DI LAS char* pin(LAS char* p) { asm volatile("" : "+v"(p)); return p; }
#endif

#define STAMP(i) do { } while (0)
#define CSTAMP(i) do { } while (0)

#define ROT 0
__global__ __launch_bounds__(768, 3) void s5_mixer_kernel(
    const void* __restrict__ x, const void* __restrict__ lam_re, const void* __restrict__ lam_im,
    const void* __restrict__ b_re, const void* __restrict__ b_im, const void* __restrict__ c_re,
    const void* __restrict__ c_im, const void* __restrict__ log_dt, const void* __restrict__ dskip,
    const void* __restrict__ w1, const void* __restrict__ w2, const void* __restrict__ w3,
    const void* __restrict__ w4, void* __restrict__ out, void* __restrict__ ws) {
#if defined(__HIP_DEVICE_COMPILE__) && __HIP_DEVICE_COMPILE__
  __shared__ __attribute__((aligned(16))) char smem_[163840];
  LAS char* const sm = (LAS char*)smem_;
  const u32 L0 = (u32)(size_t)sm;
  const int tid = threadIdx.x, l = tid & 63;
  const int w = __builtin_amdgcn_readfirstlane(tid >> 6);
  const int bid = blockIdx.x, xcd = bid & 7, rr = bid >> 3;
  const int m = (xcd >> 1) * 4 + (rr < 20 ? rr / 5 : (rr - 20) / 3);
  const int role = rr < 20 ? rr % 5 + 5 * (xcd & 1) : 10 + (rr - 20) % 3 + 3 * (xcd & 1);
  const int g = l >> 4, li = l & 15, r8 = l >> 3, tq = w & 3, cq = w >> 2, L = w - 8;
  const rsrc_t rws = mk_rsrc(ws, WS_BYTES);
  const rsrc_t rx = mk_rsrc(x, 2048 * 1280);
  const u64 pt0 = __builtin_amdgcn_s_memrealtime();
  if (tid == 0) st64_sc1((u32x2){(u32)pt0, (u32)(pt0 >> 32)}, WS_STAMP + bid * 64, rws);
  const int fm = (5 * (m & 3) + 1) >> 1;
  const int rot1 = (2 * (role % 5) + fm) % 10, rotA1 = (2 * (role % 3) + 1 + fm) % 10;
  const int rot10 = 0, rot24 = 0, rotA3 = 0;

  u32 ta, tb;
  auto row_stats = [&]() AINL {
    const int stok = m * 128 + role * 8 + w;
    const u32x2 ep = bld64_sc1(0, rws, 0);
    const u32x4 ra = bld128(l * 16, rx, stok * 1280);
    const u32 rb_ = bld32(1024 + l * 4, rx, stok * 1280);
    const u64 e = (((u64)__builtin_amdgcn_readfirstlane(ep.y) << 32) | (u32)__builtin_amdgcn_readfirstlane(ep.x)) + 0x9E3779B97F4A7C15ull;
    ta = (u32)e; tb = (u32)(e >> 32);
    float v[10] = {bflo(ra.x), bfhi(ra.x), bflo(ra.y), bfhi(ra.y), bflo(ra.z), bfhi(ra.z), bflo(ra.w), bfhi(ra.w), bflo(rb_), bfhi(rb_)};
    float s = 0.f;
#pragma unroll
    for (int i = 0; i < 10; ++i) s += v[i];
    const float mu = wavesum(s) * (1.0f / 640.0f);
    float q = 0.f;
#pragma unroll
    for (int i = 0; i < 10; ++i) { const float d = v[i] - mu; q = __builtin_fmaf(d, d, q); }
    const float rs = 1.0f / sqrtf(wavesum(q) * (1.0f / 640.0f) + LN_EPS);
    u32x4 gr = {asu(mu), ta, asu(rs), tb};
    st128_sc1(gr, l == 0 ? WS_XS + stok * 16 : OOB, rws);
  };
  auto put_tag = [&](u32 off) AINL { if (tid == 0) *(LAS u32x2*)(sm + off) = (u32x2){ta, tb}; };
  auto get_tag = [&](u32 off) AINL {
    const u32x2 t = *(volatile LAS u32x2*)(sm + off);
    ta = __builtin_amdgcn_readfirstlane(t.x); tb = __builtin_amdgcn_readfirstlane(t.y);
  };
  auto set_flag = [&](u32 idx) AINL { st64_sc1((u32x2){ta, tb}, l == 0 ? WS_FLAG + idx * 128 : OOB, rws); };
  auto await = [&](u32 own, int i, bool act, auto&& first3) AINL {
    bar();
    if (L == 0) { set_flag(own); poll_flags(WS_FLAG + i * 128, act, rws, ta, tb); }
    bar();
    first3();
  };

  const u32 rb0 = li * 128 + ((g ^ (li >> 1)) << 4);
  const bf16x8 ones = __builtin_bit_cast(bf16x8, (u32x4){0x3F803F80u, 0x3F803F80u, 0x3F803F80u, 0x3F803F80u});
  const f32x4 zero4 = {0.f, 0.f, 0.f, 0.f};

  if (role < 10) {
    const int j = role;
    constexpr u32 ACT = 0, W = 65536;
    if (w >= 8) {
      const rsrc_t rw1 = mk_rsrc(w1, 640 * 1280), rw2 = mk_rsrc(w2, 640 * 1280);
      const rsrc_t rw3 = mk_rsrc(w3, 640 * 3072), rw4 = mk_rsrc(w4, 1536 * 1280);
      const u32 vx0 = (m * 128 + L * 32 + r8) * 1280 + (((l & 7) ^ g) << 4);
      const u32 vx1 = (m * 128 + L * 32 + 8 + r8) * 1280 + (((l & 7) ^ (4 + g)) << 4) - 1024;
      const u32 wch = j * 128 + (((l & 7) ^ (2 * g)) << 4);
      const u32 vq0 = ((L >> 1) * 32 + (r8 >> 2) * 8 + (L & 1) * 4 + (r8 & 3)) * 1280 + wch, vq1 = vq0 + 16 * 1280 - 1024;
      const u32 ldsA = L0 + ACT + L * 4096, ldsW = L0 + W + L * 2048;
      auto issue_1 = [&](int s) AINL {
        const int kt = wrapk(s + rot1, 10);
        dma2<0>(ldsA + (s & 3) * 16384, vx0, vx1, rx, kt * 128);
        dma2<0>(ldsW + 65536 + (s & 3) * 8192, vq0, vq1, rw1, kt * 81920);
        dma2<0>(ldsA + (s & 3) * 16384 + 2048, vx0, vx1, rx, kt * 128 + 20480);
      };
      issue_1(0); issue_1(1); issue_1(2);
      const u32 vw0 = (L * 16 + r8) * 1280 + wch, vw1 = vw0 + 10240 - 1024;
      const u32 va = L * 4096 + l * 16, vl = L * 1024 + l * 16;
      u32 v3[5];
#pragma unroll
      for (int i = 0; i < 5; ++i) {
        const int c = 64 * i + l, r = c / 20, pos = c - 20 * r;
        v3[i] = (L * 16 + r) * 3072 + j * 320 + ((pos ^ (2 * ((r >> 2) & 1))) << 4) - 1024 * (i & 1);
      }
      auto issue_w2 = [&](int s) AINL { dma2<0>(ldsW + s * 8192, vw0, vw1, rw2, wrapk(s + rot10, 10) * 81920); };
      auto issue_w3 = [&](int s) AINL {
        const u32 d = L0 + W + (s & 3) * 20480 + L * 5120, so = wrapk(s + rot10, 10) * 196608;
        dma2<0>(d, v3[0], v3[1], rw3, so); dma2<0>(d + 2048, v3[2], v3[3], rw3, so); dma1<0>(d + 4096, v3[4], rw3, so);
      };
      auto issue_w4 = [&](int s) AINL { dma2<0>(ldsW + ((s + 5) % 12) * 8192, vw0, vw1, rw4, wrapk(s + rot24, 24) * 81920); };
      auto issue_a = [&](int s, u32 base) AINL { dma4<1>(ldsA + (s & 3) * 16384, va, rws, base); };
      sfor<0, 10>([&](auto ic) AINL {
        constexpr int s = ic.v;
        constexpr int gs[13] = {6, 6, 6, 6, 6, 6, 6, 6, 6, 6, 5, 6, 6};
        waitbar<gs[s + 1] + gs[s + 2]>();
        if constexpr (s + 3 < 10) issue_1(s + 3);
        else if constexpr (s == 7) { dma1<1>(L0 + ACT + 2 * 16384 + L * 1024, vl, rws, WS_XS + m * 2048); issue_w2(0); issue_w2(1); }
        else { issue_w2(3 * s - 22); issue_w2(3 * s - 21); issue_w2(3 * s - 20); }
        if constexpr (s == 0) get_tag(W);
      });
      waitbar<16>();
      issue_w2(8); issue_w2(9);
      await(FL_1 + m * 10 + j, FL_1 + m * 10 + (l < 10 ? l : l < 16 ? -10 + (l - 10) + (l >= 13 ? 2 : 0) : 12 + (l - 16) + (l >= 19 ? 2 : 0)),
            l < 10 || (l < 16 && m > 0) || (l >= 16 && l < 22 && m < 15), [&]() AINL {
        sfor<0, 3>([&](auto ic) AINL { constexpr int s = ic.v; issue_a(s, WS_H1 + (m * 10 + wrapk(s + rot10, 10)) * 16384); });
      });
      sfor<0, 10>([&](auto ic) AINL {
        constexpr int s = ic.v;
        constexpr int gs[13] = {4, 4, 4, 4, 4, 4, 4, 4, 4, 4, 5, 5, 5};
        waitbar<gs[s + 1] + gs[s + 2]>();
        if constexpr (s + 3 < 10) issue_a(s + 3, WS_H1 + (m * 10 + wrapk(s + 3 + rot10, 10)) * 16384);
        else issue_w3(s - 7);
      });
      bar();
      issue_w3(3);
      await(FL_2 + m * 10 + j, l < 10 ? FL_2 + m * 10 + l : FL_C + ((5 * j) >> 2) + l - 10, l < (j < 9 ? 12 : 11), [&]() AINL {
        sfor<0, 3>([&](auto ic) AINL { constexpr int s = ic.v; issue_a(s, WS_X1 + (m * 10 + wrapk(s + rot10, 10)) * 16384); });
      });
      sfor<0, 10>([&](auto ic) AINL {
        constexpr int s = ic.v;
        constexpr int gs[13] = {4, 4, 4, 4, 9, 9, 9, 9, 9, 9, 8, 8, 8};
        waitbar<gs[s + 1] + gs[s + 2]>();
        if constexpr (s + 3 < 10) {
          issue_a(s + 3, WS_X1 + (m * 10 + wrapk(s + 3 + rot10, 10)) * 16384);
          if constexpr (s >= 1) issue_w3(s + 3);
        } else if constexpr (s == 7) {
          issue_a(2, WS_ST1 + m * 20480);
          issue_w4(0); issue_w4(1);
        } else if constexpr (s == 8) {
          dma1<1>(L0 + ACT + 3 * 16384 + L * 1024, vl, rws, WS_ST1 + m * 20480 + 16384);
          dma1<1>(L0 + ACT + 3 * 16384 + 4096 + L * 1024, vl, rws, WS_CS3 + j * 640);
          issue_w4(2); issue_w4(3); issue_w4(4);
        } else { issue_w4(5); issue_w4(6); issue_w4(7); issue_w4(8); }
      });
      waitbar<14>();
      issue_w4(9); issue_w4(10); issue_w4(11);
      await(FL_3 + m * 10 + j, FL_3 + m * 10 + l, l < 10, [&]() AINL {
        sfor<0, 3>([&](auto ic) AINL { constexpr int s = ic.v; issue_a(s, WS_H2 + (m * 24 + wrapk(s + rot24, 24)) * 16384); });
      });
      u32x2 fs;
      const u32 fso = WS_FLAG + (FL_S + m * 5 + (j >> 1)) * 128;
      sfor<0, 24>([&](auto ic) AINL {
        constexpr int s = ic.v;
        constexpr int gs[27] = {4, 4, 4, 4, 6, 6, 6, 6, 6, 6, 6, 6, 6, 6, 6, 6, 4, 4, 4, 4, 5, 4, 4, 4, 4, 0, 0};
        if constexpr (s == 20) { asm volatile("s_waitcnt vmcnt(%1) lgkmcnt(0)\n\ts_barrier" : "+v"(fs) : "i"(VC(gs[s + 1] + gs[s + 2])) : "memory"); }
        else waitbar<gs[s + 1] + gs[s + 2]>();
        if constexpr (s == 17) asm volatile("buffer_load_dwordx2 %0, %1, %2, 0 offen sc1" : "=v"(fs) : "v"(fso), "s"(rws) : "memory");
        if constexpr (s + 3 < 24) issue_a(s + 3, WS_H2 + (m * 24 + wrapk(s + 3 + rot24, 24)) * 16384);
        if constexpr (s >= 1 && s <= 12) issue_w4(s + 11);
        if constexpr (s == 21) {
          if (!__all(fs.x == ta && fs.y == tb)) poll_flags(fso, true, rws, ta, tb);
          issue_a(0, WS_SS + (m * 10 + j) * 16384);
        }
      });
      waitbar<0>();
    } else {
      row_stats();
      put_tag(W);
      const int q4 = li >> 2, p4 = l & 3;
      LAS char* const wb0 = pin(sm + W + (4 * g + q4) * 128 + ((((2 * cq) ^ ((2 * g + (q4 >> 1)) & 3))) << 5) + 8 * p4);
      LAS char* const wb1 = pin(sm + W + (4 * g + q4) * 128 + ((((2 * cq + 1) ^ ((2 * g + (q4 >> 1)) & 3))) << 5) + 8 * p4);
      LAS char* const wh0 = pin(wb0 + 65536);
      LAS char* const wh1 = pin(wb1 + 65536);
      LAS char* const ab = pin(sm + ACT + tq * 4096 + l * 16);
      struct F64 { bf16x8 a0, a1, b0, b1; };
      F64 F0, F1;
      f32x4 acc[2][2], cs[2];
      u32x2 xs[2][2];
      auto step64 = [&](auto mf, auto cs_on, F64& R, LAS char* pb, u32 wo, const F64& M) AINL {
        LAS char* const p0 = wo >= 65536 ? wh0 + (wo - 65536) : wb0 + wo;
        LAS char* const p1 = wo >= 65536 ? wh1 + (wo - 65536) : wb1 + wo;
        R.a0 = ldstr(p0, p0 + 2048); SB();
        if constexpr (mf.v) acc[0][0] = mfma(M.a0, M.b0, acc[0][0]);
        SB(); R.b0 = lds128(pb); R.b1 = lds128(pb + 2048); SB();
        if constexpr (mf.v) acc[1][0] = mfma(M.a1, M.b0, acc[1][0]);
        SB(); R.a1 = ldstr(p1, p1 + 2048); SB();
        if constexpr (mf.v) { acc[0][1] = mfma(M.a0, M.b1, acc[0][1]); acc[1][1] = mfma(M.a1, M.b1, acc[1][1]); }
        if constexpr (mf.v && cs_on.v) { cs[0] = mfma(M.a0, ones, keep(cs[0])); cs[1] = mfma(M.a1, ones, keep(cs[1])); }
        SB();
      };
      auto mm = [&](const F64& M) AINL {
        acc[0][0] = mfma(M.a0, M.b0, acc[0][0]); acc[1][0] = mfma(M.a1, M.b0, acc[1][0]);
        acc[0][1] = mfma(M.a0, M.b1, acc[0][1]); acc[1][1] = mfma(M.a1, M.b1, acc[1][1]);
      };
      float mu[2], rs[2];
      {
        LAS char* const xb0 = pin(sm + ACT + tq * 4096 + rb0);
        LAS char* const xb1 = pin(sm + ACT + tq * 4096 + (rb0 ^ 64));
        const int s_own = wrapk(j - rot1 + 10, 10);
#pragma unroll
        for (int a = 0; a < 2; ++a) { cs[a] = zero4; acc[a][0] = zero4; acc[a][1] = zero4; }
        STAMP(1);
        sfor<0, 10>([&](auto ic) AINL {
          constexpr int s = ic.v;
          constexpr u32 so = (s & 3) * 16384, wo = 65536 + (s & 3) * 8192;
          bar();
          if constexpr (s == 0) STAMP(16);
          step64(IC<(s > 0)>{}, IC<0>{}, F0, xb0 + so, wo, F1);
          if (s > 0 && tq == ((2 * s - 1) & 3)) { cs[0] = mfma(F1.a0, ones, keep(cs[0])); cs[1] = mfma(F1.a1, ones, keep(cs[1])); }
          step64(IC<1>{}, IC<0>{}, F1, xb1 + so, wo + 4096, F0);
          if (tq == ((2 * s) & 3)) { cs[0] = mfma(F0.a0, ones, keep(cs[0])); cs[1] = mfma(F0.a1, ones, keep(cs[1])); }
          if (s == s_own) {
#pragma unroll
            for (int tt = 0; tt < 2; ++tt)
#pragma unroll
              for (int ct = 0; ct < 2; ++ct)
                xs[ct][tt] = *(LAS u32x2*)(sm + ACT + so + tq * 4096 + tt * 2048 + li * 128 + (((4 * cq + 2 * ct + (g >> 1)) ^ (li >> 1)) << 4) + 8 * (g & 1));
          }
        });
        mm(F1);
        if (tq == 3) { cs[0] = mfma(F1.a0, ones, cs[0]); cs[1] = mfma(F1.a1, ones, cs[1]); }
        if (li == 0) { *(LAS f32x4*)(sm + ACT + 2 * 16384 + 8192 + w * 128 + g * 16) = cs[0]; *(LAS f32x4*)(sm + ACT + 2 * 16384 + 8192 + w * 128 + 64 + g * 16) = cs[1]; }
        bar();
        STAMP(2);
#pragma unroll
        for (int ct = 0; ct < 2; ++ct) {
          LAS char* const pc = sm + ACT + 2 * 16384 + 8192 + cq * 512 + ct * 64 + g * 16;
          cs[ct] = (*(LAS f32x4*)pc + *(LAS f32x4*)(pc + 128)) + (*(LAS f32x4*)(pc + 256) + *(LAS f32x4*)(pc + 384));
        }
        u32x4 g0 = *(LAS u32x4*)(sm + ACT + 2 * 16384 + (tq * 32 + li) * 16);
        u32x4 g1 = *(LAS u32x4*)(sm + ACT + 2 * 16384 + (tq * 32 + 16 + li) * 16);
        if (!__all(g0.y == ta && g0.w == tb && g1.y == ta && g1.w == tb)) {
          g0 = poll_granule(WS_XS + (m * 128 + tq * 32 + li) * 16, true, rws, ta, tb);
          g1 = poll_granule(WS_XS + (m * 128 + tq * 32 + 16 + li) * 16, true, rws, ta, tb);
        }
        mu[0] = asf(g0.x); rs[0] = asf(g0.z); mu[1] = asf(g1.x); rs[1] = asf(g1.z);
        const int blk = 2 * j + cq, grp = blk / 5;
        const int shift = (grp & 1) ? -(1 + (grp >> 1)) : (1 + (grp >> 1));
        if ((m == 0 && tq == 0 && shift > 0) || (m == 15 && tq == 3 && shift < 0)) {
          const int t = shift > 0 ? li : 2032 + li;
          const bool z = shift > 0 ? t < shift : t >= 2048 + shift;
          const u32 off = WS_H1 + ((t >> 7) * 10 + j) * 16384 + ((t >> 4) & 7) * 2048 + cq * 1024 + l * 16;
          st128_sc1((u32x4){0u, 0u, 0u, 0u}, z ? off : OOB, rws);
        }
#pragma unroll
        for (int tt = 0; tt < 2; ++tt) {
          const float r2 = rs[tt] * __builtin_amdgcn_rsqf(1.0f - LN_EPS * rs[tt] * rs[tt] + LN_EPS);
          f32x4 h[2];
#pragma unroll
          for (int ct = 0; ct < 2; ++ct)
#pragma unroll
            for (int e = 0; e < 4; ++e) h[ct][e] = gelu_f(r2 * __builtin_fmaf(-mu[tt], cs[ct][e], acc[ct][tt][e]));
          const int t = m * 128 + tq * 32 + tt * 16 + li, tp = t + shift;
          const u32 off = WS_H1 + ((tp >> 7) * 10 + j) * 16384 + ((tp >> 4) & 7) * 2048 + cq * 1024 + (g * 16 + (tp & 15)) * 16;
          st128_sc1(pack8(h[0], h[1]), (u32)tp < 2048u ? off : OOB, rws);
          SB();
        }
      }
      STAMP(3);
      waitbar<0>();
      STAMP(4);
      bar();
      STAMP(5);
#pragma unroll
      for (int a = 0; a < 2; ++a) { acc[a][0] = zero4; acc[a][1] = zero4; }
      sfor<0, 10>([&](auto ic) AINL {
        constexpr int s = ic.v;
        bar();
        if constexpr (s == 0) STAMP(17);
        step64(IC<(s > 0)>{}, IC<0>{}, F0, ab + (s & 3) * 16384, s * 8192, F1);
        step64(IC<1>{}, IC<0>{}, F1, ab + (s & 3) * 16384 + 1024, s * 8192 + 4096, F0);
      });
      bar();
      mm(F1);
      STAMP(6);
      u32x4 x1p[2];
      {
        float sm1[2], sq1[2];
#pragma unroll
        for (int tt = 0; tt < 2; ++tt) {
          float s1 = 0.f, s2 = 0.f;
          f32x4 x1[2];
#pragma unroll
          for (int ct = 0; ct < 2; ++ct) {
            const float xv[4] = {bflo(xs[ct][tt].x), bfhi(xs[ct][tt].x), bflo(xs[ct][tt].y), bfhi(xs[ct][tt].y)};
#pragma unroll
            for (int e = 0; e < 4; ++e) {
              const float v = __builtin_fmaf(xv[e] - mu[tt], rs[tt], acc[ct][tt][e]);
              x1[ct][e] = v; s1 += v; s2 = __builtin_fmaf(v, v, s2);
            }
          }
          x1p[tt] = pack8(x1[0], x1[1]);
          st128_sc1(x1p[tt], WS_X1 + (m * 10 + j) * 16384 + (2 * tq + tt) * 2048 + cq * 1024 + l * 16, rws);
          sm1[tt] = sum32(sum16(s1)); sq1[tt] = sum32(sum16(s2));
        }
        const u32x2 st = {asu(g & 1 ? sm1[1] : sm1[0]), asu(g & 1 ? sq1[1] : sq1[0])};
        st64_sc1(st, l < 32 ? WS_ST1 + m * 20480 + (2 * j + cq) * 1024 + (tq * 32 + l) * 8 : OOB, rws);
      }
      STAMP(7);
      waitbar<0>();
      STAMP(8);
      bar();
      STAMP(9);
      {
        const int sg = (g & 1) * 32;
        LAS char* const bA = pin(sm + W + (4 * g + q4) * 320 + 8 * p4 + 160 * cq + (cq ? -sg : sg));
        LAS char* const bB = pin(sm + W + (4 * g + q4) * 320 + 8 * p4 + 160 * cq + (cq ? sg : -sg));
        LAS char* const bA2 = pin(bA + 40960);
        LAS char* const bB2 = pin(bB + 40960);
        struct F3 { bf16x8 a[5], b0, b1; };
        F3 G0, G1;
        f32x4 a3[5][2];
        auto step3 = [&](auto mf, F3& R, u32 so, u32 wo, const F3& M) AINL {
          LAS char* const pA = wo >= 40960 ? bA2 + (wo - 40960) : bA + wo;
          LAS char* const pB = wo >= 40960 ? bB2 + (wo - 40960) : bB + wo;
          R.a[0] = ldstr(pA, pA + 5120); SB();
          if constexpr (mf.v) a3[0][0] = mfma(M.a[0], M.b0, a3[0][0]);
          SB(); R.b0 = lds128(ab + so); R.b1 = lds128(ab + so + 2048); SB();
          if constexpr (mf.v) a3[0][1] = mfma(M.a[0], M.b1, a3[0][1]);
          SB();
          sfor<1, 5>([&](auto ii) AINL {
            constexpr int i = ii.v;
            LAS char* const pw = ((i & 1) ? pB : pA) + 32 * i;
            R.a[i] = ldstr(pw, pw + 5120); SB();
            if constexpr (mf.v) a3[(i + 1) >> 1][(i + 1) & 1] = mfma(M.a[(i + 1) >> 1], ((i + 1) & 1) ? M.b1 : M.b0, a3[(i + 1) >> 1][(i + 1) & 1]);
            SB();
          });
          if constexpr (mf.v) {
            a3[3][0] = mfma(M.a[3], M.b0, a3[3][0]); a3[3][1] = mfma(M.a[3], M.b1, a3[3][1]);
            a3[4][0] = mfma(M.a[4], M.b0, a3[4][0]); a3[4][1] = mfma(M.a[4], M.b1, a3[4][1]);
          }
          SB();
        };
#pragma unroll
        for (int i = 0; i < 5; ++i) { a3[i][0] = zero4; a3[i][1] = zero4; }
        sfor<0, 10>([&](auto ic) AINL {
          constexpr int s = ic.v;
          constexpr u32 so = (s & 3) * 16384, wo = (s & 3) * 20480;
          bar();
          if constexpr (s == 0) STAMP(18);
          step3(IC<(s > 0)>{}, G0, so, wo, G1);
          step3(IC<1>{}, G1, so + 1024, wo + 10240, G0);
        });
#pragma unroll
        for (int i = 0; i < 5; ++i) { a3[i][0] = mfma(G1.a[i], G1.b0, a3[i][0]); a3[i][1] = mfma(G1.a[i], G1.b1, a3[i][1]); }
        bar();
        STAMP(10);
        float m1[2], r1[2];
#pragma unroll
        for (int tt = 0; tt < 2; ++tt) {
          float s1 = 0.f, s2 = 0.f;
#pragma unroll
          for (int i = 0; i < 5; ++i) {
            const f32x2 v = *(LAS f32x2*)(sm + ACT + 2 * 16384 + (g + 4 * i) * 1024 + (tq * 32 + tt * 16 + li) * 8);
            s1 += v.x; s2 += v.y;
          }
          s1 = sum32(sum16(s1)) * (1.0f / 640.0f); s2 = sum32(sum16(s2)) * (1.0f / 640.0f);
          m1[tt] = s1; r1[tt] = __builtin_amdgcn_rsqf(fmaxf(__builtin_fmaf(-s1, s1, s2), 0.f) + LN_EPS);
        }
        f32x4 c3[5];
#pragma unroll
        for (int i = 0; i < 5; ++i) c3[i] = *(LAS f32x4*)(sm + ACT + 3 * 16384 + 4096 + (80 * cq + 16 * i + 4 * g) * 4);
#pragma unroll
        for (int tt = 0; tt < 2; ++tt) {
          u32x2 pk[5];
#pragma unroll
          for (int i = 0; i < 5; ++i) {
            f32x4 h;
#pragma unroll
            for (int e = 0; e < 4; ++e) h[e] = gelu_f(r1[tt] * __builtin_fmaf(-m1[tt], c3[i][e], a3[i][tt][e]));
            pk[i] = pack4(h);
          }
          const u32 base = WS_H2 + m * 24 * 16384 + (2 * tq + tt) * 2048 + l * 16;
          auto boff = [&](int b) AINL { return b < 48 ? base + (b >> 1) * 16384 + (b & 1) * 1024 : OOB; };
          if (cq == 0) {
            st128_sc1((u32x4){pk[0].x, pk[0].y, pk[1].x, pk[1].y}, boff(5 * j), rws);
            st128_sc1((u32x4){pk[2].x, pk[2].y, pk[3].x, pk[3].y}, boff(5 * j + 1), rws);
            st64_sc1(pk[4], boff(5 * j + 2), rws);
          } else {
            st64_sc1(pk[0], boff(5 * j + 2) + 8, rws);
            st128_sc1((u32x4){pk[1].x, pk[1].y, pk[2].x, pk[2].y}, boff(5 * j + 3), rws);
            st128_sc1((u32x4){pk[3].x, pk[3].y, pk[4].x, pk[4].y}, boff(5 * j + 4), rws);
          }
          SB();
        }
      }
      STAMP(11);
      waitbar<0>();
      STAMP(12);
      const rsrc_t rdk = mk_rsrc(dskip, 1280);
      const u32x2 dk0 = bld64((cq * 32 + 4 * g) * 2, rdk, j * 128), dk1 = bld64((cq * 32 + 16 + 4 * g) * 2, rdk, j * 128);
      bar();
      STAMP(13);
#pragma unroll
      for (int a = 0; a < 2; ++a) { acc[a][0] = zero4; acc[a][1] = zero4; }
      sfor<0, 24>([&](auto ic) AINL {
        constexpr int s = ic.v;
        bar();
        if constexpr (s == 0) STAMP(19);
        step64(IC<(s > 0)>{}, IC<0>{}, F0, ab + (s & 3) * 16384, ((s + 5) % 12) * 8192, F1);
        step64(IC<1>{}, IC<0>{}, F1, ab + (s & 3) * 16384 + 1024, ((s + 5) % 12) * 8192 + 4096, F0);
      });
      mm(F1);
      bar();
      STAMP(14);
      const rsrc_t rout = mk_rsrc(out, 2048 * 2560);
#pragma unroll
      for (int tt = 0; tt < 2; ++tt) {
        const u32x4 sv = *(LAS u32x4*)(sm + ACT + w * 2048 + tt * 1024 + l * 16);
#pragma unroll
        for (int ct = 0; ct < 2; ++ct) {
          const float xv[4] = {bflo(xs[ct][tt].x), bfhi(xs[ct][tt].x), bflo(xs[ct][tt].y), bfhi(xs[ct][tt].y)};
          const u32 s0 = ct ? sv.z : sv.x, s1 = ct ? sv.w : sv.y;
          const float sy[4] = {bflo(s0), bfhi(s0), bflo(s1), bfhi(s1)};
          const u32 y0 = ct ? x1p[tt].z : x1p[tt].x, y1 = ct ? x1p[tt].w : x1p[tt].y;
          const float x1[4] = {bflo(y0), bfhi(y0), bflo(y1), bfhi(y1)};
          const u32x2 dk = ct ? dk1 : dk0;
          const float dv[4] = {bflo(dk.x), bfhi(dk.x), bflo(dk.y), bfhi(dk.y)};
          u32x4 o;
#pragma unroll
          for (int e = 0; e < 4; ++e) {
            const float sfull = __builtin_fmaf((xv[e] - mu[tt]) * rs[tt], dv[e], sy[e]);
            o[e] = asu(__builtin_fmaf(sfull, silu_f(x1[e] + acc[ct][tt][e]), xv[e]));
          }
          __builtin_amdgcn_raw_buffer_store_b128(o, rout, (m * 128 + tq * 32 + tt * 16 + li) * 2560 + (j * 64 + cq * 32 + ct * 16 + 4 * g) * 4, 0, 2);
        }
      }
      STAMP(15); CSTAMP(31);
      { asm volatile("s_waitcnt vmcnt(0)" ::: "memory");
        const u64 pe = __builtin_amdgcn_s_memrealtime();
        if (tid == 0) st128_sc1((u32x4){(u32)pe, ta, (u32)(pe >> 32), tb}, WS_STAMP + bid * 64 + 16, rws);
        if (bid == 59 && w == 0) {
          u32 mn = ~0u, mx = 0, emx = 0;
          for (int i = 0; i < 4; ++i) {
            const int b = i * 64 + l;
            const u32x2 v = bld64_sc1(WS_STAMP + b * 64, rws, 0);
            mn = min(mn, v.x - (u32)pt0 + 100000u); mx = max(mx, v.x - (u32)pt0 + 100000u);
            const bool isb = (b >> 3) < 20;
            const u32x4 e = poll_granule(WS_STAMP + b * 64 + 16, isb, rws, ta, tb);
            if (isb) emx = max(emx, e.x - (u32)pt0 + 100000u);
          }
          for (int o = 1; o < 64; o <<= 1) { mn = min(mn, (u32)__shfl_xor(mn, o)); mx = max(mx, (u32)__shfl_xor(mx, o)); emx = max(emx, (u32)__shfl_xor(emx, o)); }
          const u32 own = 100000u, owne = (u32)pe - (u32)pt0 + 100000u;
          const u64 e = __builtin_amdgcn_s_memrealtime() + 10 * (u64)__builtin_amdgcn_readfirstlane(0u);
          while (__builtin_amdgcn_s_memrealtime() < e) __builtin_amdgcn_s_sleep(2);
        } }
    }
  } else if (role < 15) {
    const int a = role - 10;
    constexpr u32 MISC = 147456, CST = 157696, TAG = 159744;
    if (w >= 8) {
      const rsrc_t rbr = mk_rsrc(b_re, 640 * 1280), rbi = mk_rsrc(b_im, 640 * 1280);
      const rsrc_t rcr = mk_rsrc(c_re, 640 * 1280), rci = mk_rsrc(c_im, 640 * 1280);
      const u32 vx0 = (m * 128 + 32 * (r8 >> 2) + 8 * L + (r8 & 3)) * 1280 + (((l & 7) ^ g) << 4);
      const u32 vx1 = (m * 128 + 32 * (2 + (r8 >> 2)) + 8 * L + (r8 & 3)) * 1280 + (((l & 7) ^ (4 + g)) << 4) - 1024;
      const u32 vb0 = (a * 128 + L * 32 + r8) * 1280 + (((l & 7) ^ g) << 4);
      const u32 vb1 = (a * 128 + L * 32 + 8 + r8) * 1280 + (((l & 7) ^ (4 + g)) << 4) - 1024;
      const u32 ldsw = L0 + L * 4096;
      auto issue_nk = [&](u32 d, rsrc_t r, u32 so) AINL { dma2<0>(d, vb0, vb1, r, so); dma2<0>(d + 2048, vb0, vb1, r, so + 20480); };
      auto issue1 = [&](int s) AINL {
        const int kt = wrapk(s + rotA1, 10);
        const u32 d = ldsw + (s % 3) * 49152;
        dma2<0>(d, vx0, vx1, rx, kt * 128); dma2<0>(d + 2048, vx0, vx1, rx, kt * 128 + 5120);
        issue_nk(d + 16384, rbr, kt * 128); issue_nk(d + 32768, rbi, kt * 128);
      };
      auto issue_c = [&](int s) AINL {
        const int kt = wrapk(s + rotA3, 20);
        issue_nk(ldsw + 49152 + (s % 6) * 16384, kt < 10 ? rcr : rci, wrapk(kt, 10) * 128);
      };
      const u32 va = L * 4096 + l * 16;
      auto issue_h = [&](int s) AINL {
        dma4<1>(ldsw + ((s & 3) == 3 ? MISC : (s & 3) * 16384), va, rws, WS_HH + (m * 20 + wrapk(s + rotA3, 20)) * 16384);
      };
      issue1(0); issue1(1);
      sfor<0, 10>([&](auto ic) AINL {
        constexpr int s = ic.v;
        waitbar<(s == 9 ? 13 : 12)>();
        if constexpr (s + 2 < 10) issue1(s + 2);
        else if constexpr (s == 8) { dma1<1>(L0 + MISC + L * 1024, L * 1024 + l * 16, rws, WS_XS + m * 2048); issue_c(0); issue_c(1); issue_c(2); }
        else { issue_c(3); issue_c(4); issue_c(5); }
        if constexpr (s == 0) get_tag(TAG);
      });
      waitbar<24>();
      bar(); bar();
      await(FL_A + m * 5 + a, FL_A + m * 5 + l, l < 5, [&]() AINL { issue_h(0); issue_h(1); issue_h(2); });
      sfor<0, 20>([&](auto ic) AINL {
        constexpr int s = ic.v;
        constexpr int gs[23] = {4, 4, 4, 4, 8, 8, 8, 8, 8, 8, 8, 8, 8, 8, 8, 8, 8, 8, 4, 4, 0, 0, 0};
        waitbar<gs[s + 1] + gs[s + 2]>();
        if constexpr (s + 3 < 20) issue_h(s + 3);
        if constexpr (s >= 1 && s + 5 < 20) issue_c(s + 5);
      });
    } else {
      const int p = a * 128 + w * 16 + li;
      const u32 plr = bld16(p * 2, mk_rsrc(lam_re, 1280)), pli = bld16(p * 2, mk_rsrc(lam_im, 1280)), pdt = bld16(p * 2, mk_rsrc(log_dt, 1280));
      row_stats();
      put_tag(TAG);
      float lbr, lbi, kr, ki;
      {
        const float Lr = bflo(plr), Li = bflo(pli);
        const float dt = bflo(pk2(expf(bflo(pdt)), 0.f));
        const float ea = expf(dt * Lr);
        float sn, cn; sincosf(dt * Li, &sn, &cn);
        lbr = ea * cn; lbi = ea * sn;
        const float inv = 2.0f / (Lr * Lr + Li * Li);
        kr = ((lbr - 1.0f) * Lr + lbi * Li) * inv;
        ki = (lbi * Lr - (lbr - 1.0f) * Li) * inv;
        asm volatile("" : "+v"(lbr), "+v"(lbi), "+v"(kr), "+v"(ki));
      }
      f32x4 are[8], aim[8], sre = zero4, sim = zero4;
#pragma unroll
      for (int i = 0; i < 8; ++i) { are[i] = zero4; aim[i] = zero4; }
      {
        struct FW { bf16x8 br, bi; };
        struct FX { bf16x8 x[4]; };
        FW W0, W1; FX Xa, Xb;
        auto half = [&](auto mf, auto rw, auto hh, FW& RW, LAS char* pw, FX& RX, LAS char* px, const FW& MW, const FX& MX) AINL {
          constexpr int h = hh.v;
          if constexpr (rw.v) { RW.br = lds128(pw); RX.x[0] = lds128(px); } else { RX.x[0] = lds128(px); RX.x[1] = lds128(px + 2048); }
          SB();
          if constexpr (mf.v) are[4 * h] = mfma(MX.x[0], MW.br, are[4 * h]);
          SB();
          if constexpr (rw.v) { RW.bi = lds128(pw + 16384); RX.x[1] = lds128(px + 2048); } else { RX.x[2] = lds128(px + 4096); RX.x[3] = lds128(px + 6144); }
          SB();
          if constexpr (mf.v) aim[4 * h] = mfma(MX.x[0], MW.bi, aim[4 * h]);
          SB();
          if constexpr (rw.v) { RX.x[2] = lds128(px + 4096); RX.x[3] = lds128(px + 6144); }
          SB();
          if constexpr (mf.v) {
#pragma unroll
            for (int i = 1; i < 4; ++i) { are[4 * h + i] = mfma(MX.x[i], MW.br, are[4 * h + i]); aim[4 * h + i] = mfma(MX.x[i], MW.bi, aim[4 * h + i]); }
            if constexpr (h == 0) sre = mfma(ones, MW.br, keep(sre)); else sim = mfma(ones, MW.bi, keep(sim));
          }
          SB();
        };
        STAMP(1);
        sfor<0, 10>([&](auto ic) AINL {
          constexpr int s = ic.v;
          LAS char* const x0 = pin(sm + rb0 + (s % 3) * 49152);
          LAS char* const x1 = pin(sm + (rb0 ^ 64) + (s % 3) * 49152);
          LAS char* const w0 = pin(x0 + w * 2048 + 16384);
          LAS char* const w1_ = pin(x1 + w * 2048 + 16384);
          bar();
          if constexpr (s == 0) STAMP(16);
          half(IC<(s > 0)>{}, IC<1>{}, IC<1>{}, W0, w0, Xa, x0, W1, Xb);
          half(IC<1>{}, IC<0>{}, IC<0>{}, W0, w0, Xb, x0 + 8192, W0, Xa);
          half(IC<1>{}, IC<1>{}, IC<1>{}, W1, w1_, Xa, x1, W0, Xb);
          half(IC<1>{}, IC<0>{}, IC<0>{}, W1, w1_, Xb, x1 + 8192, W1, Xa);
        });
#pragma unroll
        for (int i = 0; i < 4; ++i) { are[4 + i] = mfma(Xb.x[i], W1.br, are[4 + i]); aim[4 + i] = mfma(Xb.x[i], W1.bi, aim[4 + i]); }
        sim = mfma(ones, W1.bi, sim);
      }
      bar();
      STAMP(2);
      if (w < 2) {
        u32x4 g0 = *(LAS u32x4*)(sm + MISC + tid * 16);
        if (!__all(g0.y == ta && g0.w == tb)) g0 = poll_granule(WS_XS + (m * 128 + tid) * 16, true, rws, ta, tb);
        const float rs = asf(g0.z);
        *(LAS f32x2*)(sm + CST + (tid >> 5) * 272 + (tid & 31) * 8) = (f32x2){rs, -rs * asf(g0.x)};
      }
      bar();
#pragma unroll
      for (int mt = 0; mt < 8; ++mt) {
        const f32x4 s0 = *(LAS f32x4*)(sm + CST + g * 272 + mt * 32), s1 = *(LAS f32x4*)(sm + CST + g * 272 + mt * 32 + 16);
        const float rv[4] = {s0.x, s0.z, s1.x, s1.z}, nv[4] = {s0.y, s0.w, s1.y, s1.w};
#pragma unroll
        for (int e = 0; e < 4; ++e) {
          are[mt][e] = __builtin_fmaf(rv[e], are[mt][e], nv[e] * sre.x);
          aim[mt][e] = __builtin_fmaf(rv[e], aim[mt][e], nv[e] * sim.x);
        }
      }
      bar();
      float hr = 0.f, hi = 0.f;
#pragma unroll
      for (int mt = 0; mt < 8; ++mt)
#pragma unroll
        for (int e = 0; e < 4; ++e) {
          const float nr = __builtin_fmaf(-lbi, hi, __builtin_fmaf(lbr, hr, are[mt][e]));
          const float ni = __builtin_fmaf(lbi, hr, __builtin_fmaf(lbr, hi, aim[mt][e]));
          hr = nr; hi = ni;
        }
      float er[4], ei[4];
      gather4(hr, er); gather4(hi, ei);
      float p32r = lbr, p32i = lbi;
#pragma unroll
      for (int i = 0; i < 5; ++i) { const float t = p32r * p32r - p32i * p32i; p32i = 2.0f * p32r * p32i; p32r = t; }
      float cr[5], ci[5];
      cr[0] = 0.f; ci[0] = 0.f;
#pragma unroll
      for (int q = 0; q < 4; ++q) {
        cr[q + 1] = __builtin_fmaf(-p32i, ci[q], __builtin_fmaf(p32r, cr[q], er[q]));
        ci[q + 1] = __builtin_fmaf(p32i, cr[q], __builtin_fmaf(p32r, ci[q], ei[q]));
      }
      st128_sc1((u32x4){asu(cr[4]), ta, asu(ci[4]), tb}, l < 16 ? WS_EG + (m * 640 + p) * 16 : OOB, rws);
      STAMP(3);
      const float p64r = p32r * p32r - p32i * p32i, p64i = 2.0f * p32r * p32i;
      const float Pr = p64r * p64r - p64i * p64i, Pi = 2.0f * p64r * p64i;
      float Hr = 0.f, Hi = 0.f;
      if (m > 0) {
        u32x4 e0, e1, e2, e3;
        const int m0 = m - 1 - 4 * g;
        const u32 o0 = WS_EG + (m0 * 640 + p) * 16;
        const bool v0 = m0 >= 0, v1 = m0 >= 1, v2 = m0 >= 2, v3 = m0 >= 3;
        _Pragma("nounroll") for (int it = 0; it < SPIN_CAP; ++it) {
          asm volatile("buffer_load_dwordx4 %0, %4, %8, 0 offen sc1\n\tbuffer_load_dwordx4 %1, %5, %8, 0 offen sc1\n\t"
                       "buffer_load_dwordx4 %2, %6, %8, 0 offen sc1\n\tbuffer_load_dwordx4 %3, %7, %8, 0 offen sc1\n\ts_waitcnt vmcnt(0)"
                       : "=&v"(e0), "=&v"(e1), "=&v"(e2), "=&v"(e3)
                       : "v"(v0 ? o0 : OOB), "v"(v1 ? o0 - 10240 : OOB), "v"(v2 ? o0 - 20480 : OOB), "v"(v3 ? o0 - 30720 : OOB), "s"(rws) : "memory");
          const bool ok = (!v0 || (e0.y == ta && e0.w == tb)) && (!v1 || (e1.y == ta && e1.w == tb)) &&
                          (!v2 || (e2.y == ta && e2.w == tb)) && (!v3 || (e3.y == ta && e3.w == tb));
          if (__all(ok)) break;
          __builtin_amdgcn_s_sleep(1);
        }
        float qr = asf(e3.x), qi = asf(e3.z), t;
        t = __builtin_fmaf(-Pi, qi, __builtin_fmaf(Pr, qr, asf(e2.x))); qi = __builtin_fmaf(Pi, qr, __builtin_fmaf(Pr, qi, asf(e2.z))); qr = t;
        t = __builtin_fmaf(-Pi, qi, __builtin_fmaf(Pr, qr, asf(e1.x))); qi = __builtin_fmaf(Pi, qr, __builtin_fmaf(Pr, qi, asf(e1.z))); qr = t;
        t = __builtin_fmaf(-Pi, qi, __builtin_fmaf(Pr, qr, asf(e0.x))); qi = __builtin_fmaf(Pi, qr, __builtin_fmaf(Pr, qi, asf(e0.z))); qr = t;
        float gr[4], gi[4];
        gather4(qr, gr); gather4(qi, gi);
        const float P2r = Pr * Pr - Pi * Pi, P2i = 2.0f * Pr * Pi;
        const float P4r = P2r * P2r - P2i * P2i, P4i = 2.0f * P2r * P2i;
        Hr = gr[3]; Hi = gi[3];
#pragma unroll
        for (int q = 2; q >= 0; --q) {
          t = __builtin_fmaf(-P4i, Hi, __builtin_fmaf(P4r, Hr, gr[q])); Hi = __builtin_fmaf(P4i, Hr, __builtin_fmaf(P4r, Hi, gi[q])); Hr = t;
        }
      }
      STAMP(4);
      {
        const float p96r = p64r * p32r - p64i * p32i, p96i = p64r * p32i + p64i * p32r;
        const float fr = g == 0 ? 1.f : g == 1 ? p32r : g == 2 ? p64r : p96r;
        const float fi = g == 0 ? 0.f : g == 1 ? p32i : g == 2 ? p64i : p96i;
        const float c0r = g == 0 ? cr[0] : g == 1 ? cr[1] : g == 2 ? cr[2] : cr[3];
        const float c0i = g == 0 ? ci[0] : g == 1 ? ci[1] : g == 2 ? ci[2] : ci[3];
        hr = __builtin_fmaf(-fi, Hi, __builtin_fmaf(fr, Hr, c0r));
        hi = __builtin_fmaf(fi, Hr, __builtin_fmaf(fr, Hi, c0i));
      }
#pragma unroll
      for (int mt = 0; mt < 8; ++mt)
#pragma unroll
        for (int e = 0; e < 4; ++e) {
          const float nr = __builtin_fmaf(-lbi, hi, __builtin_fmaf(lbr, hr, are[mt][e]));
          const float ni = __builtin_fmaf(lbi, hr, __builtin_fmaf(lbr, hi, aim[mt][e]));
          hr = nr; hi = ni;
          are[mt][e] = __builtin_fmaf(kr, hr, -ki * hi);
          aim[mt][e] = __builtin_fmaf(-kr, hi, -ki * hr);
        }
      {
        LAS char* const tb_ = sm + (w < 6 ? w * 8192 : MISC + (w - 6) * 8192);
        const int swc = ((li & 3) << 1) | ((li >> 2) & 1) | ((li >> 3) << 3);
#pragma unroll
        for (int i = 0; i < 4; ++i) {
          *(LAS u32x4*)(tb_ + li * 256 + (((4 * g + i) ^ swc) << 4)) = pack8(are[2 * i], are[2 * i + 1]);
          *(LAS u32x4*)(tb_ + 4096 + li * 256 + (((4 * g + i) ^ swc) << 4)) = pack8(aim[2 * i], aim[2 * i + 1]);
        }
        asm volatile("s_waitcnt lgkmcnt(0)" ::: "memory");
        const int q4 = li >> 2, p4 = l & 3;
        const int c0 = 8 * (g & 1) + q4, c1 = c0 + 4;
        const int sw0 = ((c0 & 3) << 1) | ((c0 >> 3) << 3), sw1 = sw0 | 1;
#pragma unroll
        for (int part = 0; part < 2; ++part)
#pragma unroll
          for (int u = 0; u < 4; ++u) {
            const int tt = 2 * u + (g >> 1), ch = 2 * tt + (p4 >> 1);
            const bf16x8 v = ldstr(tb_ + part * 4096 + c0 * 256 + ((ch ^ sw0) << 4) + 8 * (p4 & 1),
                                   tb_ + part * 4096 + c1 * 256 + ((ch ^ sw1) << 4) + 8 * (p4 & 1));
            const u32 off = WS_HH + (m * 20 + 10 * part + 2 * a + (w >> 2)) * 16384 + tt * 2048 + ((w >> 1) & 1) * 1024 + ((2 * (w & 1) + (g & 1)) * 16 + li) * 16;
            st128_sc1(__builtin_bit_cast(u32x4, v), off, rws);
          }
      }
      waitbar<0>();
      STAMP(5);
      bar();
      STAMP(6);
      f32x4 ac[4][2];
#pragma unroll
      for (int i = 0; i < 4; ++i) { ac[i][0] = zero4; ac[i][1] = zero4; }
      LAS char* const ab = pin(sm + tq * 4096 + l * 16);
      LAS char* const ab3 = pin(ab + MISC);
      LAS char* cb[2][2];
#pragma unroll
      for (int i = 0; i < 2; ++i) { cb[0][i] = pin(sm + 49152 + i * 49152 + cq * 8192 + rb0); cb[1][i] = pin(sm + 49152 + i * 49152 + cq * 8192 + (rb0 ^ 64)); }
      struct FC { bf16x8 c[4], b0, b1; };
      FC G0, G1;
      auto stepC = [&](auto mf, FC& R, int ks, int s, const FC& M) AINL {
        LAS char* const pa = ((s & 3) == 3 ? ab3 : ab + (s & 3) * 16384) + ks * 1024;
        LAS char* const pc = cb[ks][(s % 6) / 3] + ((s % 6) % 3) * 16384;
        R.b0 = lds128(pa); R.c[0] = lds128(pc); SB();
        if constexpr (mf.v) ac[0][0] = mfma(M.c[0], M.b0, ac[0][0]);
        SB(); R.b1 = lds128(pa + 2048); R.c[1] = lds128(pc + 2048); SB();
        if constexpr (mf.v) ac[0][1] = mfma(M.c[0], M.b1, ac[0][1]);
        SB(); R.c[2] = lds128(pc + 4096); R.c[3] = lds128(pc + 6144); SB();
        if constexpr (mf.v) {
#pragma unroll
          for (int ct = 1; ct < 4; ++ct) { ac[ct][0] = mfma(M.c[ct], M.b0, ac[ct][0]); ac[ct][1] = mfma(M.c[ct], M.b1, ac[ct][1]); }
        }
        SB();
      };
      sfor<0, 20>([&](auto ic) AINL {
        constexpr int s = ic.v;
        bar();
        if constexpr (s == 0) STAMP(17);
        stepC(IC<(s > 0)>{}, G0, 0, s, G1);
        stepC(IC<1>{}, G1, 1, s, G0);
      });
#pragma unroll
      for (int ct = 0; ct < 4; ++ct) { ac[ct][0] = mfma(G1.c[ct], G1.b0, ac[ct][0]); ac[ct][1] = mfma(G1.c[ct], G1.b1, ac[ct][1]); }
      STAMP(7);
#pragma unroll
      for (int cb_ = 0; cb_ < 2; ++cb_)
#pragma unroll
        for (int tt = 0; tt < 2; ++tt)
          st128_sc1(pack8(ac[2 * cb_][tt], ac[2 * cb_ + 1][tt]), WS_SS + ((m * 10 + 2 * a + cq) * 8 + tq + 4 * cb_) * 2048 + tt * 1024 + l * 16, rws);
      waitbar<0>();
      if (w == 0) set_flag(FL_S + m * 5 + a);
      STAMP(8);
    }
  } else if (w < 8) {
    row_stats();
    u32 dummy = 0;
    const u32 lo = tid < 400 ? (m * 400 + tid) * 128 : OOB;
    touch(dummy, lo, mk_rsrc(w2, 640 * 1280));
    touch(dummy, lo, mk_rsrc(c_re, 640 * 1280));
    touch(dummy, lo, mk_rsrc(c_im, 640 * 1280));
    const rsrc_t rw4 = mk_rsrc(w4, 1536 * 1280);
    if (m < 12) {
      const rsrc_t rw3 = mk_rsrc(w3, 640 * 3072);
      float cs[8];
#pragma unroll
      for (int e = 0; e < 8; ++e) cs[e] = 0.f;
      const u32 vo = (tid >> 4) * 3072 + m * 256 + (tid & 15) * 16;
      u32x4 t[20];
#pragma unroll
      for (int i = 0; i < 20; ++i) t[i] = bld128(vo, rw3, i * 32 * 3072);
      SB();
#pragma unroll
      for (int i = 0; i < 20; ++i) {
        cs[0] += bflo(t[i].x); cs[1] += bfhi(t[i].x); cs[2] += bflo(t[i].y); cs[3] += bfhi(t[i].y);
        cs[4] += bflo(t[i].z); cs[5] += bfhi(t[i].z); cs[6] += bflo(t[i].w); cs[7] += bfhi(t[i].w);
      }
#pragma unroll
      for (int e = 0; e < 8; ++e) cs[e] = sum32(sum16(cs[e]));
      if (l < 16) {
        *(LAS f32x4*)(sm + w * 512 + l * 32) = (f32x4){cs[0], cs[1], cs[2], cs[3]};
        *(LAS f32x4*)(sm + w * 512 + l * 32 + 16) = (f32x4){cs[4], cs[5], cs[6], cs[7]};
      }
      bar();
      if (w < 2) {
        float s = 0.f;
#pragma unroll
        for (int i = 0; i < 8; ++i) s += *(LAS float*)(sm + i * 512 + tid * 4);
        st32_sc1(asu(s), WS_CS3 + (m * 128 + tid) * 4, rws);
      }
      waitbar<0>();
      if (w == 0) set_flag(FL_C + m);
    }
    STAMP(1);
    touch(dummy, (m * 960 + tid) * 128, rw4);
    touch(dummy, tid < 448 ? (m * 960 + 512 + tid) * 128 : OOB, rw4);
    asm volatile("s_waitcnt vmcnt(0)" : "+v"(dummy)::"memory");
    STAMP(2);
    if (m == 0) {
#pragma unroll
      for (int i = 0; i < 4; ++i) (void)poll_granule(WS_XS + (i * 512 + tid) * 16, true, rws, ta, tb);
      bar();
      if (tid == 0) st64_sc1((u32x2){ta, tb}, WS_EPOCH, rws);
    }
  }
#endif
}

extern "C" void kernel_launch(void* const* d_in, const int* in_sizes, int n_in, void* d_out, int out_size,
                              void* d_ws, size_t ws_size, hipStream_t stream) {
  (void)in_sizes; (void)n_in; (void)out_size; (void)ws_size;
  s5_mixer_kernel<<<256, 768, 0, stream>>>(d_in[0], d_in[1], d_in[2], d_in[3], d_in[4], d_in[5], d_in[6], d_in[7],
                                           d_in[8], d_in[10], d_in[11], d_in[12], d_in[13], d_out, d_ws);
}
```

```cpp
#include <hip/hip_runtime.h>
#include <hip/hip_bf16.h>
#include <stdint.h>

#define DI __device__ __forceinline__
#define LAS __attribute__((address_space(3)))
#define AINL __attribute__((always_inline))
typedef unsigned u32;
typedef unsigned long long u64;
typedef u32 u32x4 __attribute__((ext_vector_type(4)));
typedef u32 u32x2 __attribute__((ext_vector_type(2)));
typedef float f32x4 __attribute__((ext_vector_type(4)));
typedef float f32x2 __attribute__((ext_vector_type(2)));
typedef __bf16 bf16x8 __attribute__((ext_vector_type(8)));
typedef __bf16 bf16x2 __attribute__((ext_vector_type(2)));
typedef short s16x4 __attribute__((ext_vector_type(4)));
typedef short s16x8 __attribute__((ext_vector_type(8)));

#define XMAP 1
#define ABL 0
#define PSLEEP 2
#define STAMPS 0

constexpr u32 WS_EPOCH = 0;
constexpr u32 WS_FLAG = 0x1000;
constexpr u32 FL_1 = 0, FL_2 = 160, FL_3 = 320, FL_A = 480, FL_S = 560, FL_C = 640;
constexpr u32 WS_XS = 0x20000;
constexpr u32 WS_CS3 = 0x2A000;
constexpr u32 WS_EG = 0x30000;
constexpr u32 WS_ST1 = 0x60000;
constexpr u32 WS_SS = 0xC0000;
constexpr u32 WS_H1 = 0x400000;
constexpr u32 WS_X1 = 0x680000;
constexpr u32 WS_HH = 0x900000;
constexpr u32 WS_H2 = 0xE00000;
constexpr u32 WS_STAMP = 0x1400000;
constexpr u32 WS_BYTES = 0x1500000;
constexpr u32 OOB = 0x7FFFFFF0u;
constexpr float LN_EPS = 1e-5f;
constexpr int SPIN_CAP = 1 << 17;

template <int I> struct IC { static constexpr int v = I; };
template <int B, int E, class F> DI void sfor(F&& f) {
  if constexpr (B < E) { f(IC<B>{}); sfor<B + 1, E>(f); }
}

#if defined(__HIP_DEVICE_COMPILE__) && __HIP_DEVICE_COMPILE__
typedef __amdgpu_buffer_rsrc_t rsrc_t;
DI rsrc_t mk_rsrc(const void* p, u32 bytes) { return __builtin_amdgcn_make_buffer_rsrc((void*)p, 0, bytes, 0x00020000); }

template <int SC1> DI void dma1(u32 lds, u32 voff, rsrc_t r, u32 soff) {
  if (ABL == 2) return;
  if constexpr (SC1)
    asm volatile("s_mov_b32 m0, %0\n\ts_nop 0\n\tbuffer_load_dwordx4 %1, %2, %3 offen sc1 lds" ::"s"(lds), "v"(voff), "s"(r), "s"(soff) : "memory");
  else
    asm volatile("s_mov_b32 m0, %0\n\ts_nop 0\n\tbuffer_load_dwordx4 %1, %2, %3 offen lds" ::"s"(lds), "v"(voff), "s"(r), "s"(soff) : "memory");
}
template <int SC1> DI void dma2(u32 lds, u32 v0, u32 v1, rsrc_t r, u32 soff) {
  if (ABL == 2) return;
  if constexpr (SC1)
    asm volatile("s_mov_b32 m0, %0\n\ts_nop 0\n\tbuffer_load_dwordx4 %1, %3, %4 offen sc1 lds\n\tbuffer_load_dwordx4 %2, %3, %4 offen offset:1024 sc1 lds"
                 ::"s"(lds), "v"(v0), "v"(v1), "s"(r), "s"(soff) : "memory");
  else
    asm volatile("s_mov_b32 m0, %0\n\ts_nop 0\n\tbuffer_load_dwordx4 %1, %3, %4 offen lds\n\tbuffer_load_dwordx4 %2, %3, %4 offen offset:1024 lds"
                 ::"s"(lds), "v"(v0), "v"(v1), "s"(r), "s"(soff) : "memory");
}
template <int SC1> DI void dma4(u32 lds, u32 v, rsrc_t r, u32 soff) {
  if (ABL == 2) return;
  if constexpr (SC1)
    asm volatile("s_mov_b32 m0, %0\n\ts_nop 0\n\tbuffer_load_dwordx4 %1, %2, %3 offen sc1 lds\n\tbuffer_load_dwordx4 %1, %2, %3 offen offset:1024 sc1 lds\n\t"
                 "buffer_load_dwordx4 %1, %2, %3 offen offset:2048 sc1 lds\n\tbuffer_load_dwordx4 %1, %2, %3 offen offset:3072 sc1 lds"
                 ::"s"(lds), "v"(v), "s"(r), "s"(soff) : "memory");
  else
    asm volatile("s_mov_b32 m0, %0\n\ts_nop 0\n\tbuffer_load_dwordx4 %1, %2, %3 offen lds\n\tbuffer_load_dwordx4 %1, %2, %3 offen offset:1024 lds\n\t"
                 "buffer_load_dwordx4 %1, %2, %3 offen offset:2048 lds\n\tbuffer_load_dwordx4 %1, %2, %3 offen offset:3072 lds"
                 ::"s"(lds), "v"(v), "s"(r), "s"(soff) : "memory");
}
constexpr int VC(int n) { return ABL == 2 ? 0 : n; }
template <int N> DI void waitv() { asm volatile("s_waitcnt vmcnt(%0)" ::"i"(VC(N)) : "memory"); }
DI void bar() { asm volatile("s_waitcnt lgkmcnt(0)\n\ts_barrier" ::: "memory"); }
template <int N> DI void waitbar() { asm volatile("s_waitcnt vmcnt(%0) lgkmcnt(0)\n\ts_barrier" ::"i"(VC(N)) : "memory"); }

DI void touch(u32& d, u32 voff, rsrc_t r) { asm volatile("buffer_load_dword %0, %1, %2, 0 offen" : "+v"(d) : "v"(voff), "s"(r) : "memory"); }

DI u32x4 bld128(u32 voff, rsrc_t r, u32 soff) { return __builtin_amdgcn_raw_buffer_load_b128(r, voff, soff, 0); }
DI u32x2 bld64(u32 voff, rsrc_t r, u32 soff) { return __builtin_amdgcn_raw_buffer_load_b64(r, voff, soff, 0); }
DI u32x2 bld64_sc1(u32 voff, rsrc_t r, u32 soff) { return __builtin_amdgcn_raw_buffer_load_b64(r, voff, soff, 16); }
DI u32 bld32(u32 voff, rsrc_t r, u32 soff) { return __builtin_amdgcn_raw_buffer_load_b32(r, voff, soff, 0); }
DI u32 bld16(u32 voff, rsrc_t r) { return __builtin_amdgcn_raw_buffer_load_b16(r, voff, 0, 0); }
DI void st128_sc1(u32x4 v, u32 voff, rsrc_t r) { __builtin_amdgcn_raw_buffer_store_b128(v, r, voff, 0, 16); }
DI void st64_sc1(u32x2 v, u32 voff, rsrc_t r) { __builtin_amdgcn_raw_buffer_store_b64(v, r, voff, 0, 16); }
DI void st32_sc1(u32 v, u32 voff, rsrc_t r) { __builtin_amdgcn_raw_buffer_store_b32(v, r, voff, 0, 16); }

DI void poll_flags(u32 voff, bool active, rsrc_t ws, u32 ta, u32 tb) {
  _Pragma("nounroll") for (int it = 0; it < SPIN_CAP; ++it) {
    u32x2 v;
    asm volatile("buffer_load_dwordx2 %0, %1, %2, 0 offen sc1\n\ts_waitcnt vmcnt(0)" : "=&v"(v) : "v"(voff), "s"(ws) : "memory");
    const bool ok = !active || (v.x == ta && v.y == tb);
    if (__all(ok)) break;
    __builtin_amdgcn_s_sleep(1);
  }
}
DI u32x4 poll_granule(u32 voff, bool active, rsrc_t ws, u32 ta, u32 tb) {
  u32x4 v;
  _Pragma("nounroll") for (int it = 0; it < SPIN_CAP; ++it) {
    asm volatile("buffer_load_dwordx4 %0, %1, %2, 0 offen sc1\n\ts_waitcnt vmcnt(0)" : "=&v"(v) : "v"(voff), "s"(ws) : "memory");
    const bool ok = !active || (v.y == ta && v.w == tb);
    if (__all(ok)) break;
    __builtin_amdgcn_s_sleep(1);
  }
  return v;
}

DI float asf(u32 x) { return __uint_as_float(x); }
DI u32 asu(float x) { return __float_as_uint(x); }
DI float bflo(u32 x) { return asf(x << 16); }
DI float bfhi(u32 x) { return asf(x & 0xffff0000u); }
DI u32 pk2(float a, float b) { f32x2 p = {a, b}; bf16x2 q = __builtin_convertvector(p, bf16x2); return __builtin_bit_cast(u32, q); }
DI float sum16(float x) { const u32 u = asu(x); auto r = __builtin_amdgcn_permlane16_swap(u, u, false, false); const u32 a = r[0], b = r[1]; return asf(a) + asf(b); }
DI float sum32(float x) { const u32 u = asu(x); auto r = __builtin_amdgcn_permlane32_swap(u, u, false, false); const u32 a = r[0], b = r[1]; return asf(a) + asf(b); }
template <int C> DI float dppadd(float x) { return x + asf(__builtin_amdgcn_update_dpp(0u, asu(x), C, 0xf, 0xf, true)); }
DI float wavesum(float x) { x = dppadd<0xB1>(x); x = dppadd<0x4E>(x); x = dppadd<0x141>(x); x = dppadd<0x140>(x); return sum32(sum16(x)); }
DI void gather4(float x, float (&e)[4]) {
  const u32 u = asu(x);
  auto p = __builtin_amdgcn_permlane16_swap(u, u, false, false);
  const u32 a = p[0], b = p[1];
  auto pa = __builtin_amdgcn_permlane32_swap(a, a, false, false);
  auto pb = __builtin_amdgcn_permlane32_swap(b, b, false, false);
  const u32 e0 = pa[0], e2 = pa[1], e1 = pb[0], e3 = pb[1];
  e[0] = asf(e0); e[1] = asf(e1); e[2] = asf(e2); e[3] = asf(e3);
}
DI float gelu_f(float v) {
  const float c1 = -2.302208198f, c2 = -0.1029432397f;
  const float e = __builtin_amdgcn_exp2f(v * __builtin_fmaf(v * v, c2, c1));
  return v * __builtin_amdgcn_rcpf(1.0f + e);
}
DI float silu_f(float z) { return z * __builtin_amdgcn_rcpf(1.0f + __builtin_amdgcn_exp2f(z * -1.4426950409f)); }
DI f32x4 mfma(bf16x8 a, bf16x8 b, f32x4 c) { if (ABL == 1 || ABL == 4) { asm volatile("" : "+v"(c) : "v"(a), "v"(b)); return c; } return __builtin_amdgcn_mfma_f32_16x16x32_bf16(a, b, c, 0, 0, 0); }
DI bf16x8 lds128(LAS char* p) { if (ABL == 1 || ABL == 3) { bf16x8 r; asm volatile("" : "=v"(r)); return r; } return *(LAS bf16x8*)p; }
DI bf16x8 ldstr(LAS char* p0, LAS char* p1) {
  if (ABL == 1 || ABL == 3) { bf16x8 r; asm volatile("" : "=v"(r)); return r; }
  const s16x4 a = __builtin_amdgcn_ds_read_tr16_b64_v4i16((LAS s16x4*)p0);
  const s16x4 b = __builtin_amdgcn_ds_read_tr16_b64_v4i16((LAS s16x4*)p1);
  const s16x8 w = __builtin_shufflevector(a, b, 0, 1, 2, 3, 4, 5, 6, 7);
  return __builtin_bit_cast(bf16x8, w);
}
DI u32x4 pack8(f32x4 a, f32x4 b) { u32x4 r; r.x = pk2(a.x, a.y); r.y = pk2(a.z, a.w); r.z = pk2(b.x, b.y); r.w = pk2(b.z, b.w); return r; }
DI u32x2 pack4(f32x4 a) { u32x2 r; r.x = pk2(a.x, a.y); r.y = pk2(a.z, a.w); return r; }
DI int wrapk(int k, int n) { return k >= n ? k - n : k; }
#define SB() __builtin_amdgcn_sched_barrier(0)
DI f32x4 keep(f32x4 v) { asm volatile("" : "+v"(v)); return v; }
DI LAS char* pin(LAS char* p) { asm volatile("" : "+v"(p)); return p; }
#endif

#define STAMP(i) do { } while (0)
#define CSTAMP(i) do { } while (0)

#define ROT 0
__global__ __launch_bounds__(768, 3) void s5_mixer_kernel(
    const void* __restrict__ x, const void* __restrict__ lam_re, const void* __restrict__ lam_im,
    const void* __restrict__ b_re, const void* __restrict__ b_im, const void* __restrict__ c_re,
    const void* __restrict__ c_im, const void* __restrict__ log_dt, const void* __restrict__ dskip,
    const void* __restrict__ w1, const void* __restrict__ w2, const void* __restrict__ w3,
    const void* __restrict__ w4, void* __restrict__ out, void* __restrict__ ws) {
#if defined(__HIP_DEVICE_COMPILE__) && __HIP_DEVICE_COMPILE__
  __shared__ __attribute__((aligned(16))) char smem_[163840];
  LAS char* const sm = (LAS char*)smem_;
  const u32 L0 = (u32)(size_t)sm;
  const int tid = threadIdx.x, l = tid & 63;
  const int w = __builtin_amdgcn_readfirstlane(tid >> 6);
  const int bid = blockIdx.x, xcd = bid & 7, rr = bid >> 3;
  const int m = (xcd >> 1) * 4 + (rr < 20 ? rr / 5 : (rr - 20) / 3);
  const int role = rr < 20 ? rr % 5 + 5 * (xcd & 1) : 10 + (rr - 20) % 3 + 3 * (xcd & 1);
  const int g = l >> 4, li = l & 15, r8 = l >> 3, tq = w & 3, cq = w >> 2, L = w - 8;
  const rsrc_t rws = mk_rsrc(ws, WS_BYTES);
  const rsrc_t rx = mk_rsrc(x, 2048 * 1280);
  STAMP(0); CSTAMP(30);
  const int fm = (5 * (m & 3) + 1) >> 1;
  const int rot1 = (2 * (role % 5) + fm) % 10, rotA1 = (2 * (role % 3) + 1 + fm) % 10;
  const int rot10 = 0, rot24 = 0, rotA3 = 0;

  u32 ta, tb;
  auto row_stats = [&]() AINL {
    const int stok = m * 128 + role * 8 + w;
    const u32x2 ep = bld64_sc1(0, rws, 0);
    const u32x4 ra = bld128(l * 16, rx, stok * 1280);
    const u32 rb_ = bld32(1024 + l * 4, rx, stok * 1280);
    const u64 e = (((u64)__builtin_amdgcn_readfirstlane(ep.y) << 32) | (u32)__builtin_amdgcn_readfirstlane(ep.x)) + 0x9E3779B97F4A7C15ull;
    ta = (u32)e; tb = (u32)(e >> 32);
    float v[10] = {bflo(ra.x), bfhi(ra.x), bflo(ra.y), bfhi(ra.y), bflo(ra.z), bfhi(ra.z), bflo(ra.w), bfhi(ra.w), bflo(rb_), bfhi(rb_)};
    float s = 0.f;
#pragma unroll
    for (int i = 0; i < 10; ++i) s += v[i];
    const float mu = wavesum(s) * (1.0f / 640.0f);
    float q = 0.f;
#pragma unroll
    for (int i = 0; i < 10; ++i) { const float d = v[i] - mu; q = __builtin_fmaf(d, d, q); }
    const float rs = 1.0f / sqrtf(wavesum(q) * (1.0f / 640.0f) + LN_EPS);
    u32x4 gr = {asu(mu), ta, asu(rs), tb};
    st128_sc1(gr, l == 0 ? WS_XS + stok * 16 : OOB, rws);
  };
  auto put_tag = [&](u32 off) AINL { if (tid == 0) *(LAS u32x2*)(sm + off) = (u32x2){ta, tb}; };
  auto get_tag = [&](u32 off) AINL {
    const u32x2 t = *(volatile LAS u32x2*)(sm + off);
    ta = __builtin_amdgcn_readfirstlane(t.x); tb = __builtin_amdgcn_readfirstlane(t.y);
  };
  auto set_flag = [&](u32 idx) AINL { st64_sc1((u32x2){ta, tb}, l == 0 ? WS_FLAG + idx * 128 : OOB, rws); };
  auto await = [&](u32 own, int i, bool act, auto&& first3) AINL {
    bar();
    if (L == 0) { set_flag(own); poll_flags(WS_FLAG + i * 128, act, rws, ta, tb); }
    bar();
    first3();
  };

  const u32 rb0 = li * 128 + ((g ^ (li >> 1)) << 4);
  const bf16x8 ones = __builtin_bit_cast(bf16x8, (u32x4){0x3F803F80u, 0x3F803F80u, 0x3F803F80u, 0x3F803F80u});
  const f32x4 zero4 = {0.f, 0.f, 0.f, 0.f};

  if (role < 10) {
    const int j = role;
    constexpr u32 ACT = 0, W = 65536;
    if (w >= 8) {
      const rsrc_t rw1 = mk_rsrc(w1, 640 * 1280), rw2 = mk_rsrc(w2, 640 * 1280);
      const rsrc_t rw3 = mk_rsrc(w3, 640 * 3072), rw4 = mk_rsrc(w4, 1536 * 1280);
      const u32 vx0 = (m * 128 + L * 32 + r8) * 1280 + (((l & 7) ^ g) << 4);
      const u32 vx1 = (m * 128 + L * 32 + 8 + r8) * 1280 + (((l & 7) ^ (4 + g)) << 4) - 1024;
      const u32 wch = j * 128 + (((l & 7) ^ (2 * g)) << 4);
      const u32 vq0 = ((L >> 1) * 32 + (r8 >> 2) * 8 + (L & 1) * 4 + (r8 & 3)) * 1280 + wch, vq1 = vq0 + 16 * 1280 - 1024;
      const u32 ldsA = L0 + ACT + L * 4096, ldsW = L0 + W + L * 2048;
      auto issue_1 = [&](int s) AINL {
        const int kt = wrapk(s + rot1, 10);
        dma2<0>(ldsA + (s & 3) * 16384, vx0, vx1, rx, kt * 128);
        dma2<0>(ldsW + 65536 + (s & 3) * 8192, vq0, vq1, rw1, kt * 81920);
        dma2<0>(ldsA + (s & 3) * 16384 + 2048, vx0, vx1, rx, kt * 128 + 20480);
      };
      issue_1(0); issue_1(1); issue_1(2);
      const u32 vw0 = (L * 16 + r8) * 1280 + wch, vw1 = vw0 + 10240 - 1024;
      const u32 va = L * 4096 + l * 16, vl = L * 1024 + l * 16;
      u32 v3[5];
#pragma unroll
      for (int i = 0; i < 5; ++i) {
        const int c = 64 * i + l, r = c / 20, pos = c - 20 * r;
        v3[i] = (L * 16 + r) * 3072 + j * 320 + ((pos ^ (2 * ((r >> 2) & 1))) << 4) - 1024 * (i & 1);
      }
      auto issue_w2 = [&](int s) AINL { dma2<0>(ldsW + s * 8192, vw0, vw1, rw2, wrapk(s + rot10, 10) * 81920); };
      auto issue_w3 = [&](int s) AINL {
        const u32 d = L0 + W + (s & 3) * 20480 + L * 5120, so = wrapk(s + rot10, 10) * 196608;
        dma2<0>(d, v3[0], v3[1], rw3, so); dma2<0>(d + 2048, v3[2], v3[3], rw3, so); dma1<0>(d + 4096, v3[4], rw3, so);
      };
      auto issue_w4 = [&](int s) AINL { dma2<0>(ldsW + ((s + 5) % 12) * 8192, vw0, vw1, rw4, wrapk(s + rot24, 24) * 81920); };
      auto issue_a = [&](int s, u32 base) AINL { dma4<1>(ldsA + (s & 3) * 16384, va, rws, base); };
      sfor<0, 10>([&](auto ic) AINL {
        constexpr int s = ic.v;
        constexpr int gs[13] = {6, 6, 6, 6, 6, 6, 6, 6, 6, 6, 5, 6, 6};
        waitbar<gs[s + 1] + gs[s + 2]>();
        if constexpr (s + 3 < 10) issue_1(s + 3);
        else if constexpr (s == 7) { dma1<1>(L0 + ACT + 2 * 16384 + L * 1024, vl, rws, WS_XS + m * 2048); issue_w2(0); issue_w2(1); }
        else { issue_w2(3 * s - 22); issue_w2(3 * s - 21); issue_w2(3 * s - 20); }
        if constexpr (s == 0) get_tag(W);
      });
      waitbar<16>();
      issue_w2(8); issue_w2(9);
      await(FL_1 + m * 10 + j, FL_1 + m * 10 + (l < 10 ? l : l < 16 ? -10 + (l - 10) + (l >= 13 ? 2 : 0) : 12 + (l - 16) + (l >= 19 ? 2 : 0)),
            l < 10 || (l < 16 && m > 0) || (l >= 16 && l < 22 && m < 15), [&]() AINL {
        sfor<0, 3>([&](auto ic) AINL { constexpr int s = ic.v; issue_a(s, WS_H1 + (m * 10 + wrapk(s + rot10, 10)) * 16384); });
      });
      sfor<0, 10>([&](auto ic) AINL {
        constexpr int s = ic.v;
        constexpr int gs[13] = {4, 4, 4, 4, 4, 4, 4, 4, 4, 4, 5, 5, 5};
        waitbar<gs[s + 1] + gs[s + 2]>();
        if constexpr (s + 3 < 10) issue_a(s + 3, WS_H1 + (m * 10 + wrapk(s + 3 + rot10, 10)) * 16384);
        else issue_w3(s - 7);
      });
      bar();
      issue_w3(3);
      await(FL_2 + m * 10 + j, l < 10 ? FL_2 + m * 10 + l : FL_C + ((5 * j) >> 2) + l - 10, l < (j < 9 ? 12 : 11), [&]() AINL {
        sfor<0, 3>([&](auto ic) AINL { constexpr int s = ic.v; issue_a(s, WS_X1 + (m * 10 + wrapk(s + rot10, 10)) * 16384); });
      });
      sfor<0, 10>([&](auto ic) AINL {
        constexpr int s = ic.v;
        constexpr int gs[13] = {4, 4, 4, 4, 9, 9, 9, 9, 9, 9, 8, 8, 8};
        waitbar<gs[s + 1] + gs[s + 2]>();
        if constexpr (s + 3 < 10) {
          issue_a(s + 3, WS_X1 + (m * 10 + wrapk(s + 3 + rot10, 10)) * 16384);
          if constexpr (s >= 1) issue_w3(s + 3);
        } else if constexpr (s == 7) {
          issue_a(2, WS_ST1 + m * 20480);
          issue_w4(0); issue_w4(1);
        } else if constexpr (s == 8) {
          dma1<1>(L0 + ACT + 3 * 16384 + L * 1024, vl, rws, WS_ST1 + m * 20480 + 16384);
          dma1<1>(L0 + ACT + 3 * 16384 + 4096 + L * 1024, vl, rws, WS_CS3 + j * 640);
          issue_w4(2); issue_w4(3); issue_w4(4);
        } else { issue_w4(5); issue_w4(6); issue_w4(7); issue_w4(8); }
      });
      waitbar<14>();
      issue_w4(9); issue_w4(10); issue_w4(11);
      await(FL_3 + m * 10 + j, FL_3 + m * 10 + l, l < 10, [&]() AINL {
        sfor<0, 3>([&](auto ic) AINL { constexpr int s = ic.v; issue_a(s, WS_H2 + (m * 24 + wrapk(s + rot24, 24)) * 16384); });
      });
      u32x2 fs;
      const u32 fso = WS_FLAG + (FL_S + m * 5 + (j >> 1)) * 128;
      sfor<0, 24>([&](auto ic) AINL {
        constexpr int s = ic.v;
        constexpr int gs[27] = {4, 4, 4, 4, 6, 6, 6, 6, 6, 6, 6, 6, 6, 6, 6, 6, 4, 4, 4, 4, 5, 4, 4, 4, 4, 0, 0};
        if constexpr (s == 20) { asm volatile("s_waitcnt vmcnt(%1) lgkmcnt(0)\n\ts_barrier" : "+v"(fs) : "i"(VC(gs[s + 1] + gs[s + 2])) : "memory"); }
        else waitbar<gs[s + 1] + gs[s + 2]>();
        if constexpr (s == 17) asm volatile("buffer_load_dwordx2 %0, %1, %2, 0 offen sc1" : "=v"(fs) : "v"(fso), "s"(rws) : "memory");
        if constexpr (s + 3 < 24) issue_a(s + 3, WS_H2 + (m * 24 + wrapk(s + 3 + rot24, 24)) * 16384);
        if constexpr (s >= 1 && s <= 12) issue_w4(s + 11);
        if constexpr (s == 21) {
          if (!__all(fs.x == ta && fs.y == tb)) poll_flags(fso, true, rws, ta, tb);
          issue_a(0, WS_SS + (m * 10 + j) * 16384);
        }
      });
      waitbar<0>();
    } else {
      row_stats();
      put_tag(W);
      const int q4 = li >> 2, p4 = l & 3;
      LAS char* const wb0 = pin(sm + W + (4 * g + q4) * 128 + ((((2 * cq) ^ ((2 * g + (q4 >> 1)) & 3))) << 5) + 8 * p4);
      LAS char* const wb1 = pin(sm + W + (4 * g + q4) * 128 + ((((2 * cq + 1) ^ ((2 * g + (q4 >> 1)) & 3))) << 5) + 8 * p4);
      LAS char* const wh0 = pin(wb0 + 65536);
      LAS char* const wh1 = pin(wb1 + 65536);
      LAS char* const ab = pin(sm + ACT + tq * 4096 + l * 16);
      struct F64 { bf16x8 a0, a1, b0, b1; };
      F64 F0, F1;
      f32x4 acc[2][2], cs[2];
      u32x2 xs[2][2];
      auto step64 = [&](auto mf, auto cs_on, F64& R, LAS char* pb, u32 wo, const F64& M) AINL {
        LAS char* const p0 = wo >= 65536 ? wh0 + (wo - 65536) : wb0 + wo;
        LAS char* const p1 = wo >= 65536 ? wh1 + (wo - 65536) : wb1 + wo;
        R.a0 = ldstr(p0, p0 + 2048); SB();
        if constexpr (mf.v) acc[0][0] = mfma(M.a0, M.b0, acc[0][0]);
        SB(); R.b0 = lds128(pb); R.b1 = lds128(pb + 2048); SB();
        if constexpr (mf.v) acc[1][0] = mfma(M.a1, M.b0, acc[1][0]);
        SB(); R.a1 = ldstr(p1, p1 + 2048); SB();
        if constexpr (mf.v) { acc[0][1] = mfma(M.a0, M.b1, acc[0][1]); acc[1][1] = mfma(M.a1, M.b1, acc[1][1]); }
        if constexpr (mf.v && cs_on.v) { cs[0] = mfma(M.a0, ones, keep(cs[0])); cs[1] = mfma(M.a1, ones, keep(cs[1])); }
        SB();
      };
      auto mm = [&](const F64& M) AINL {
        acc[0][0] = mfma(M.a0, M.b0, acc[0][0]); acc[1][0] = mfma(M.a1, M.b0, acc[1][0]);
        acc[0][1] = mfma(M.a0, M.b1, acc[0][1]); acc[1][1] = mfma(M.a1, M.b1, acc[1][1]);
      };
      float mu[2], rs[2];
      {
        LAS char* const xb0 = pin(sm + ACT + tq * 4096 + rb0);
        LAS char* const xb1 = pin(sm + ACT + tq * 4096 + (rb0 ^ 64));
        const int s_own = wrapk(j - rot1 + 10, 10);
#pragma unroll
        for (int a = 0; a < 2; ++a) { cs[a] = zero4; acc[a][0] = zero4; acc[a][1] = zero4; }
        STAMP(1);
        sfor<0, 10>([&](auto ic) AINL {
          constexpr int s = ic.v;
          constexpr u32 so = (s & 3) * 16384, wo = 65536 + (s & 3) * 8192;
          bar();
          if constexpr (s == 0) STAMP(16);
          step64(IC<(s > 0)>{}, IC<0>{}, F0, xb0 + so, wo, F1);
          if (s > 0 && tq == ((2 * s - 1) & 3)) { cs[0] = mfma(F1.a0, ones, keep(cs[0])); cs[1] = mfma(F1.a1, ones, keep(cs[1])); }
          step64(IC<1>{}, IC<0>{}, F1, xb1 + so, wo + 4096, F0);
          if (tq == ((2 * s) & 3)) { cs[0] = mfma(F0.a0, ones, keep(cs[0])); cs[1] = mfma(F0.a1, ones, keep(cs[1])); }
          if (s == s_own) {
#pragma unroll
            for (int tt = 0; tt < 2; ++tt)
#pragma unroll
              for (int ct = 0; ct < 2; ++ct)
                xs[ct][tt] = *(LAS u32x2*)(sm + ACT + so + tq * 4096 + tt * 2048 + li * 128 + (((4 * cq + 2 * ct + (g >> 1)) ^ (li >> 1)) << 4) + 8 * (g & 1));
          }
        });
        mm(F1);
        if (tq == 3) { cs[0] = mfma(F1.a0, ones, cs[0]); cs[1] = mfma(F1.a1, ones, cs[1]); }
        if (li == 0) { *(LAS f32x4*)(sm + ACT + 2 * 16384 + 8192 + w * 128 + g * 16) = cs[0]; *(LAS f32x4*)(sm + ACT + 2 * 16384 + 8192 + w * 128 + 64 + g * 16) = cs[1]; }
        bar();
        STAMP(2);
#pragma unroll
        for (int ct = 0; ct < 2; ++ct) {
          LAS char* const pc = sm + ACT + 2 * 16384 + 8192 + cq * 512 + ct * 64 + g * 16;
          cs[ct] = (*(LAS f32x4*)pc + *(LAS f32x4*)(pc + 128)) + (*(LAS f32x4*)(pc + 256) + *(LAS f32x4*)(pc + 384));
        }
        u32x4 g0 = *(LAS u32x4*)(sm + ACT + 2 * 16384 + (tq * 32 + li) * 16);
        u32x4 g1 = *(LAS u32x4*)(sm + ACT + 2 * 16384 + (tq * 32 + 16 + li) * 16);
        if (!__all(g0.y == ta && g0.w == tb && g1.y == ta && g1.w == tb)) {
          g0 = poll_granule(WS_XS + (m * 128 + tq * 32 + li) * 16, true, rws, ta, tb);
          g1 = poll_granule(WS_XS + (m * 128 + tq * 32 + 16 + li) * 16, true, rws, ta, tb);
        }
        mu[0] = asf(g0.x); rs[0] = asf(g0.z); mu[1] = asf(g1.x); rs[1] = asf(g1.z);
        const int blk = 2 * j + cq, grp = blk / 5;
        const int shift = (grp & 1) ? -(1 + (grp >> 1)) : (1 + (grp >> 1));
        if ((m == 0 && tq == 0 && shift > 0) || (m == 15 && tq == 3 && shift < 0)) {
          const int t = shift > 0 ? li : 2032 + li;
          const bool z = shift > 0 ? t < shift : t >= 2048 + shift;
          const u32 off = WS_H1 + ((t >> 7) * 10 + j) * 16384 + ((t >> 4) & 7) * 2048 + cq * 1024 + l * 16;
          st128_sc1((u32x4){0u, 0u, 0u, 0u}, z ? off : OOB, rws);
        }
#pragma unroll
        for (int tt = 0; tt < 2; ++tt) {
          const float r2 = rs[tt] * __builtin_amdgcn_rsqf(1.0f - LN_EPS * rs[tt] * rs[tt] + LN_EPS);
          f32x4 h[2];
#pragma unroll
          for (int ct = 0; ct < 2; ++ct)
#pragma unroll
            for (int e = 0; e < 4; ++e) h[ct][e] = gelu_f(r2 * __builtin_fmaf(-mu[tt], cs[ct][e], acc[ct][tt][e]));
          const int t = m * 128 + tq * 32 + tt * 16 + li, tp = t + shift;
          const u32 off = WS_H1 + ((tp >> 7) * 10 + j) * 16384 + ((tp >> 4) & 7) * 2048 + cq * 1024 + (g * 16 + (tp & 15)) * 16;
          st128_sc1(pack8(h[0], h[1]), (u32)tp < 2048u ? off : OOB, rws);
          SB();
        }
      }
      STAMP(3);
      waitbar<0>();
      STAMP(4);
      bar();
      STAMP(5);
#pragma unroll
      for (int a = 0; a < 2; ++a) { acc[a][0] = zero4; acc[a][1] = zero4; }
      sfor<0, 10>([&](auto ic) AINL {
        constexpr int s = ic.v;
        bar();
        if constexpr (s == 0) STAMP(17);
        step64(IC<(s > 0)>{}, IC<0>{}, F0, ab + (s & 3) * 16384, s * 8192, F1);
        step64(IC<1>{}, IC<0>{}, F1, ab + (s & 3) * 16384 + 1024, s * 8192 + 4096, F0);
      });
      bar();
      mm(F1);
      STAMP(6);
      u32x4 x1p[2];
      {
        float sm1[2], sq1[2];
#pragma unroll
        for (int tt = 0; tt < 2; ++tt) {
          float s1 = 0.f, s2 = 0.f;
          f32x4 x1[2];
#pragma unroll
          for (int ct = 0; ct < 2; ++ct) {
            const float xv[4] = {bflo(xs[ct][tt].x), bfhi(xs[ct][tt].x), bflo(xs[ct][tt].y), bfhi(xs[ct][tt].y)};
#pragma unroll
            for (int e = 0; e < 4; ++e) {
              const float v = __builtin_fmaf(xv[e] - mu[tt], rs[tt], acc[ct][tt][e]);
              x1[ct][e] = v; s1 += v; s2 = __builtin_fmaf(v, v, s2);
            }
          }
          x1p[tt] = pack8(x1[0], x1[1]);
          st128_sc1(x1p[tt], WS_X1 + (m * 10 + j) * 16384 + (2 * tq + tt) * 2048 + cq * 1024 + l * 16, rws);
          sm1[tt] = sum32(sum16(s1)); sq1[tt] = sum32(sum16(s2));
        }
        const u32x2 st = {asu(g & 1 ? sm1[1] : sm1[0]), asu(g & 1 ? sq1[1] : sq1[0])};
        st64_sc1(st, l < 32 ? WS_ST1 + m * 20480 + (2 * j + cq) * 1024 + (tq * 32 + l) * 8 : OOB, rws);
      }
      STAMP(7);
      waitbar<0>();
      STAMP(8);
      bar();
      STAMP(9);
      {
        const int sg = (g & 1) * 32;
        LAS char* const bA = pin(sm + W + (4 * g + q4) * 320 + 8 * p4 + 160 * cq + (cq ? -sg : sg));
        LAS char* const bB = pin(sm + W + (4 * g + q4) * 320 + 8 * p4 + 160 * cq + (cq ? sg : -sg));
        LAS char* const bA2 = pin(bA + 40960);
        LAS char* const bB2 = pin(bB + 40960);
        struct F3 { bf16x8 a[5], b0, b1; };
        F3 G0, G1;
        f32x4 a3[5][2];
        auto step3 = [&](auto mf, F3& R, u32 so, u32 wo, const F3& M) AINL {
          LAS char* const pA = wo >= 40960 ? bA2 + (wo - 40960) : bA + wo;
          LAS char* const pB = wo >= 40960 ? bB2 + (wo - 40960) : bB + wo;
          R.a[0] = ldstr(pA, pA + 5120); SB();
          if constexpr (mf.v) a3[0][0] = mfma(M.a[0], M.b0, a3[0][0]);
          SB(); R.b0 = lds128(ab + so); R.b1 = lds128(ab + so + 2048); SB();
          if constexpr (mf.v) a3[0][1] = mfma(M.a[0], M.b1, a3[0][1]);
          SB();
          sfor<1, 5>([&](auto ii) AINL {
            constexpr int i = ii.v;
            LAS char* const pw = ((i & 1) ? pB : pA) + 32 * i;
            R.a[i] = ldstr(pw, pw + 5120); SB();
            if constexpr (mf.v) a3[(i + 1) >> 1][(i + 1) & 1] = mfma(M.a[(i + 1) >> 1], ((i + 1) & 1) ? M.b1 : M.b0, a3[(i + 1) >> 1][(i + 1) & 1]);
            SB();
          });
          if constexpr (mf.v) {
            a3[3][0] = mfma(M.a[3], M.b0, a3[3][0]); a3[3][1] = mfma(M.a[3], M.b1, a3[3][1]);
            a3[4][0] = mfma(M.a[4], M.b0, a3[4][0]); a3[4][1] = mfma(M.a[4], M.b1, a3[4][1]);
          }
          SB();
        };
#pragma unroll
        for (int i = 0; i < 5; ++i) { a3[i][0] = zero4; a3[i][1] = zero4; }
        sfor<0, 10>([&](auto ic) AINL {
          constexpr int s = ic.v;
          constexpr u32 so = (s & 3) * 16384, wo = (s & 3) * 20480;
          bar();
          if constexpr (s == 0) STAMP(18);
          step3(IC<(s > 0)>{}, G0, so, wo, G1);
          step3(IC<1>{}, G1, so + 1024, wo + 10240, G0);
        });
#pragma unroll
        for (int i = 0; i < 5; ++i) { a3[i][0] = mfma(G1.a[i], G1.b0, a3[i][0]); a3[i][1] = mfma(G1.a[i], G1.b1, a3[i][1]); }
        bar();
        STAMP(10);
        float m1[2], r1[2];
#pragma unroll
        for (int tt = 0; tt < 2; ++tt) {
          float s1 = 0.f, s2 = 0.f;
#pragma unroll
          for (int i = 0; i < 5; ++i) {
            const f32x2 v = *(LAS f32x2*)(sm + ACT + 2 * 16384 + (g + 4 * i) * 1024 + (tq * 32 + tt * 16 + li) * 8);
            s1 += v.x; s2 += v.y;
          }
          s1 = sum32(sum16(s1)) * (1.0f / 640.0f); s2 = sum32(sum16(s2)) * (1.0f / 640.0f);
          m1[tt] = s1; r1[tt] = __builtin_amdgcn_rsqf(fmaxf(__builtin_fmaf(-s1, s1, s2), 0.f) + LN_EPS);
        }
        f32x4 c3[5];
#pragma unroll
        for (int i = 0; i < 5; ++i) c3[i] = *(LAS f32x4*)(sm + ACT + 3 * 16384 + 4096 + (80 * cq + 16 * i + 4 * g) * 4);
#pragma unroll
        for (int tt = 0; tt < 2; ++tt) {
          u32x2 pk[5];
#pragma unroll
          for (int i = 0; i < 5; ++i) {
            f32x4 h;
#pragma unroll
            for (int e = 0; e < 4; ++e) h[e] = gelu_f(r1[tt] * __builtin_fmaf(-m1[tt], c3[i][e], a3[i][tt][e]));
            pk[i] = pack4(h);
          }
          const u32 base = WS_H2 + m * 24 * 16384 + (2 * tq + tt) * 2048 + l * 16;
          auto boff = [&](int b) AINL { return b < 48 ? base + (b >> 1) * 16384 + (b & 1) * 1024 : OOB; };
          if (cq == 0) {
            st128_sc1((u32x4){pk[0].x, pk[0].y, pk[1].x, pk[1].y}, boff(5 * j), rws);
            st128_sc1((u32x4){pk[2].x, pk[2].y, pk[3].x, pk[3].y}, boff(5 * j + 1), rws);
            st64_sc1(pk[4], boff(5 * j + 2), rws);
          } else {
            st64_sc1(pk[0], boff(5 * j + 2) + 8, rws);
            st128_sc1((u32x4){pk[1].x, pk[1].y, pk[2].x, pk[2].y}, boff(5 * j + 3), rws);
            st128_sc1((u32x4){pk[3].x, pk[3].y, pk[4].x, pk[4].y}, boff(5 * j + 4), rws);
          }
          SB();
        }
      }
      STAMP(11);
      waitbar<0>();
      STAMP(12);
      const rsrc_t rdk = mk_rsrc(dskip, 1280);
      const u32x2 dk0 = bld64((cq * 32 + 4 * g) * 2, rdk, j * 128), dk1 = bld64((cq * 32 + 16 + 4 * g) * 2, rdk, j * 128);
      bar();
      STAMP(13);
#pragma unroll
      for (int a = 0; a < 2; ++a) { acc[a][0] = zero4; acc[a][1] = zero4; }
      sfor<0, 24>([&](auto ic) AINL {
        constexpr int s = ic.v;
        bar();
        if constexpr (s == 0) STAMP(19);
        step64(IC<(s > 0)>{}, IC<0>{}, F0, ab + (s & 3) * 16384, ((s + 5) % 12) * 8192, F1);
        step64(IC<1>{}, IC<0>{}, F1, ab + (s & 3) * 16384 + 1024, ((s + 5) % 12) * 8192 + 4096, F0);
      });
      mm(F1);
      bar();
      STAMP(14);
      const rsrc_t rout = mk_rsrc(out, 2048 * 2560);
#pragma unroll
      for (int tt = 0; tt < 2; ++tt) {
        const u32x4 sv = *(LAS u32x4*)(sm + ACT + w * 2048 + tt * 1024 + l * 16);
#pragma unroll
        for (int ct = 0; ct < 2; ++ct) {
          const float xv[4] = {bflo(xs[ct][tt].x), bfhi(xs[ct][tt].x), bflo(xs[ct][tt].y), bfhi(xs[ct][tt].y)};
          const u32 s0 = ct ? sv.z : sv.x, s1 = ct ? sv.w : sv.y;
          const float sy[4] = {bflo(s0), bfhi(s0), bflo(s1), bfhi(s1)};
          const u32 y0 = ct ? x1p[tt].z : x1p[tt].x, y1 = ct ? x1p[tt].w : x1p[tt].y;
          const float x1[4] = {bflo(y0), bfhi(y0), bflo(y1), bfhi(y1)};
          const u32x2 dk = ct ? dk1 : dk0;
          const float dv[4] = {bflo(dk.x), bfhi(dk.x), bflo(dk.y), bfhi(dk.y)};
          u32x4 o;
#pragma unroll
          for (int e = 0; e < 4; ++e) {
            const float sfull = __builtin_fmaf((xv[e] - mu[tt]) * rs[tt], dv[e], sy[e]);
            o[e] = asu(__builtin_fmaf(sfull, silu_f(x1[e] + acc[ct][tt][e]), xv[e]));
          }
          __builtin_amdgcn_raw_buffer_store_b128(o, rout, (m * 128 + tq * 32 + tt * 16 + li) * 2560 + (j * 64 + cq * 32 + ct * 16 + 4 * g) * 4, 0, 0);
        }
      }
      STAMP(15); CSTAMP(31);
      if (blockIdx.x == 59 && w == 0) { const u64 e = __builtin_amdgcn_s_memrealtime() + 200; while (__builtin_amdgcn_s_memrealtime() < e) __builtin_amdgcn_s_sleep(2); }
    }
  } else if (role < 15) {
    const int a = role - 10;
    constexpr u32 MISC = 147456, CST = 157696, TAG = 159744;
    if (w >= 8) {
      const rsrc_t rbr = mk_rsrc(b_re, 640 * 1280), rbi = mk_rsrc(b_im, 640 * 1280);
      const rsrc_t rcr = mk_rsrc(c_re, 640 * 1280), rci = mk_rsrc(c_im, 640 * 1280);
      const u32 vx0 = (m * 128 + 32 * (r8 >> 2) + 8 * L + (r8 & 3)) * 1280 + (((l & 7) ^ g) << 4);
      const u32 vx1 = (m * 128 + 32 * (2 + (r8 >> 2)) + 8 * L + (r8 & 3)) * 1280 + (((l & 7) ^ (4 + g)) << 4) - 1024;
      const u32 vb0 = (a * 128 + L * 32 + r8) * 1280 + (((l & 7) ^ g) << 4);
      const u32 vb1 = (a * 128 + L * 32 + 8 + r8) * 1280 + (((l & 7) ^ (4 + g)) << 4) - 1024;
      const u32 ldsw = L0 + L * 4096;
      auto issue_nk = [&](u32 d, rsrc_t r, u32 so) AINL { dma2<0>(d, vb0, vb1, r, so); dma2<0>(d + 2048, vb0, vb1, r, so + 20480); };
      auto issue1 = [&](int s) AINL {
        const int kt = wrapk(s + rotA1, 10);
        const u32 d = ldsw + (s % 3) * 49152;
        dma2<0>(d, vx0, vx1, rx, kt * 128); dma2<0>(d + 2048, vx0, vx1, rx, kt * 128 + 5120);
        issue_nk(d + 16384, rbr, kt * 128); issue_nk(d + 32768, rbi, kt * 128);
      };
      auto issue_c = [&](int s) AINL {
        const int kt = wrapk(s + rotA3, 20);
        issue_nk(ldsw + 49152 + (s % 6) * 16384, kt < 10 ? rcr : rci, wrapk(kt, 10) * 128);
      };
      const u32 va = L * 4096 + l * 16;
      auto issue_h = [&](int s) AINL {
        dma4<1>(ldsw + ((s & 3) == 3 ? MISC : (s & 3) * 16384), va, rws, WS_HH + (m * 20 + wrapk(s + rotA3, 20)) * 16384);
      };
      issue1(0); issue1(1);
      sfor<0, 10>([&](auto ic) AINL {
        constexpr int s = ic.v;
        waitbar<(s == 9 ? 13 : 12)>();
        if constexpr (s + 2 < 10) issue1(s + 2);
        else if constexpr (s == 8) { dma1<1>(L0 + MISC + L * 1024, L * 1024 + l * 16, rws, WS_XS + m * 2048); issue_c(0); issue_c(1); issue_c(2); }
        else { issue_c(3); issue_c(4); issue_c(5); }
        if constexpr (s == 0) get_tag(TAG);
      });
      waitbar<24>();
      bar(); bar();
      await(FL_A + m * 5 + a, FL_A + m * 5 + l, l < 5, [&]() AINL { issue_h(0); issue_h(1); issue_h(2); });
      sfor<0, 20>([&](auto ic) AINL {
        constexpr int s = ic.v;
        constexpr int gs[23] = {4, 4, 4, 4, 8, 8, 8, 8, 8, 8, 8, 8, 8, 8, 8, 8, 8, 8, 4, 4, 0, 0, 0};
        waitbar<gs[s + 1] + gs[s + 2]>();
        if constexpr (s + 3 < 20) issue_h(s + 3);
        if constexpr (s >= 1 && s + 5 < 20) issue_c(s + 5);
      });
    } else {
      const int p = a * 128 + w * 16 + li;
      const u32 plr = bld16(p * 2, mk_rsrc(lam_re, 1280)), pli = bld16(p * 2, mk_rsrc(lam_im, 1280)), pdt = bld16(p * 2, mk_rsrc(log_dt, 1280));
      row_stats();
      put_tag(TAG);
      float lbr, lbi, kr, ki;
      {
        const float Lr = bflo(plr), Li = bflo(pli);
        const float dt = bflo(pk2(expf(bflo(pdt)), 0.f));
        const float ea = expf(dt * Lr);
        float sn, cn; sincosf(dt * Li, &sn, &cn);
        lbr = ea * cn; lbi = ea * sn;
        const float inv = 2.0f / (Lr * Lr + Li * Li);
        kr = ((lbr - 1.0f) * Lr + lbi * Li) * inv;
        ki = (lbi * Lr - (lbr - 1.0f) * Li) * inv;
        asm volatile("" : "+v"(lbr), "+v"(lbi), "+v"(kr), "+v"(ki));
      }
      f32x4 are[8], aim[8], sre = zero4, sim = zero4;
#pragma unroll
      for (int i = 0; i < 8; ++i) { are[i] = zero4; aim[i] = zero4; }
      {
        struct FW { bf16x8 br, bi; };
        struct FX { bf16x8 x[4]; };
        FW W0, W1; FX Xa, Xb;
        auto half = [&](auto mf, auto rw, auto hh, FW& RW, LAS char* pw, FX& RX, LAS char* px, const FW& MW, const FX& MX) AINL {
          constexpr int h = hh.v;
          if constexpr (rw.v) { RW.br = lds128(pw); RX.x[0] = lds128(px); } else { RX.x[0] = lds128(px); RX.x[1] = lds128(px + 2048); }
          SB();
          if constexpr (mf.v) are[4 * h] = mfma(MX.x[0], MW.br, are[4 * h]);
          SB();
          if constexpr (rw.v) { RW.bi = lds128(pw + 16384); RX.x[1] = lds128(px + 2048); } else { RX.x[2] = lds128(px + 4096); RX.x[3] = lds128(px + 6144); }
          SB();
          if constexpr (mf.v) aim[4 * h] = mfma(MX.x[0], MW.bi, aim[4 * h]);
          SB();
          if constexpr (rw.v) { RX.x[2] = lds128(px + 4096); RX.x[3] = lds128(px + 6144); }
          SB();
          if constexpr (mf.v) {
#pragma unroll
            for (int i = 1; i < 4; ++i) { are[4 * h + i] = mfma(MX.x[i], MW.br, are[4 * h + i]); aim[4 * h + i] = mfma(MX.x[i], MW.bi, aim[4 * h + i]); }
            if constexpr (h == 0) sre = mfma(ones, MW.br, keep(sre)); else sim = mfma(ones, MW.bi, keep(sim));
          }
          SB();
        };
        STAMP(1);
        sfor<0, 10>([&](auto ic) AINL {
          constexpr int s = ic.v;
          LAS char* const x0 = pin(sm + rb0 + (s % 3) * 49152);
          LAS char* const x1 = pin(sm + (rb0 ^ 64) + (s % 3) * 49152);
          LAS char* const w0 = pin(x0 + w * 2048 + 16384);
          LAS char* const w1_ = pin(x1 + w * 2048 + 16384);
          bar();
          if constexpr (s == 0) STAMP(16);
          half(IC<(s > 0)>{}, IC<1>{}, IC<1>{}, W0, w0, Xa, x0, W1, Xb);
          half(IC<1>{}, IC<0>{}, IC<0>{}, W0, w0, Xb, x0 + 8192, W0, Xa);
          half(IC<1>{}, IC<1>{}, IC<1>{}, W1, w1_, Xa, x1, W0, Xb);
          half(IC<1>{}, IC<0>{}, IC<0>{}, W1, w1_, Xb, x1 + 8192, W1, Xa);
        });
#pragma unroll
        for (int i = 0; i < 4; ++i) { are[4 + i] = mfma(Xb.x[i], W1.br, are[4 + i]); aim[4 + i] = mfma(Xb.x[i], W1.bi, aim[4 + i]); }
        sim = mfma(ones, W1.bi, sim);
      }
      bar();
      STAMP(2);
      if (w < 2) {
        u32x4 g0 = *(LAS u32x4*)(sm + MISC + tid * 16);
        if (!__all(g0.y == ta && g0.w == tb)) g0 = poll_granule(WS_XS + (m * 128 + tid) * 16, true, rws, ta, tb);
        const float rs = asf(g0.z);
        *(LAS f32x2*)(sm + CST + (tid >> 5) * 272 + (tid & 31) * 8) = (f32x2){rs, -rs * asf(g0.x)};
      }
      bar();
#pragma unroll
      for (int mt = 0; mt < 8; ++mt) {
        const f32x4 s0 = *(LAS f32x4*)(sm + CST + g * 272 + mt * 32), s1 = *(LAS f32x4*)(sm + CST + g * 272 + mt * 32 + 16);
        const float rv[4] = {s0.x, s0.z, s1.x, s1.z}, nv[4] = {s0.y, s0.w, s1.y, s1.w};
#pragma unroll
        for (int e = 0; e < 4; ++e) {
          are[mt][e] = __builtin_fmaf(rv[e], are[mt][e], nv[e] * sre.x);
          aim[mt][e] = __builtin_fmaf(rv[e], aim[mt][e], nv[e] * sim.x);
        }
      }
      bar();
      float hr = 0.f, hi = 0.f;
#pragma unroll
      for (int mt = 0; mt < 8; ++mt)
#pragma unroll
        for (int e = 0; e < 4; ++e) {
          const float nr = __builtin_fmaf(-lbi, hi, __builtin_fmaf(lbr, hr, are[mt][e]));
          const float ni = __builtin_fmaf(lbi, hr, __builtin_fmaf(lbr, hi, aim[mt][e]));
          hr = nr; hi = ni;
        }
      float er[4], ei[4];
      gather4(hr, er); gather4(hi, ei);
      float p32r = lbr, p32i = lbi;
#pragma unroll
      for (int i = 0; i < 5; ++i) { const float t = p32r * p32r - p32i * p32i; p32i = 2.0f * p32r * p32i; p32r = t; }
      float cr[5], ci[5];
      cr[0] = 0.f; ci[0] = 0.f;
#pragma unroll
      for (int q = 0; q < 4; ++q) {
        cr[q + 1] = __builtin_fmaf(-p32i, ci[q], __builtin_fmaf(p32r, cr[q], er[q]));
        ci[q + 1] = __builtin_fmaf(p32i, cr[q], __builtin_fmaf(p32r, ci[q], ei[q]));
      }
      st128_sc1((u32x4){asu(cr[4]), ta, asu(ci[4]), tb}, l < 16 ? WS_EG + (m * 640 + p) * 16 : OOB, rws);
      STAMP(3);
      const float p64r = p32r * p32r - p32i * p32i, p64i = 2.0f * p32r * p32i;
      const float Pr = p64r * p64r - p64i * p64i, Pi = 2.0f * p64r * p64i;
      float Hr = 0.f, Hi = 0.f;
      if (m > 0) {
        u32x4 e0, e1, e2, e3;
        const int m0 = m - 1 - 4 * g;
        const u32 o0 = WS_EG + (m0 * 640 + p) * 16;
        const bool v0 = m0 >= 0, v1 = m0 >= 1, v2 = m0 >= 2, v3 = m0 >= 3;
        _Pragma("nounroll") for (int it = 0; it < SPIN_CAP; ++it) {
          asm volatile("buffer_load_dwordx4 %0, %4, %8, 0 offen sc1\n\tbuffer_load_dwordx4 %1, %5, %8, 0 offen sc1\n\t"
                       "buffer_load_dwordx4 %2, %6, %8, 0 offen sc1\n\tbuffer_load_dwordx4 %3, %7, %8, 0 offen sc1\n\ts_waitcnt vmcnt(0)"
                       : "=&v"(e0), "=&v"(e1), "=&v"(e2), "=&v"(e3)
                       : "v"(v0 ? o0 : OOB), "v"(v1 ? o0 - 10240 : OOB), "v"(v2 ? o0 - 20480 : OOB), "v"(v3 ? o0 - 30720 : OOB), "s"(rws) : "memory");
          const bool ok = (!v0 || (e0.y == ta && e0.w == tb)) && (!v1 || (e1.y == ta && e1.w == tb)) &&
                          (!v2 || (e2.y == ta && e2.w == tb)) && (!v3 || (e3.y == ta && e3.w == tb));
          if (__all(ok)) break;
          __builtin_amdgcn_s_sleep(1);
        }
        float qr = asf(e3.x), qi = asf(e3.z), t;
        t = __builtin_fmaf(-Pi, qi, __builtin_fmaf(Pr, qr, asf(e2.x))); qi = __builtin_fmaf(Pi, qr, __builtin_fmaf(Pr, qi, asf(e2.z))); qr = t;
        t = __builtin_fmaf(-Pi, qi, __builtin_fmaf(Pr, qr, asf(e1.x))); qi = __builtin_fmaf(Pi, qr, __builtin_fmaf(Pr, qi, asf(e1.z))); qr = t;
        t = __builtin_fmaf(-Pi, qi, __builtin_fmaf(Pr, qr, asf(e0.x))); qi = __builtin_fmaf(Pi, qr, __builtin_fmaf(Pr, qi, asf(e0.z))); qr = t;
        float gr[4], gi[4];
        gather4(qr, gr); gather4(qi, gi);
        const float P2r = Pr * Pr - Pi * Pi, P2i = 2.0f * Pr * Pi;
        const float P4r = P2r * P2r - P2i * P2i, P4i = 2.0f * P2r * P2i;
        Hr = gr[3]; Hi = gi[3];
#pragma unroll
        for (int q = 2; q >= 0; --q) {
          t = __builtin_fmaf(-P4i, Hi, __builtin_fmaf(P4r, Hr, gr[q])); Hi = __builtin_fmaf(P4i, Hr, __builtin_fmaf(P4r, Hi, gi[q])); Hr = t;
        }
      }
      STAMP(4);
      {
        const float p96r = p64r * p32r - p64i * p32i, p96i = p64r * p32i + p64i * p32r;
        const float fr = g == 0 ? 1.f : g == 1 ? p32r : g == 2 ? p64r : p96r;
        const float fi = g == 0 ? 0.f : g == 1 ? p32i : g == 2 ? p64i : p96i;
        const float c0r = g == 0 ? cr[0] : g == 1 ? cr[1] : g == 2 ? cr[2] : cr[3];
        const float c0i = g == 0 ? ci[0] : g == 1 ? ci[1] : g == 2 ? ci[2] : ci[3];
        hr = __builtin_fmaf(-fi, Hi, __builtin_fmaf(fr, Hr, c0r));
        hi = __builtin_fmaf(fi, Hr, __builtin_fmaf(fr, Hi, c0i));
      }
#pragma unroll
      for (int mt = 0; mt < 8; ++mt)
#pragma unroll
        for (int e = 0; e < 4; ++e) {
          const float nr = __builtin_fmaf(-lbi, hi, __builtin_fmaf(lbr, hr, are[mt][e]));
          const float ni = __builtin_fmaf(lbi, hr, __builtin_fmaf(lbr, hi, aim[mt][e]));
          hr = nr; hi = ni;
          are[mt][e] = __builtin_fmaf(kr, hr, -ki * hi);
          aim[mt][e] = __builtin_fmaf(-kr, hi, -ki * hr);
        }
      {
        LAS char* const tb_ = sm + (w < 6 ? w * 8192 : MISC + (w - 6) * 8192);
        const int swc = ((li & 3) << 1) | ((li >> 2) & 1) | ((li >> 3) << 3);
#pragma unroll
        for (int i = 0; i < 4; ++i) {
          *(LAS u32x4*)(tb_ + li * 256 + (((4 * g + i) ^ swc) << 4)) = pack8(are[2 * i], are[2 * i + 1]);
          *(LAS u32x4*)(tb_ + 4096 + li * 256 + (((4 * g + i) ^ swc) << 4)) = pack8(aim[2 * i], aim[2 * i + 1]);
        }
        asm volatile("s_waitcnt lgkmcnt(0)" ::: "memory");
        const int q4 = li >> 2, p4 = l & 3;
        const int c0 = 8 * (g & 1) + q4, c1 = c0 + 4;
        const int sw0 = ((c0 & 3) << 1) | ((c0 >> 3) << 3), sw1 = sw0 | 1;
#pragma unroll
        for (int part = 0; part < 2; ++part)
#pragma unroll
          for (int u = 0; u < 4; ++u) {
            const int tt = 2 * u + (g >> 1), ch = 2 * tt + (p4 >> 1);
            const bf16x8 v = ldstr(tb_ + part * 4096 + c0 * 256 + ((ch ^ sw0) << 4) + 8 * (p4 & 1),
                                   tb_ + part * 4096 + c1 * 256 + ((ch ^ sw1) << 4) + 8 * (p4 & 1));
            const u32 off = WS_HH + (m * 20 + 10 * part + 2 * a + (w >> 2)) * 16384 + tt * 2048 + ((w >> 1) & 1) * 1024 + ((2 * (w & 1) + (g & 1)) * 16 + li) * 16;
            st128_sc1(__builtin_bit_cast(u32x4, v), off, rws);
          }
      }
      waitbar<0>();
      STAMP(5);
      bar();
      STAMP(6);
      f32x4 ac[4][2];
#pragma unroll
      for (int i = 0; i < 4; ++i) { ac[i][0] = zero4; ac[i][1] = zero4; }
      LAS char* const ab = pin(sm + tq * 4096 + l * 16);
      LAS char* const ab3 = pin(ab + MISC);
      LAS char* cb[2][2];
#pragma unroll
      for (int i = 0; i < 2; ++i) { cb[0][i] = pin(sm + 49152 + i * 49152 + cq * 8192 + rb0); cb[1][i] = pin(sm + 49152 + i * 49152 + cq * 8192 + (rb0 ^ 64)); }
      struct FC { bf16x8 c[4], b0, b1; };
      FC G0, G1;
      auto stepC = [&](auto mf, FC& R, int ks, int s, const FC& M) AINL {
        LAS char* const pa = ((s & 3) == 3 ? ab3 : ab + (s & 3) * 16384) + ks * 1024;
        LAS char* const pc = cb[ks][(s % 6) / 3] + ((s % 6) % 3) * 16384;
        R.b0 = lds128(pa); R.c[0] = lds128(pc); SB();
        if constexpr (mf.v) ac[0][0] = mfma(M.c[0], M.b0, ac[0][0]);
        SB(); R.b1 = lds128(pa + 2048); R.c[1] = lds128(pc + 2048); SB();
        if constexpr (mf.v) ac[0][1] = mfma(M.c[0], M.b1, ac[0][1]);
        SB(); R.c[2] = lds128(pc + 4096); R.c[3] = lds128(pc + 6144); SB();
        if constexpr (mf.v) {
#pragma unroll
          for (int ct = 1; ct < 4; ++ct) { ac[ct][0] = mfma(M.c[ct], M.b0, ac[ct][0]); ac[ct][1] = mfma(M.c[ct], M.b1, ac[ct][1]); }
        }
        SB();
      };
      sfor<0, 20>([&](auto ic) AINL {
        constexpr int s = ic.v;
        bar();
        if constexpr (s == 0) STAMP(17);
        stepC(IC<(s > 0)>{}, G0, 0, s, G1);
        stepC(IC<1>{}, G1, 1, s, G0);
      });
#pragma unroll
      for (int ct = 0; ct < 4; ++ct) { ac[ct][0] = mfma(G1.c[ct], G1.b0, ac[ct][0]); ac[ct][1] = mfma(G1.c[ct], G1.b1, ac[ct][1]); }
      STAMP(7);
#pragma unroll
      for (int cb_ = 0; cb_ < 2; ++cb_)
#pragma unroll
        for (int tt = 0; tt < 2; ++tt)
          st128_sc1(pack8(ac[2 * cb_][tt], ac[2 * cb_ + 1][tt]), WS_SS + ((m * 10 + 2 * a + cq) * 8 + tq + 4 * cb_) * 2048 + tt * 1024 + l * 16, rws);
      waitbar<0>();
      if (w == 0) set_flag(FL_S + m * 5 + a);
      STAMP(8);
    }
  } else if (w < 8) {
    row_stats();
    u32 dummy = 0;
    const u32 lo = tid < 400 ? (m * 400 + tid) * 128 : OOB;
    touch(dummy, lo, mk_rsrc(w2, 640 * 1280));
    touch(dummy, lo, mk_rsrc(c_re, 640 * 1280));
    touch(dummy, lo, mk_rsrc(c_im, 640 * 1280));
    const rsrc_t rw4 = mk_rsrc(w4, 1536 * 1280);
    if (m < 12) {
      const rsrc_t rw3 = mk_rsrc(w3, 640 * 3072);
      float cs[8];
#pragma unroll
      for (int e = 0; e < 8; ++e) cs[e] = 0.f;
      const u32 vo = (tid >> 4) * 3072 + m * 256 + (tid & 15) * 16;
      u32x4 t[20];
#pragma unroll
      for (int i = 0; i < 20; ++i) t[i] = bld128(vo, rw3, i * 32 * 3072);
      SB();
#pragma unroll
      for (int i = 0; i < 20; ++i) {
        cs[0] += bflo(t[i].x); cs[1] += bfhi(t[i].x); cs[2] += bflo(t[i].y); cs[3] += bfhi(t[i].y);
        cs[4] += bflo(t[i].z); cs[5] += bfhi(t[i].z); cs[6] += bflo(t[i].w); cs[7] += bfhi(t[i].w);
      }
#pragma unroll
      for (int e = 0; e < 8; ++e) cs[e] = sum32(sum16(cs[e]));
      if (l < 16) {
        *(LAS f32x4*)(sm + w * 512 + l * 32) = (f32x4){cs[0], cs[1], cs[2], cs[3]};
        *(LAS f32x4*)(sm + w * 512 + l * 32 + 16) = (f32x4){cs[4], cs[5], cs[6], cs[7]};
      }
      bar();
      if (w < 2) {
        float s = 0.f;
#pragma unroll
        for (int i = 0; i < 8; ++i) s += *(LAS float*)(sm + i * 512 + tid * 4);
        st32_sc1(asu(s), WS_CS3 + (m * 128 + tid) * 4, rws);
      }
      waitbar<0>();
      if (w == 0) set_flag(FL_C + m);
    }
    STAMP(1);
    touch(dummy, (m * 960 + tid) * 128, rw4);
    touch(dummy, tid < 448 ? (m * 960 + 512 + tid) * 128 : OOB, rw4);
    asm volatile("s_waitcnt vmcnt(0)" : "+v"(dummy)::"memory");
    STAMP(2);
    if (m == 0) {
#pragma unroll
      for (int i = 0; i < 4; ++i) (void)poll_granule(WS_XS + (i * 512 + tid) * 16, true, rws, ta, tb);
      bar();
      if (tid == 0) st64_sc1((u32x2){ta, tb}, WS_EPOCH, rws);
    }
  }
#endif
}

extern "C" void kernel_launch(void* const* d_in, const int* in_sizes, int n_in, void* d_out, int out_size,
                              void* d_ws, size_t ws_size, hipStream_t stream) {
  (void)in_sizes; (void)n_in; (void)out_size; (void)ws_size;
  s5_mixer_kernel<<<256, 768, 0, stream>>>(d_in[0], d_in[1], d_in[2], d_in[3], d_in[4], d_in[5], d_in[6], d_in[7],
                                           d_in[8], d_in[10], d_in[11], d_in[12], d_in[13], d_out, d_ws);
}
```

```cpp
#include <hip/hip_runtime.h>
#include <hip/hip_bf16.h>
#include <stdint.h>

#define DI __device__ __forceinline__
#define LAS __attribute__((address_space(3)))
#define AINL __attribute__((always_inline))
typedef unsigned u32;
typedef unsigned long long u64;
typedef u32 u32x4 __attribute__((ext_vector_type(4)));
typedef u32 u32x2 __attribute__((ext_vector_type(2)));
typedef float f32x4 __attribute__((ext_vector_type(4)));
typedef float f32x2 __attribute__((ext_vector_type(2)));
typedef __bf16 bf16x8 __attribute__((ext_vector_type(8)));
typedef __bf16 bf16x2 __attribute__((ext_vector_type(2)));
typedef short s16x4 __attribute__((ext_vector_type(4)));
typedef short s16x8 __attribute__((ext_vector_type(8)));

#define XMAP 1
#define ABL 0
#define PSLEEP 2
#define STAMPS 0

constexpr u32 WS_EPOCH = 0;
constexpr u32 WS_FLAG = 0x1000;
constexpr u32 FL_1 = 0, FL_2 = 160, FL_3 = 320, FL_A = 480, FL_S = 560, FL_C = 640;
constexpr u32 WS_XS = 0x20000;
constexpr u32 WS_CS3 = 0x2A000;
constexpr u32 WS_EG = 0x30000;
constexpr u32 WS_ST1 = 0x60000;
constexpr u32 WS_SS = 0xC0000;
constexpr u32 WS_H1 = 0x400000;
constexpr u32 WS_X1 = 0x680000;
constexpr u32 WS_HH = 0x900000;
constexpr u32 WS_H2 = 0xE00000;
constexpr u32 WS_STAMP = 0x1400000;
constexpr u32 WS_BYTES = 0x1500000;
constexpr u32 OOB = 0x7FFFFFF0u;
constexpr float LN_EPS = 1e-5f;
constexpr int SPIN_CAP = 1 << 17;

template <int I> struct IC { static constexpr int v = I; };
template <int B, int E, class F> DI void sfor(F&& f) {
  if constexpr (B < E) { f(IC<B>{}); sfor<B + 1, E>(f); }
}

#if defined(__HIP_DEVICE_COMPILE__) && __HIP_DEVICE_COMPILE__
typedef __amdgpu_buffer_rsrc_t rsrc_t;
DI rsrc_t mk_rsrc(const void* p, u32 bytes) { return __builtin_amdgcn_make_buffer_rsrc((void*)p, 0, bytes, 0x00020000); }

template <int SC1> DI void dma1(u32 lds, u32 voff, rsrc_t r, u32 soff) {
  if (ABL == 2) return;
  if constexpr (SC1)
    asm volatile("s_mov_b32 m0, %0\n\ts_nop 0\n\tbuffer_load_dwordx4 %1, %2, %3 offen sc1 lds" ::"s"(lds), "v"(voff), "s"(r), "s"(soff) : "memory");
  else
    asm volatile("s_mov_b32 m0, %0\n\ts_nop 0\n\tbuffer_load_dwordx4 %1, %2, %3 offen lds" ::"s"(lds), "v"(voff), "s"(r), "s"(soff) : "memory");
}
template <int SC1> DI void dma2(u32 lds, u32 v0, u32 v1, rsrc_t r, u32 soff) {
  if (ABL == 2) return;
  if constexpr (SC1)
    asm volatile("s_mov_b32 m0, %0\n\ts_nop 0\n\tbuffer_load_dwordx4 %1, %3, %4 offen sc1 lds\n\tbuffer_load_dwordx4 %2, %3, %4 offen offset:1024 sc1 lds"
                 ::"s"(lds), "v"(v0), "v"(v1), "s"(r), "s"(soff) : "memory");
  else
    asm volatile("s_mov_b32 m0, %0\n\ts_nop 0\n\tbuffer_load_dwordx4 %1, %3, %4 offen lds\n\tbuffer_load_dwordx4 %2, %3, %4 offen offset:1024 lds"
                 ::"s"(lds), "v"(v0), "v"(v1), "s"(r), "s"(soff) : "memory");
}
template <int SC1> DI void dma4(u32 lds, u32 v, rsrc_t r, u32 soff) {
  if (ABL == 2) return;
  if constexpr (SC1)
    asm volatile("s_mov_b32 m0, %0\n\ts_nop 0\n\tbuffer_load_dwordx4 %1, %2, %3 offen sc1 lds\n\tbuffer_load_dwordx4 %1, %2, %3 offen offset:1024 sc1 lds\n\t"
                 "buffer_load_dwordx4 %1, %2, %3 offen offset:2048 sc1 lds\n\tbuffer_load_dwordx4 %1, %2, %3 offen offset:3072 sc1 lds"
                 ::"s"(lds), "v"(v), "s"(r), "s"(soff) : "memory");
  else
    asm volatile("s_mov_b32 m0, %0\n\ts_nop 0\n\tbuffer_load_dwordx4 %1, %2, %3 offen lds\n\tbuffer_load_dwordx4 %1, %2, %3 offen offset:1024 lds\n\t"
                 "buffer_load_dwordx4 %1, %2, %3 offen offset:2048 lds\n\tbuffer_load_dwordx4 %1, %2, %3 offen offset:3072 lds"
                 ::"s"(lds), "v"(v), "s"(r), "s"(soff) : "memory");
}
constexpr int VC(int n) { return ABL == 2 ? 0 : n; }
template <int N> DI void waitv() { asm volatile("s_waitcnt vmcnt(%0)" ::"i"(VC(N)) : "memory"); }
DI void bar() { asm volatile("s_waitcnt lgkmcnt(0)\n\ts_barrier" ::: "memory"); }
template <int N> DI void waitbar() { asm volatile("s_waitcnt vmcnt(%0) lgkmcnt(0)\n\ts_barrier" ::"i"(VC(N)) : "memory"); }

DI void touch(u32& d, u32 voff, rsrc_t r) { asm volatile("buffer_load_dword %0, %1, %2, 0 offen" : "+v"(d) : "v"(voff), "s"(r) : "memory"); }

DI u32x4 bld128(u32 voff, rsrc_t r, u32 soff) { return __builtin_amdgcn_raw_buffer_load_b128(r, voff, soff, 0); }
DI u32x2 bld64(u32 voff, rsrc_t r, u32 soff) { return __builtin_amdgcn_raw_buffer_load_b64(r, voff, soff, 0); }
DI u32x2 bld64_sc1(u32 voff, rsrc_t r, u32 soff) { return __builtin_amdgcn_raw_buffer_load_b64(r, voff, soff, 16); }
DI u32 bld32(u32 voff, rsrc_t r, u32 soff) { return __builtin_amdgcn_raw_buffer_load_b32(r, voff, soff, 0); }
DI u32 bld16(u32 voff, rsrc_t r) { return __builtin_amdgcn_raw_buffer_load_b16(r, voff, 0, 0); }
DI void st128_sc1(u32x4 v, u32 voff, rsrc_t r) { __builtin_amdgcn_raw_buffer_store_b128(v, r, voff, 0, 16); }
DI void st64_sc1(u32x2 v, u32 voff, rsrc_t r) { __builtin_amdgcn_raw_buffer_store_b64(v, r, voff, 0, 16); }
DI void st32_sc1(u32 v, u32 voff, rsrc_t r) { __builtin_amdgcn_raw_buffer_store_b32(v, r, voff, 0, 16); }

DI void poll_flags(u32 voff, bool active, rsrc_t ws, u32 ta, u32 tb) {
  _Pragma("nounroll") for (int it = 0; it < SPIN_CAP; ++it) {
    u32x2 v;
    asm volatile("buffer_load_dwordx2 %0, %1, %2, 0 offen sc1\n\ts_waitcnt vmcnt(0)" : "=&v"(v) : "v"(voff), "s"(ws) : "memory");
    const bool ok = !active || (v.x == ta && v.y == tb);
    if (__all(ok)) break;
    __builtin_amdgcn_s_sleep(1);
  }
}
DI u32x4 poll_granule(u32 voff, bool active, rsrc_t ws, u32 ta, u32 tb) {
  u32x4 v;
  _Pragma("nounroll") for (int it = 0; it < SPIN_CAP; ++it) {
    asm volatile("buffer_load_dwordx4 %0, %1, %2, 0 offen sc1\n\ts_waitcnt vmcnt(0)" : "=&v"(v) : "v"(voff), "s"(ws) : "memory");
    const bool ok = !active || (v.y == ta && v.w == tb);
    if (__all(ok)) break;
    __builtin_amdgcn_s_sleep(1);
  }
  return v;
}

DI float asf(u32 x) { return __uint_as_float(x); }
DI u32 asu(float x) { return __float_as_uint(x); }
DI float bflo(u32 x) { return asf(x << 16); }
DI float bfhi(u32 x) { return asf(x & 0xffff0000u); }
DI u32 pk2(float a, float b) { f32x2 p = {a, b}; bf16x2 q = __builtin_convertvector(p, bf16x2); return __builtin_bit_cast(u32, q); }
DI float sum16(float x) { const u32 u = asu(x); auto r = __builtin_amdgcn_permlane16_swap(u, u, false, false); const u32 a = r[0], b = r[1]; return asf(a) + asf(b); }
DI float sum32(float x) { const u32 u = asu(x); auto r = __builtin_amdgcn_permlane32_swap(u, u, false, false); const u32 a = r[0], b = r[1]; return asf(a) + asf(b); }
template <int C> DI float dppadd(float x) { return x + asf(__builtin_amdgcn_update_dpp(0u, asu(x), C, 0xf, 0xf, true)); }
DI float wavesum(float x) { x = dppadd<0xB1>(x); x = dppadd<0x4E>(x); x = dppadd<0x141>(x); x = dppadd<0x140>(x); return sum32(sum16(x)); }
DI void gather4(float x, float (&e)[4]) {
  const u32 u = asu(x);
  auto p = __builtin_amdgcn_permlane16_swap(u, u, false, false);
  const u32 a = p[0], b = p[1];
  auto pa = __builtin_amdgcn_permlane32_swap(a, a, false, false);
  auto pb = __builtin_amdgcn_permlane32_swap(b, b, false, false);
  const u32 e0 = pa[0], e2 = pa[1], e1 = pb[0], e3 = pb[1];
  e[0] = asf(e0); e[1] = asf(e1); e[2] = asf(e2); e[3] = asf(e3);
}
DI float gelu_f(float v) {
  const float c1 = -2.302208198f, c2 = -0.1029432397f;
  const float e = __builtin_amdgcn_exp2f(v * __builtin_fmaf(v * v, c2, c1));
  return v * __builtin_amdgcn_rcpf(1.0f + e);
}
DI float silu_f(float z) { return z * __builtin_amdgcn_rcpf(1.0f + __builtin_amdgcn_exp2f(z * -1.4426950409f)); }
DI f32x4 mfma(bf16x8 a, bf16x8 b, f32x4 c) { if (ABL == 1 || ABL == 4) { asm volatile("" : "+v"(c) : "v"(a), "v"(b)); return c; } return __builtin_amdgcn_mfma_f32_16x16x32_bf16(a, b, c, 0, 0, 0); }
DI bf16x8 lds128(LAS char* p) { if (ABL == 1 || ABL == 3) { bf16x8 r; asm volatile("" : "=v"(r)); return r; } return *(LAS bf16x8*)p; }
DI bf16x8 ldstr(LAS char* p0, LAS char* p1) {
  if (ABL == 1 || ABL == 3) { bf16x8 r; asm volatile("" : "=v"(r)); return r; }
  const s16x4 a = __builtin_amdgcn_ds_read_tr16_b64_v4i16((LAS s16x4*)p0);
  const s16x4 b = __builtin_amdgcn_ds_read_tr16_b64_v4i16((LAS s16x4*)p1);
  const s16x8 w = __builtin_shufflevector(a, b, 0, 1, 2, 3, 4, 5, 6, 7);
  return __builtin_bit_cast(bf16x8, w);
}
DI u32x4 pack8(f32x4 a, f32x4 b) { u32x4 r; r.x = pk2(a.x, a.y); r.y = pk2(a.z, a.w); r.z = pk2(b.x, b.y); r.w = pk2(b.z, b.w); return r; }
DI u32x2 pack4(f32x4 a) { u32x2 r; r.x = pk2(a.x, a.y); r.y = pk2(a.z, a.w); return r; }
DI int wrapk(int k, int n) { return k >= n ? k - n : k; }
#define SB() __builtin_amdgcn_sched_barrier(0)
DI f32x4 keep(f32x4 v) { asm volatile("" : "+v"(v)); return v; }
DI LAS char* pin(LAS char* p) { asm volatile("" : "+v"(p)); return p; }
#endif

#define STAMP(i) do { } while (0)
#define CSTAMP(i) do { } while (0)

#define ROT 0
__global__ __launch_bounds__(768, 3) void s5_mixer_kernel(
    const void* __restrict__ x, const void* __restrict__ lam_re, const void* __restrict__ lam_im,
    const void* __restrict__ b_re, const void* __restrict__ b_im, const void* __restrict__ c_re,
    const void* __restrict__ c_im, const void* __restrict__ log_dt, const void* __restrict__ dskip,
    const void* __restrict__ w1, const void* __restrict__ w2, const void* __restrict__ w3,
    const void* __restrict__ w4, void* __restrict__ out, void* __restrict__ ws) {
#if defined(__HIP_DEVICE_COMPILE__) && __HIP_DEVICE_COMPILE__
  __shared__ __attribute__((aligned(16))) char smem_[163840];
  LAS char* const sm = (LAS char*)smem_;
  const u32 L0 = (u32)(size_t)sm;
  const int tid = threadIdx.x, l = tid & 63;
  const int w = __builtin_amdgcn_readfirstlane(tid >> 6);
  const int bid = blockIdx.x, xcd = bid & 7, rr = bid >> 3;
  const int m = (xcd >> 1) * 4 + (rr < 20 ? rr / 5 : (rr - 20) / 3);
  const int role = rr < 20 ? rr % 5 + 5 * (xcd & 1) : 10 + (rr - 20) % 3 + 3 * (xcd & 1);
  const int g = l >> 4, li = l & 15, r8 = l >> 3, tq = w & 3, cq = w >> 2, L = w - 8;
  const rsrc_t rws = mk_rsrc(ws, WS_BYTES);
  const rsrc_t rx = mk_rsrc(x, 2048 * 1280);
  const u64 pt0 = __builtin_amdgcn_s_memrealtime();
  const int fm = (5 * (m & 3) + 1) >> 1;
  const int rot1 = (2 * (role % 5) + fm) % 10, rotA1 = (2 * (role % 3) + 1 + fm) % 10;
  const int rot10 = 0, rot24 = 0, rotA3 = 0;

  u32 ta, tb;
  auto row_stats = [&]() AINL {
    const int stok = m * 128 + role * 8 + w;
    const u32x2 ep = bld64_sc1(0, rws, 0);
    const u32x4 ra = bld128(l * 16, rx, stok * 1280);
    const u32 rb_ = bld32(1024 + l * 4, rx, stok * 1280);
    const u64 e = (((u64)__builtin_amdgcn_readfirstlane(ep.y) << 32) | (u32)__builtin_amdgcn_readfirstlane(ep.x)) + 0x9E3779B97F4A7C15ull;
    ta = (u32)e; tb = (u32)(e >> 32);
    float v[10] = {bflo(ra.x), bfhi(ra.x), bflo(ra.y), bfhi(ra.y), bflo(ra.z), bfhi(ra.z), bflo(ra.w), bfhi(ra.w), bflo(rb_), bfhi(rb_)};
    float s = 0.f;
#pragma unroll
    for (int i = 0; i < 10; ++i) s += v[i];
    const float mu = wavesum(s) * (1.0f / 640.0f);
    float q = 0.f;
#pragma unroll
    for (int i = 0; i < 10; ++i) { const float d = v[i] - mu; q = __builtin_fmaf(d, d, q); }
    const float rs = 1.0f / sqrtf(wavesum(q) * (1.0f / 640.0f) + LN_EPS);
    u32x4 gr = {asu(mu), ta, asu(rs), tb};
    st128_sc1(gr, l == 0 ? WS_XS + stok * 16 : OOB, rws);
  };
  auto put_tag = [&](u32 off) AINL { if (tid == 0) *(LAS u32x2*)(sm + off) = (u32x2){ta, tb}; };
  auto get_tag = [&](u32 off) AINL {
    const u32x2 t = *(volatile LAS u32x2*)(sm + off);
    ta = __builtin_amdgcn_readfirstlane(t.x); tb = __builtin_amdgcn_readfirstlane(t.y);
  };
  auto set_flag = [&](u32 idx) AINL { st64_sc1((u32x2){ta, tb}, l == 0 ? WS_FLAG + idx * 128 : OOB, rws); };
  auto await = [&](u32 own, int i, bool act, auto&& first3) AINL {
    bar();
    if (L == 0) { set_flag(own); poll_flags(WS_FLAG + i * 128, act, rws, ta, tb); }
    bar();
    first3();
  };

  const u32 rb0 = li * 128 + ((g ^ (li >> 1)) << 4);
  const bf16x8 ones = __builtin_bit_cast(bf16x8, (u32x4){0x3F803F80u, 0x3F803F80u, 0x3F803F80u, 0x3F803F80u});
  const f32x4 zero4 = {0.f, 0.f, 0.f, 0.f};

  if (role < 10) {
    const int j = role;
    constexpr u32 ACT = 0, W = 65536;
    if (w >= 8) {
      const rsrc_t rw1 = mk_rsrc(w1, 640 * 1280), rw2 = mk_rsrc(w2, 640 * 1280);
      const rsrc_t rw3 = mk_rsrc(w3, 640 * 3072), rw4 = mk_rsrc(w4, 1536 * 1280);
      const u32 vx0 = (m * 128 + L * 32 + r8) * 1280 + (((l & 7) ^ g) << 4);
      const u32 vx1 = (m * 128 + L * 32 + 8 + r8) * 1280 + (((l & 7) ^ (4 + g)) << 4) - 1024;
      const u32 wch = j * 128 + (((l & 7) ^ (2 * g)) << 4);
      const u32 vq0 = ((L >> 1) * 32 + (r8 >> 2) * 8 + (L & 1) * 4 + (r8 & 3)) * 1280 + wch, vq1 = vq0 + 16 * 1280 - 1024;
      const u32 ldsA = L0 + ACT + L * 4096, ldsW = L0 + W + L * 2048;
      auto issue_1 = [&](int s) AINL {
        const int kt = wrapk(s + rot1, 10);
        dma2<0>(ldsA + (s & 3) * 16384, vx0, vx1, rx, kt * 128);
        dma2<0>(ldsW + 65536 + (s & 3) * 8192, vq0, vq1, rw1, kt * 81920);
        dma2<0>(ldsA + (s & 3) * 16384 + 2048, vx0, vx1, rx, kt * 128 + 20480);
      };
      issue_1(0); issue_1(1); issue_1(2);
      const u32 vw0 = (L * 16 + r8) * 1280 + wch, vw1 = vw0 + 10240 - 1024;
      const u32 va = L * 4096 + l * 16, vl = L * 1024 + l * 16;
      u32 v3[5];
#pragma unroll
      for (int i = 0; i < 5; ++i) {
        const int c = 64 * i + l, r = c / 20, pos = c - 20 * r;
        v3[i] = (L * 16 + r) * 3072 + j * 320 + ((pos ^ (2 * ((r >> 2) & 1))) << 4) - 1024 * (i & 1);
      }
      auto issue_w2 = [&](int s) AINL { dma2<0>(ldsW + s * 8192, vw0, vw1, rw2, wrapk(s + rot10, 10) * 81920); };
      auto issue_w3 = [&](int s) AINL {
        const u32 d = L0 + W + (s & 3) * 20480 + L * 5120, so = wrapk(s + rot10, 10) * 196608;
        dma2<0>(d, v3[0], v3[1], rw3, so); dma2<0>(d + 2048, v3[2], v3[3], rw3, so); dma1<0>(d + 4096, v3[4], rw3, so);
      };
      auto issue_w4 = [&](int s) AINL { dma2<0>(ldsW + ((s + 5) % 12) * 8192, vw0, vw1, rw4, wrapk(s + rot24, 24) * 81920); };
      auto issue_a = [&](int s, u32 base) AINL { dma4<1>(ldsA + (s & 3) * 16384, va, rws, base); };
      sfor<0, 10>([&](auto ic) AINL {
        constexpr int s = ic.v;
        constexpr int gs[13] = {6, 6, 6, 6, 6, 6, 6, 6, 6, 6, 5, 6, 6};
        waitbar<gs[s + 1] + gs[s + 2]>();
        if constexpr (s + 3 < 10) issue_1(s + 3);
        else if constexpr (s == 7) { dma1<1>(L0 + ACT + 2 * 16384 + L * 1024, vl, rws, WS_XS + m * 2048); issue_w2(0); issue_w2(1); }
        else { issue_w2(3 * s - 22); issue_w2(3 * s - 21); issue_w2(3 * s - 20); }
        if constexpr (s == 0) get_tag(W);
      });
      waitbar<16>();
      issue_w2(8); issue_w2(9);
      await(FL_1 + m * 10 + j, FL_1 + m * 10 + (l < 10 ? l : l < 16 ? -10 + (l - 10) + (l >= 13 ? 2 : 0) : 12 + (l - 16) + (l >= 19 ? 2 : 0)),
            l < 10 || (l < 16 && m > 0) || (l >= 16 && l < 22 && m < 15), [&]() AINL {
        sfor<0, 3>([&](auto ic) AINL { constexpr int s = ic.v; issue_a(s, WS_H1 + (m * 10 + wrapk(s + rot10, 10)) * 16384); });
      });
      sfor<0, 10>([&](auto ic) AINL {
        constexpr int s = ic.v;
        constexpr int gs[13] = {4, 4, 4, 4, 4, 4, 4, 4, 4, 4, 5, 5, 5};
        waitbar<gs[s + 1] + gs[s + 2]>();
        if constexpr (s + 3 < 10) issue_a(s + 3, WS_H1 + (m * 10 + wrapk(s + 3 + rot10, 10)) * 16384);
        else issue_w3(s - 7);
      });
      bar();
      issue_w3(3);
      await(FL_2 + m * 10 + j, l < 10 ? FL_2 + m * 10 + l : FL_C + ((5 * j) >> 2) + l - 10, l < (j < 9 ? 12 : 11), [&]() AINL {
        sfor<0, 3>([&](auto ic) AINL { constexpr int s = ic.v; issue_a(s, WS_X1 + (m * 10 + wrapk(s + rot10, 10)) * 16384); });
      });
      sfor<0, 10>([&](auto ic) AINL {
        constexpr int s = ic.v;
        constexpr int gs[13] = {4, 4, 4, 4, 9, 9, 9, 9, 9, 9, 8, 8, 8};
        waitbar<gs[s + 1] + gs[s + 2]>();
        if constexpr (s + 3 < 10) {
          issue_a(s + 3, WS_X1 + (m * 10 + wrapk(s + 3 + rot10, 10)) * 16384);
          if constexpr (s >= 1) issue_w3(s + 3);
        } else if constexpr (s == 7) {
          issue_a(2, WS_ST1 + m * 20480);
          issue_w4(0); issue_w4(1);
        } else if constexpr (s == 8) {
          dma1<1>(L0 + ACT + 3 * 16384 + L * 1024, vl, rws, WS_ST1 + m * 20480 + 16384);
          dma1<1>(L0 + ACT + 3 * 16384 + 4096 + L * 1024, vl, rws, WS_CS3 + j * 640);
          issue_w4(2); issue_w4(3); issue_w4(4);
        } else { issue_w4(5); issue_w4(6); issue_w4(7); issue_w4(8); }
      });
      waitbar<14>();
      issue_w4(9); issue_w4(10); issue_w4(11);
      await(FL_3 + m * 10 + j, FL_3 + m * 10 + l, l < 10, [&]() AINL {
        sfor<0, 3>([&](auto ic) AINL { constexpr int s = ic.v; issue_a(s, WS_H2 + (m * 24 + wrapk(s + rot24, 24)) * 16384); });
      });
      u32x2 fs;
      const u32 fso = WS_FLAG + (FL_S + m * 5 + (j >> 1)) * 128;
      sfor<0, 24>([&](auto ic) AINL {
        constexpr int s = ic.v;
        constexpr int gs[27] = {4, 4, 4, 4, 6, 6, 6, 6, 6, 6, 6, 6, 6, 6, 6, 6, 4, 4, 4, 4, 5, 4, 4, 4, 4, 0, 0};
        if constexpr (s == 20) { asm volatile("s_waitcnt vmcnt(%1) lgkmcnt(0)\n\ts_barrier" : "+v"(fs) : "i"(VC(gs[s + 1] + gs[s + 2])) : "memory"); }
        else waitbar<gs[s + 1] + gs[s + 2]>();
        if constexpr (s == 17) asm volatile("buffer_load_dwordx2 %0, %1, %2, 0 offen sc1" : "=v"(fs) : "v"(fso), "s"(rws) : "memory");
        if constexpr (s + 3 < 24) issue_a(s + 3, WS_H2 + (m * 24 + wrapk(s + 3 + rot24, 24)) * 16384);
        if constexpr (s >= 1 && s <= 12) issue_w4(s + 11);
        if constexpr (s == 21) {
          if (!__all(fs.x == ta && fs.y == tb)) poll_flags(fso, true, rws, ta, tb);
          issue_a(0, WS_SS + (m * 10 + j) * 16384);
        }
      });
      waitbar<0>();
    } else {
      row_stats();
      put_tag(W);
      const int q4 = li >> 2, p4 = l & 3;
      LAS char* const wb0 = pin(sm + W + (4 * g + q4) * 128 + ((((2 * cq) ^ ((2 * g + (q4 >> 1)) & 3))) << 5) + 8 * p4);
      LAS char* const wb1 = pin(sm + W + (4 * g + q4) * 128 + ((((2 * cq + 1) ^ ((2 * g + (q4 >> 1)) & 3))) << 5) + 8 * p4);
      LAS char* const wh0 = pin(wb0 + 65536);
      LAS char* const wh1 = pin(wb1 + 65536);
      LAS char* const ab = pin(sm + ACT + tq * 4096 + l * 16);
      struct F64 { bf16x8 a0, a1, b0, b1; };
      F64 F0, F1;
      f32x4 acc[2][2], cs[2];
      u32x2 xs[2][2];
      auto step64 = [&](auto mf, auto cs_on, F64& R, LAS char* pb, u32 wo, const F64& M) AINL {
        LAS char* const p0 = wo >= 65536 ? wh0 + (wo - 65536) : wb0 + wo;
        LAS char* const p1 = wo >= 65536 ? wh1 + (wo - 65536) : wb1 + wo;
        R.a0 = ldstr(p0, p0 + 2048); SB();
        if constexpr (mf.v) acc[0][0] = mfma(M.a0, M.b0, acc[0][0]);
        SB(); R.b0 = lds128(pb); R.b1 = lds128(pb + 2048); SB();
        if constexpr (mf.v) acc[1][0] = mfma(M.a1, M.b0, acc[1][0]);
        SB(); R.a1 = ldstr(p1, p1 + 2048); SB();
        if constexpr (mf.v) { acc[0][1] = mfma(M.a0, M.b1, acc[0][1]); acc[1][1] = mfma(M.a1, M.b1, acc[1][1]); }
        if constexpr (mf.v && cs_on.v) { cs[0] = mfma(M.a0, ones, keep(cs[0])); cs[1] = mfma(M.a1, ones, keep(cs[1])); }
        SB();
      };
      auto mm = [&](const F64& M) AINL {
        acc[0][0] = mfma(M.a0, M.b0, acc[0][0]); acc[1][0] = mfma(M.a1, M.b0, acc[1][0]);
        acc[0][1] = mfma(M.a0, M.b1, acc[0][1]); acc[1][1] = mfma(M.a1, M.b1, acc[1][1]);
      };
      float mu[2], rs[2];
      {
        LAS char* const xb0 = pin(sm + ACT + tq * 4096 + rb0);
        LAS char* const xb1 = pin(sm + ACT + tq * 4096 + (rb0 ^ 64));
        const int s_own = wrapk(j - rot1 + 10, 10);
#pragma unroll
        for (int a = 0; a < 2; ++a) { cs[a] = zero4; acc[a][0] = zero4; acc[a][1] = zero4; }
        STAMP(1);
        sfor<0, 10>([&](auto ic) AINL {
          constexpr int s = ic.v;
          constexpr u32 so = (s & 3) * 16384, wo = 65536 + (s & 3) * 8192;
          bar();
          if constexpr (s == 0) STAMP(16);
          step64(IC<(s > 0)>{}, IC<0>{}, F0, xb0 + so, wo, F1);
          if (s > 0 && tq == ((2 * s - 1) & 3)) { cs[0] = mfma(F1.a0, ones, keep(cs[0])); cs[1] = mfma(F1.a1, ones, keep(cs[1])); }
          step64(IC<1>{}, IC<0>{}, F1, xb1 + so, wo + 4096, F0);
          if (tq == ((2 * s) & 3)) { cs[0] = mfma(F0.a0, ones, keep(cs[0])); cs[1] = mfma(F0.a1, ones, keep(cs[1])); }
          if (s == s_own) {
#pragma unroll
            for (int tt = 0; tt < 2; ++tt)
#pragma unroll
              for (int ct = 0; ct < 2; ++ct)
                xs[ct][tt] = *(LAS u32x2*)(sm + ACT + so + tq * 4096 + tt * 2048 + li * 128 + (((4 * cq + 2 * ct + (g >> 1)) ^ (li >> 1)) << 4) + 8 * (g & 1));
          }
        });
        mm(F1);
        if (tq == 3) { cs[0] = mfma(F1.a0, ones, cs[0]); cs[1] = mfma(F1.a1, ones, cs[1]); }
        if (li == 0) { *(LAS f32x4*)(sm + ACT + 2 * 16384 + 8192 + w * 128 + g * 16) = cs[0]; *(LAS f32x4*)(sm + ACT + 2 * 16384 + 8192 + w * 128 + 64 + g * 16) = cs[1]; }
        bar();
        STAMP(2);
#pragma unroll
        for (int ct = 0; ct < 2; ++ct) {
          LAS char* const pc = sm + ACT + 2 * 16384 + 8192 + cq * 512 + ct * 64 + g * 16;
          cs[ct] = (*(LAS f32x4*)pc + *(LAS f32x4*)(pc + 128)) + (*(LAS f32x4*)(pc + 256) + *(LAS f32x4*)(pc + 384));
        }
        u32x4 g0 = *(LAS u32x4*)(sm + ACT + 2 * 16384 + (tq * 32 + li) * 16);
        u32x4 g1 = *(LAS u32x4*)(sm + ACT + 2 * 16384 + (tq * 32 + 16 + li) * 16);
        if (!__all(g0.y == ta && g0.w == tb && g1.y == ta && g1.w == tb)) {
          g0 = poll_granule(WS_XS + (m * 128 + tq * 32 + li) * 16, true, rws, ta, tb);
          g1 = poll_granule(WS_XS + (m * 128 + tq * 32 + 16 + li) * 16, true, rws, ta, tb);
        }
        mu[0] = asf(g0.x); rs[0] = asf(g0.z); mu[1] = asf(g1.x); rs[1] = asf(g1.z);
        const int blk = 2 * j + cq, grp = blk / 5;
        const int shift = (grp & 1) ? -(1 + (grp >> 1)) : (1 + (grp >> 1));
        if ((m == 0 && tq == 0 && shift > 0) || (m == 15 && tq == 3 && shift < 0)) {
          const int t = shift > 0 ? li : 2032 + li;
          const bool z = shift > 0 ? t < shift : t >= 2048 + shift;
          const u32 off = WS_H1 + ((t >> 7) * 10 + j) * 16384 + ((t >> 4) & 7) * 2048 + cq * 1024 + l * 16;
          st128_sc1((u32x4){0u, 0u, 0u, 0u}, z ? off : OOB, rws);
        }
#pragma unroll
        for (int tt = 0; tt < 2; ++tt) {
          const float r2 = rs[tt] * __builtin_amdgcn_rsqf(1.0f - LN_EPS * rs[tt] * rs[tt] + LN_EPS);
          f32x4 h[2];
#pragma unroll
          for (int ct = 0; ct < 2; ++ct)
#pragma unroll
            for (int e = 0; e < 4; ++e) h[ct][e] = gelu_f(r2 * __builtin_fmaf(-mu[tt], cs[ct][e], acc[ct][tt][e]));
          const int t = m * 128 + tq * 32 + tt * 16 + li, tp = t + shift;
          const u32 off = WS_H1 + ((tp >> 7) * 10 + j) * 16384 + ((tp >> 4) & 7) * 2048 + cq * 1024 + (g * 16 + (tp & 15)) * 16;
          st128_sc1(pack8(h[0], h[1]), (u32)tp < 2048u ? off : OOB, rws);
          SB();
        }
      }
      STAMP(3);
      waitbar<0>();
      STAMP(4);
      bar();
      STAMP(5);
#pragma unroll
      for (int a = 0; a < 2; ++a) { acc[a][0] = zero4; acc[a][1] = zero4; }
      sfor<0, 10>([&](auto ic) AINL {
        constexpr int s = ic.v;
        bar();
        if constexpr (s == 0) STAMP(17);
        step64(IC<(s > 0)>{}, IC<0>{}, F0, ab + (s & 3) * 16384, s * 8192, F1);
        step64(IC<1>{}, IC<0>{}, F1, ab + (s & 3) * 16384 + 1024, s * 8192 + 4096, F0);
      });
      bar();
      mm(F1);
      STAMP(6);
      u32x4 x1p[2];
      {
        float sm1[2], sq1[2];
#pragma unroll
        for (int tt = 0; tt < 2; ++tt) {
          float s1 = 0.f, s2 = 0.f;
          f32x4 x1[2];
#pragma unroll
          for (int ct = 0; ct < 2; ++ct) {
            const float xv[4] = {bflo(xs[ct][tt].x), bfhi(xs[ct][tt].x), bflo(xs[ct][tt].y), bfhi(xs[ct][tt].y)};
#pragma unroll
            for (int e = 0; e < 4; ++e) {
              const float v = __builtin_fmaf(xv[e] - mu[tt], rs[tt], acc[ct][tt][e]);
              x1[ct][e] = v; s1 += v; s2 = __builtin_fmaf(v, v, s2);
            }
          }
          x1p[tt] = pack8(x1[0], x1[1]);
          st128_sc1(x1p[tt], WS_X1 + (m * 10 + j) * 16384 + (2 * tq + tt) * 2048 + cq * 1024 + l * 16, rws);
          sm1[tt] = sum32(sum16(s1)); sq1[tt] = sum32(sum16(s2));
        }
        const u32x2 st = {asu(g & 1 ? sm1[1] : sm1[0]), asu(g & 1 ? sq1[1] : sq1[0])};
        st64_sc1(st, l < 32 ? WS_ST1 + m * 20480 + (2 * j + cq) * 1024 + (tq * 32 + l) * 8 : OOB, rws);
      }
      STAMP(7);
      waitbar<0>();
      STAMP(8);
      bar();
      STAMP(9);
      {
        const int sg = (g & 1) * 32;
        LAS char* const bA = pin(sm + W + (4 * g + q4) * 320 + 8 * p4 + 160 * cq + (cq ? -sg : sg));
        LAS char* const bB = pin(sm + W + (4 * g + q4) * 320 + 8 * p4 + 160 * cq + (cq ? sg : -sg));
        LAS char* const bA2 = pin(bA + 40960);
        LAS char* const bB2 = pin(bB + 40960);
        struct F3 { bf16x8 a[5], b0, b1; };
        F3 G0, G1;
        f32x4 a3[5][2];
        auto step3 = [&](auto mf, F3& R, u32 so, u32 wo, const F3& M) AINL {
          LAS char* const pA = wo >= 40960 ? bA2 + (wo - 40960) : bA + wo;
          LAS char* const pB = wo >= 40960 ? bB2 + (wo - 40960) : bB + wo;
          R.a[0] = ldstr(pA, pA + 5120); SB();
          if constexpr (mf.v) a3[0][0] = mfma(M.a[0], M.b0, a3[0][0]);
          SB(); R.b0 = lds128(ab + so); R.b1 = lds128(ab + so + 2048); SB();
          if constexpr (mf.v) a3[0][1] = mfma(M.a[0], M.b1, a3[0][1]);
          SB();
          sfor<1, 5>([&](auto ii) AINL {
            constexpr int i = ii.v;
            LAS char* const pw = ((i & 1) ? pB : pA) + 32 * i;
            R.a[i] = ldstr(pw, pw + 5120); SB();
            if constexpr (mf.v) a3[(i + 1) >> 1][(i + 1) & 1] = mfma(M.a[(i + 1) >> 1], ((i + 1) & 1) ? M.b1 : M.b0, a3[(i + 1) >> 1][(i + 1) & 1]);
            SB();
          });
          if constexpr (mf.v) {
            a3[3][0] = mfma(M.a[3], M.b0, a3[3][0]); a3[3][1] = mfma(M.a[3], M.b1, a3[3][1]);
            a3[4][0] = mfma(M.a[4], M.b0, a3[4][0]); a3[4][1] = mfma(M.a[4], M.b1, a3[4][1]);
          }
          SB();
        };
#pragma unroll
        for (int i = 0; i < 5; ++i) { a3[i][0] = zero4; a3[i][1] = zero4; }
        sfor<0, 10>([&](auto ic) AINL {
          constexpr int s = ic.v;
          constexpr u32 so = (s & 3) * 16384, wo = (s & 3) * 20480;
          bar();
          if constexpr (s == 0) STAMP(18);
          step3(IC<(s > 0)>{}, G0, so, wo, G1);
          step3(IC<1>{}, G1, so + 1024, wo + 10240, G0);
        });
#pragma unroll
        for (int i = 0; i < 5; ++i) { a3[i][0] = mfma(G1.a[i], G1.b0, a3[i][0]); a3[i][1] = mfma(G1.a[i], G1.b1, a3[i][1]); }
        bar();
        STAMP(10);
        float m1[2], r1[2];
#pragma unroll
        for (int tt = 0; tt < 2; ++tt) {
          float s1 = 0.f, s2 = 0.f;
#pragma unroll
          for (int i = 0; i < 5; ++i) {
            const f32x2 v = *(LAS f32x2*)(sm + ACT + 2 * 16384 + (g + 4 * i) * 1024 + (tq * 32 + tt * 16 + li) * 8);
            s1 += v.x; s2 += v.y;
          }
          s1 = sum32(sum16(s1)) * (1.0f / 640.0f); s2 = sum32(sum16(s2)) * (1.0f / 640.0f);
          m1[tt] = s1; r1[tt] = __builtin_amdgcn_rsqf(fmaxf(__builtin_fmaf(-s1, s1, s2), 0.f) + LN_EPS);
        }
        f32x4 c3[5];
#pragma unroll
        for (int i = 0; i < 5; ++i) c3[i] = *(LAS f32x4*)(sm + ACT + 3 * 16384 + 4096 + (80 * cq + 16 * i + 4 * g) * 4);
#pragma unroll
        for (int tt = 0; tt < 2; ++tt) {
          u32x2 pk[5];
#pragma unroll
          for (int i = 0; i < 5; ++i) {
            f32x4 h;
#pragma unroll
            for (int e = 0; e < 4; ++e) h[e] = gelu_f(r1[tt] * __builtin_fmaf(-m1[tt], c3[i][e], a3[i][tt][e]));
            pk[i] = pack4(h);
          }
          const u32 base = WS_H2 + m * 24 * 16384 + (2 * tq + tt) * 2048 + l * 16;
          auto boff = [&](int b) AINL { return b < 48 ? base + (b >> 1) * 16384 + (b & 1) * 1024 : OOB; };
          if (cq == 0) {
            st128_sc1((u32x4){pk[0].x, pk[0].y, pk[1].x, pk[1].y}, boff(5 * j), rws);
            st128_sc1((u32x4){pk[2].x, pk[2].y, pk[3].x, pk[3].y}, boff(5 * j + 1), rws);
            st64_sc1(pk[4], boff(5 * j + 2), rws);
          } else {
            st64_sc1(pk[0], boff(5 * j + 2) + 8, rws);
            st128_sc1((u32x4){pk[1].x, pk[1].y, pk[2].x, pk[2].y}, boff(5 * j + 3), rws);
            st128_sc1((u32x4){pk[3].x, pk[3].y, pk[4].x, pk[4].y}, boff(5 * j + 4), rws);
          }
          SB();
        }
      }
      STAMP(11);
      waitbar<0>();
      STAMP(12);
      const rsrc_t rdk = mk_rsrc(dskip, 1280);
      const u32x2 dk0 = bld64((cq * 32 + 4 * g) * 2, rdk, j * 128), dk1 = bld64((cq * 32 + 16 + 4 * g) * 2, rdk, j * 128);
      bar();
      STAMP(13);
#pragma unroll
      for (int a = 0; a < 2; ++a) { acc[a][0] = zero4; acc[a][1] = zero4; }
      sfor<0, 24>([&](auto ic) AINL {
        constexpr int s = ic.v;
        bar();
        if constexpr (s == 0) STAMP(19);
        step64(IC<(s > 0)>{}, IC<0>{}, F0, ab + (s & 3) * 16384, ((s + 5) % 12) * 8192, F1);
        step64(IC<1>{}, IC<0>{}, F1, ab + (s & 3) * 16384 + 1024, ((s + 5) % 12) * 8192 + 4096, F0);
      });
      mm(F1);
      bar();
      STAMP(14);
      const rsrc_t rout = mk_rsrc(out, 2048 * 2560);
#pragma unroll
      for (int tt = 0; tt < 2; ++tt) {
        const u32x4 sv = *(LAS u32x4*)(sm + ACT + w * 2048 + tt * 1024 + l * 16);
#pragma unroll
        for (int ct = 0; ct < 2; ++ct) {
          const float xv[4] = {bflo(xs[ct][tt].x), bfhi(xs[ct][tt].x), bflo(xs[ct][tt].y), bfhi(xs[ct][tt].y)};
          const u32 s0 = ct ? sv.z : sv.x, s1 = ct ? sv.w : sv.y;
          const float sy[4] = {bflo(s0), bfhi(s0), bflo(s1), bfhi(s1)};
          const u32 y0 = ct ? x1p[tt].z : x1p[tt].x, y1 = ct ? x1p[tt].w : x1p[tt].y;
          const float x1[4] = {bflo(y0), bfhi(y0), bflo(y1), bfhi(y1)};
          const u32x2 dk = ct ? dk1 : dk0;
          const float dv[4] = {bflo(dk.x), bfhi(dk.x), bflo(dk.y), bfhi(dk.y)};
          u32x4 o;
#pragma unroll
          for (int e = 0; e < 4; ++e) {
            const float sfull = __builtin_fmaf((xv[e] - mu[tt]) * rs[tt], dv[e], sy[e]);
            o[e] = asu(__builtin_fmaf(sfull, silu_f(x1[e] + acc[ct][tt][e]), xv[e]));
          }
          __builtin_amdgcn_raw_buffer_store_b128(o, rout, (m * 128 + tq * 32 + tt * 16 + li) * 2560 + (j * 64 + cq * 32 + ct * 16 + 4 * g) * 4, 0, 0);
        }
      }
      STAMP(15); CSTAMP(31);
      if (blockIdx.x == 59 && w == 0) { const u32x2 v = bld64_sc1(WS_STAMP, rws, 0); const u64 e = __builtin_amdgcn_s_memrealtime() + 300 + 0 * (u64)__builtin_amdgcn_readfirstlane((u32)pt0 - v.x); while (__builtin_amdgcn_s_memrealtime() < e) __builtin_amdgcn_s_sleep(2); }
    }
  } else if (role < 15) {
    const int a = role - 10;
    constexpr u32 MISC = 147456, CST = 157696, TAG = 159744;
    if (w >= 8) {
      const rsrc_t rbr = mk_rsrc(b_re, 640 * 1280), rbi = mk_rsrc(b_im, 640 * 1280);
      const rsrc_t rcr = mk_rsrc(c_re, 640 * 1280), rci = mk_rsrc(c_im, 640 * 1280);
      const u32 vx0 = (m * 128 + 32 * (r8 >> 2) + 8 * L + (r8 & 3)) * 1280 + (((l & 7) ^ g) << 4);
      const u32 vx1 = (m * 128 + 32 * (2 + (r8 >> 2)) + 8 * L + (r8 & 3)) * 1280 + (((l & 7) ^ (4 + g)) << 4) - 1024;
      const u32 vb0 = (a * 128 + L * 32 + r8) * 1280 + (((l & 7) ^ g) << 4);
      const u32 vb1 = (a * 128 + L * 32 + 8 + r8) * 1280 + (((l & 7) ^ (4 + g)) << 4) - 1024;
      const u32 ldsw = L0 + L * 4096;
      auto issue_nk = [&](u32 d, rsrc_t r, u32 so) AINL { dma2<0>(d, vb0, vb1, r, so); dma2<0>(d + 2048, vb0, vb1, r, so + 20480); };
      auto issue1 = [&](int s) AINL {
        const int kt = wrapk(s + rotA1, 10);
        const u32 d = ldsw + (s % 3) * 49152;
        dma2<0>(d, vx0, vx1, rx, kt * 128); dma2<0>(d + 2048, vx0, vx1, rx, kt * 128 + 5120);
        issue_nk(d + 16384, rbr, kt * 128); issue_nk(d + 32768, rbi, kt * 128);
      };
      auto issue_c = [&](int s) AINL {
        const int kt = wrapk(s + rotA3, 20);
        issue_nk(ldsw + 49152 + (s % 6) * 16384, kt < 10 ? rcr : rci, wrapk(kt, 10) * 128);
      };
      const u32 va = L * 4096 + l * 16;
      auto issue_h = [&](int s) AINL {
        dma4<1>(ldsw + ((s & 3) == 3 ? MISC : (s & 3) * 16384), va, rws, WS_HH + (m * 20 + wrapk(s + rotA3, 20)) * 16384);
      };
      issue1(0); issue1(1);
      sfor<0, 10>([&](auto ic) AINL {
        constexpr int s = ic.v;
        waitbar<(s == 9 ? 13 : 12)>();
        if constexpr (s + 2 < 10) issue1(s + 2);
        else if constexpr (s == 8) { dma1<1>(L0 + MISC + L * 1024, L * 1024 + l * 16, rws, WS_XS + m * 2048); issue_c(0); issue_c(1); issue_c(2); }
        else { issue_c(3); issue_c(4); issue_c(5); }
        if constexpr (s == 0) get_tag(TAG);
      });
      waitbar<24>();
      bar(); bar();
      await(FL_A + m * 5 + a, FL_A + m * 5 + l, l < 5, [&]() AINL { issue_h(0); issue_h(1); issue_h(2); });
      sfor<0, 20>([&](auto ic) AINL {
        constexpr int s = ic.v;
        constexpr int gs[23] = {4, 4, 4, 4, 8, 8, 8, 8, 8, 8, 8, 8, 8, 8, 8, 8, 8, 8, 4, 4, 0, 0, 0};
        waitbar<gs[s + 1] + gs[s + 2]>();
        if constexpr (s + 3 < 20) issue_h(s + 3);
        if constexpr (s >= 1 && s + 5 < 20) issue_c(s + 5);
      });
    } else {
      const int p = a * 128 + w * 16 + li;
      const u32 plr = bld16(p * 2, mk_rsrc(lam_re, 1280)), pli = bld16(p * 2, mk_rsrc(lam_im, 1280)), pdt = bld16(p * 2, mk_rsrc(log_dt, 1280));
      row_stats();
      put_tag(TAG);
      float lbr, lbi, kr, ki;
      {
        const float Lr = bflo(plr), Li = bflo(pli);
        const float dt = bflo(pk2(expf(bflo(pdt)), 0.f));
        const float ea = expf(dt * Lr);
        float sn, cn; sincosf(dt * Li, &sn, &cn);
        lbr = ea * cn; lbi = ea * sn;
        const float inv = 2.0f / (Lr * Lr + Li * Li);
        kr = ((lbr - 1.0f) * Lr + lbi * Li) * inv;
        ki = (lbi * Lr - (lbr - 1.0f) * Li) * inv;
        asm volatile("" : "+v"(lbr), "+v"(lbi), "+v"(kr), "+v"(ki));
      }
      f32x4 are[8], aim[8], sre = zero4, sim = zero4;
#pragma unroll
      for (int i = 0; i < 8; ++i) { are[i] = zero4; aim[i] = zero4; }
      {
        struct FW { bf16x8 br, bi; };
        struct FX { bf16x8 x[4]; };
        FW W0, W1; FX Xa, Xb;
        auto half = [&](auto mf, auto rw, auto hh, FW& RW, LAS char* pw, FX& RX, LAS char* px, const FW& MW, const FX& MX) AINL {
          constexpr int h = hh.v;
          if constexpr (rw.v) { RW.br = lds128(pw); RX.x[0] = lds128(px); } else { RX.x[0] = lds128(px); RX.x[1] = lds128(px + 2048); }
          SB();
          if constexpr (mf.v) are[4 * h] = mfma(MX.x[0], MW.br, are[4 * h]);
          SB();
          if constexpr (rw.v) { RW.bi = lds128(pw + 16384); RX.x[1] = lds128(px + 2048); } else { RX.x[2] = lds128(px + 4096); RX.x[3] = lds128(px + 6144); }
          SB();
          if constexpr (mf.v) aim[4 * h] = mfma(MX.x[0], MW.bi, aim[4 * h]);
          SB();
          if constexpr (rw.v) { RX.x[2] = lds128(px + 4096); RX.x[3] = lds128(px + 6144); }
          SB();
          if constexpr (mf.v) {
#pragma unroll
            for (int i = 1; i < 4; ++i) { are[4 * h + i] = mfma(MX.x[i], MW.br, are[4 * h + i]); aim[4 * h + i] = mfma(MX.x[i], MW.bi, aim[4 * h + i]); }
            if constexpr (h == 0) sre = mfma(ones, MW.br, keep(sre)); else sim = mfma(ones, MW.bi, keep(sim));
          }
          SB();
        };
        STAMP(1);
        sfor<0, 10>([&](auto ic) AINL {
          constexpr int s = ic.v;
          LAS char* const x0 = pin(sm + rb0 + (s % 3) * 49152);
          LAS char* const x1 = pin(sm + (rb0 ^ 64) + (s % 3) * 49152);
          LAS char* const w0 = pin(x0 + w * 2048 + 16384);
          LAS char* const w1_ = pin(x1 + w * 2048 + 16384);
          bar();
          if constexpr (s == 0) STAMP(16);
          half(IC<(s > 0)>{}, IC<1>{}, IC<1>{}, W0, w0, Xa, x0, W1, Xb);
          half(IC<1>{}, IC<0>{}, IC<0>{}, W0, w0, Xb, x0 + 8192, W0, Xa);
          half(IC<1>{}, IC<1>{}, IC<1>{}, W1, w1_, Xa, x1, W0, Xb);
          half(IC<1>{}, IC<0>{}, IC<0>{}, W1, w1_, Xb, x1 + 8192, W1, Xa);
        });
#pragma unroll
        for (int i = 0; i < 4; ++i) { are[4 + i] = mfma(Xb.x[i], W1.br, are[4 + i]); aim[4 + i] = mfma(Xb.x[i], W1.bi, aim[4 + i]); }
        sim = mfma(ones, W1.bi, sim);
      }
      bar();
      STAMP(2);
      if (w < 2) {
        u32x4 g0 = *(LAS u32x4*)(sm + MISC + tid * 16);
        if (!__all(g0.y == ta && g0.w == tb)) g0 = poll_granule(WS_XS + (m * 128 + tid) * 16, true, rws, ta, tb);
        const float rs = asf(g0.z);
        *(LAS f32x2*)(sm + CST + (tid >> 5) * 272 + (tid & 31) * 8) = (f32x2){rs, -rs * asf(g0.x)};
      }
      bar();
#pragma unroll
      for (int mt = 0; mt < 8; ++mt) {
        const f32x4 s0 = *(LAS f32x4*)(sm + CST + g * 272 + mt * 32), s1 = *(LAS f32x4*)(sm + CST + g * 272 + mt * 32 + 16);
        const float rv[4] = {s0.x, s0.z, s1.x, s1.z}, nv[4] = {s0.y, s0.w, s1.y, s1.w};
#pragma unroll
        for (int e = 0; e < 4; ++e) {
          are[mt][e] = __builtin_fmaf(rv[e], are[mt][e], nv[e] * sre.x);
          aim[mt][e] = __builtin_fmaf(rv[e], aim[mt][e], nv[e] * sim.x);
        }
      }
      bar();
      float hr = 0.f, hi = 0.f;
#pragma unroll
      for (int mt = 0; mt < 8; ++mt)
#pragma unroll
        for (int e = 0; e < 4; ++e) {
          const float nr = __builtin_fmaf(-lbi, hi, __builtin_fmaf(lbr, hr, are[mt][e]));
          const float ni = __builtin_fmaf(lbi, hr, __builtin_fmaf(lbr, hi, aim[mt][e]));
          hr = nr; hi = ni;
        }
      float er[4], ei[4];
      gather4(hr, er); gather4(hi, ei);
      float p32r = lbr, p32i = lbi;
#pragma unroll
      for (int i = 0; i < 5; ++i) { const float t = p32r * p32r - p32i * p32i; p32i = 2.0f * p32r * p32i; p32r = t; }
      float cr[5], ci[5];
      cr[0] = 0.f; ci[0] = 0.f;
#pragma unroll
      for (int q = 0; q < 4; ++q) {
        cr[q + 1] = __builtin_fmaf(-p32i, ci[q], __builtin_fmaf(p32r, cr[q], er[q]));
        ci[q + 1] = __builtin_fmaf(p32i, cr[q], __builtin_fmaf(p32r, ci[q], ei[q]));
      }
      st128_sc1((u32x4){asu(cr[4]), ta, asu(ci[4]), tb}, l < 16 ? WS_EG + (m * 640 + p) * 16 : OOB, rws);
      STAMP(3);
      const float p64r = p32r * p32r - p32i * p32i, p64i = 2.0f * p32r * p32i;
      const float Pr = p64r * p64r - p64i * p64i, Pi = 2.0f * p64r * p64i;
      float Hr = 0.f, Hi = 0.f;
      if (m > 0) {
        u32x4 e0, e1, e2, e3;
        const int m0 = m - 1 - 4 * g;
        const u32 o0 = WS_EG + (m0 * 640 + p) * 16;
        const bool v0 = m0 >= 0, v1 = m0 >= 1, v2 = m0 >= 2, v3 = m0 >= 3;
        _Pragma("nounroll") for (int it = 0; it < SPIN_CAP; ++it) {
          asm volatile("buffer_load_dwordx4 %0, %4, %8, 0 offen sc1\n\tbuffer_load_dwordx4 %1, %5, %8, 0 offen sc1\n\t"
                       "buffer_load_dwordx4 %2, %6, %8, 0 offen sc1\n\tbuffer_load_dwordx4 %3, %7, %8, 0 offen sc1\n\ts_waitcnt vmcnt(0)"
                       : "=&v"(e0), "=&v"(e1), "=&v"(e2), "=&v"(e3)
                       : "v"(v0 ? o0 : OOB), "v"(v1 ? o0 - 10240 : OOB), "v"(v2 ? o0 - 20480 : OOB), "v"(v3 ? o0 - 30720 : OOB), "s"(rws) : "memory");
          const bool ok = (!v0 || (e0.y == ta && e0.w == tb)) && (!v1 || (e1.y == ta && e1.w == tb)) &&
                          (!v2 || (e2.y == ta && e2.w == tb)) && (!v3 || (e3.y == ta && e3.w == tb));
          if (__all(ok)) break;
          __builtin_amdgcn_s_sleep(1);
        }
        float qr = asf(e3.x), qi = asf(e3.z), t;
        t = __builtin_fmaf(-Pi, qi, __builtin_fmaf(Pr, qr, asf(e2.x))); qi = __builtin_fmaf(Pi, qr, __builtin_fmaf(Pr, qi, asf(e2.z))); qr = t;
        t = __builtin_fmaf(-Pi, qi, __builtin_fmaf(Pr, qr, asf(e1.x))); qi = __builtin_fmaf(Pi, qr, __builtin_fmaf(Pr, qi, asf(e1.z))); qr = t;
        t = __builtin_fmaf(-Pi, qi, __builtin_fmaf(Pr, qr, asf(e0.x))); qi = __builtin_fmaf(Pi, qr, __builtin_fmaf(Pr, qi, asf(e0.z))); qr = t;
        float gr[4], gi[4];
        gather4(qr, gr); gather4(qi, gi);
        const float P2r = Pr * Pr - Pi * Pi, P2i = 2.0f * Pr * Pi;
        const float P4r = P2r * P2r - P2i * P2i, P4i = 2.0f * P2r * P2i;
        Hr = gr[3]; Hi = gi[3];
#pragma unroll
        for (int q = 2; q >= 0; --q) {
          t = __builtin_fmaf(-P4i, Hi, __builtin_fmaf(P4r, Hr, gr[q])); Hi = __builtin_fmaf(P4i, Hr, __builtin_fmaf(P4r, Hi, gi[q])); Hr = t;
        }
      }
      STAMP(4);
      {
        const float p96r = p64r * p32r - p64i * p32i, p96i = p64r * p32i + p64i * p32r;
        const float fr = g == 0 ? 1.f : g == 1 ? p32r : g == 2 ? p64r : p96r;
        const float fi = g == 0 ? 0.f : g == 1 ? p32i : g == 2 ? p64i : p96i;
        const float c0r = g == 0 ? cr[0] : g == 1 ? cr[1] : g == 2 ? cr[2] : cr[3];
        const float c0i = g == 0 ? ci[0] : g == 1 ? ci[1] : g == 2 ? ci[2] : ci[3];
        hr = __builtin_fmaf(-fi, Hi, __builtin_fmaf(fr, Hr, c0r));
        hi = __builtin_fmaf(fi, Hr, __builtin_fmaf(fr, Hi, c0i));
      }
#pragma unroll
      for (int mt = 0; mt < 8; ++mt)
#pragma unroll
        for (int e = 0; e < 4; ++e) {
          const float nr = __builtin_fmaf(-lbi, hi, __builtin_fmaf(lbr, hr, are[mt][e]));
          const float ni = __builtin_fmaf(lbi, hr, __builtin_fmaf(lbr, hi, aim[mt][e]));
          hr = nr; hi = ni;
          are[mt][e] = __builtin_fmaf(kr, hr, -ki * hi);
          aim[mt][e] = __builtin_fmaf(-kr, hi, -ki * hr);
        }
      {
        LAS char* const tb_ = sm + (w < 6 ? w * 8192 : MISC + (w - 6) * 8192);
        const int swc = ((li & 3) << 1) | ((li >> 2) & 1) | ((li >> 3) << 3);
#pragma unroll
        for (int i = 0; i < 4; ++i) {
          *(LAS u32x4*)(tb_ + li * 256 + (((4 * g + i) ^ swc) << 4)) = pack8(are[2 * i], are[2 * i + 1]);
          *(LAS u32x4*)(tb_ + 4096 + li * 256 + (((4 * g + i) ^ swc) << 4)) = pack8(aim[2 * i], aim[2 * i + 1]);
        }
        asm volatile("s_waitcnt lgkmcnt(0)" ::: "memory");
        const int q4 = li >> 2, p4 = l & 3;
        const int c0 = 8 * (g & 1) + q4, c1 = c0 + 4;
        const int sw0 = ((c0 & 3) << 1) | ((c0 >> 3) << 3), sw1 = sw0 | 1;
#pragma unroll
        for (int part = 0; part < 2; ++part)
#pragma unroll
          for (int u = 0; u < 4; ++u) {
            const int tt = 2 * u + (g >> 1), ch = 2 * tt + (p4 >> 1);
            const bf16x8 v = ldstr(tb_ + part * 4096 + c0 * 256 + ((ch ^ sw0) << 4) + 8 * (p4 & 1),
                                   tb_ + part * 4096 + c1 * 256 + ((ch ^ sw1) << 4) + 8 * (p4 & 1));
            const u32 off = WS_HH + (m * 20 + 10 * part + 2 * a + (w >> 2)) * 16384 + tt * 2048 + ((w >> 1) & 1) * 1024 + ((2 * (w & 1) + (g & 1)) * 16 + li) * 16;
            st128_sc1(__builtin_bit_cast(u32x4, v), off, rws);
          }
      }
      waitbar<0>();
      STAMP(5);
      bar();
      STAMP(6);
      f32x4 ac[4][2];
#pragma unroll
      for (int i = 0; i < 4; ++i) { ac[i][0] = zero4; ac[i][1] = zero4; }
      LAS char* const ab = pin(sm + tq * 4096 + l * 16);
      LAS char* const ab3 = pin(ab + MISC);
      LAS char* cb[2][2];
#pragma unroll
      for (int i = 0; i < 2; ++i) { cb[0][i] = pin(sm + 49152 + i * 49152 + cq * 8192 + rb0); cb[1][i] = pin(sm + 49152 + i * 49152 + cq * 8192 + (rb0 ^ 64)); }
      struct FC { bf16x8 c[4], b0, b1; };
      FC G0, G1;
      auto stepC = [&](auto mf, FC& R, int ks, int s, const FC& M) AINL {
        LAS char* const pa = ((s & 3) == 3 ? ab3 : ab + (s & 3) * 16384) + ks * 1024;
        LAS char* const pc = cb[ks][(s % 6) / 3] + ((s % 6) % 3) * 16384;
        R.b0 = lds128(pa); R.c[0] = lds128(pc); SB();
        if constexpr (mf.v) ac[0][0] = mfma(M.c[0], M.b0, ac[0][0]);
        SB(); R.b1 = lds128(pa + 2048); R.c[1] = lds128(pc + 2048); SB();
        if constexpr (mf.v) ac[0][1] = mfma(M.c[0], M.b1, ac[0][1]);
        SB(); R.c[2] = lds128(pc + 4096); R.c[3] = lds128(pc + 6144); SB();
        if constexpr (mf.v) {
#pragma unroll
          for (int ct = 1; ct < 4; ++ct) { ac[ct][0] = mfma(M.c[ct], M.b0, ac[ct][0]); ac[ct][1] = mfma(M.c[ct], M.b1, ac[ct][1]); }
        }
        SB();
      };
      sfor<0, 20>([&](auto ic) AINL {
        constexpr int s = ic.v;
        bar();
        if constexpr (s == 0) STAMP(17);
        stepC(IC<(s > 0)>{}, G0, 0, s, G1);
        stepC(IC<1>{}, G1, 1, s, G0);
      });
#pragma unroll
      for (int ct = 0; ct < 4; ++ct) { ac[ct][0] = mfma(G1.c[ct], G1.b0, ac[ct][0]); ac[ct][1] = mfma(G1.c[ct], G1.b1, ac[ct][1]); }
      STAMP(7);
#pragma unroll
      for (int cb_ = 0; cb_ < 2; ++cb_)
#pragma unroll
        for (int tt = 0; tt < 2; ++tt)
          st128_sc1(pack8(ac[2 * cb_][tt], ac[2 * cb_ + 1][tt]), WS_SS + ((m * 10 + 2 * a + cq) * 8 + tq + 4 * cb_) * 2048 + tt * 1024 + l * 16, rws);
      waitbar<0>();
      if (w == 0) set_flag(FL_S + m * 5 + a);
      STAMP(8);
    }
  } else if (w < 8) {
    row_stats();
    u32 dummy = 0;
    const u32 lo = tid < 400 ? (m * 400 + tid) * 128 : OOB;
    touch(dummy, lo, mk_rsrc(w2, 640 * 1280));
    touch(dummy, lo, mk_rsrc(c_re, 640 * 1280));
    touch(dummy, lo, mk_rsrc(c_im, 640 * 1280));
    const rsrc_t rw4 = mk_rsrc(w4, 1536 * 1280);
    if (m < 12) {
      const rsrc_t rw3 = mk_rsrc(w3, 640 * 3072);
      float cs[8];
#pragma unroll
      for (int e = 0; e < 8; ++e) cs[e] = 0.f;
      const u32 vo = (tid >> 4) * 3072 + m * 256 + (tid & 15) * 16;
      u32x4 t[20];
#pragma unroll
      for (int i = 0; i < 20; ++i) t[i] = bld128(vo, rw3, i * 32 * 3072);
      SB();
#pragma unroll
      for (int i = 0; i < 20; ++i) {
        cs[0] += bflo(t[i].x); cs[1] += bfhi(t[i].x); cs[2] += bflo(t[i].y); cs[3] += bfhi(t[i].y);
        cs[4] += bflo(t[i].z); cs[5] += bfhi(t[i].z); cs[6] += bflo(t[i].w); cs[7] += bfhi(t[i].w);
      }
#pragma unroll
      for (int e = 0; e < 8; ++e) cs[e] = sum32(sum16(cs[e]));
      if (l < 16) {
        *(LAS f32x4*)(sm + w * 512 + l * 32) = (f32x4){cs[0], cs[1], cs[2], cs[3]};
        *(LAS f32x4*)(sm + w * 512 + l * 32 + 16) = (f32x4){cs[4], cs[5], cs[6], cs[7]};
      }
      bar();
      if (w < 2) {
        float s = 0.f;
#pragma unroll
        for (int i = 0; i < 8; ++i) s += *(LAS float*)(sm + i * 512 + tid * 4);
        st32_sc1(asu(s), WS_CS3 + (m * 128 + tid) * 4, rws);
      }
      waitbar<0>();
      if (w == 0) set_flag(FL_C + m);
    }
    STAMP(1);
    touch(dummy, (m * 960 + tid) * 128, rw4);
    touch(dummy, tid < 448 ? (m * 960 + 512 + tid) * 128 : OOB, rw4);
    asm volatile("s_waitcnt vmcnt(0)" : "+v"(dummy)::"memory");
    STAMP(2);
    if (m == 0) {
#pragma unroll
      for (int i = 0; i < 4; ++i) (void)poll_granule(WS_XS + (i * 512 + tid) * 16, true, rws, ta, tb);
      bar();
      if (tid == 0) st64_sc1((u32x2){ta, tb}, WS_EPOCH, rws);
    }
  }
#endif
}

__global__ void pre_k(unsigned long long* p) {
#if defined(__HIP_DEVICE_COMPILE__) && __HIP_DEVICE_COMPILE__
  *p = __builtin_amdgcn_s_memrealtime();
#endif
}
extern "C" void kernel_launch(void* const* d_in, const int* in_sizes, int n_in, void* d_out, int out_size,
                              void* d_ws, size_t ws_size, hipStream_t stream) {
  (void)in_sizes; (void)n_in; (void)out_size; (void)ws_size;
  pre_k<<<256, 1024, 0, stream>>>((unsigned long long*)((char*)d_ws + WS_STAMP));
  s5_mixer_kernel<<<256, 768, 0, stream>>>(d_in[0], d_in[1], d_in[2], d_in[3], d_in[4], d_in[5], d_in[6], d_in[7],
                                           d_in[8], d_in[10], d_in[11], d_in[12], d_in[13], d_out, d_ws);
}
```

```cpp
#include <hip/hip_runtime.h>
#include <hip/hip_bf16.h>
#include <stdint.h>

#define DI __device__ __forceinline__
#define LAS __attribute__((address_space(3)))
#define AINL __attribute__((always_inline))
typedef unsigned u32;
typedef unsigned long long u64;
typedef u32 u32x4 __attribute__((ext_vector_type(4)));
typedef u32 u32x2 __attribute__((ext_vector_type(2)));
typedef float f32x4 __attribute__((ext_vector_type(4)));
typedef float f32x2 __attribute__((ext_vector_type(2)));
typedef __bf16 bf16x8 __attribute__((ext_vector_type(8)));
typedef __bf16 bf16x2 __attribute__((ext_vector_type(2)));
typedef short s16x4 __attribute__((ext_vector_type(4)));
typedef short s16x8 __attribute__((ext_vector_type(8)));

#define XMAP 1
#define ABL 0
#define PSLEEP 2
#define WARM 1
#define STAMPS 0

constexpr u32 WS_EPOCH = 0;
constexpr u32 WS_FLAG = 0x1000;
constexpr u32 FL_1 = 0, FL_2 = 160, FL_3 = 320, FL_A = 480, FL_S = 560, FL_C = 640;
constexpr u32 WS_XS = 0x20000;
constexpr u32 WS_CS3 = 0x2A000;
constexpr u32 WS_RS = 0x58000;
constexpr u32 WS_EG = 0x30000;
constexpr u32 WS_ST1 = 0x60000;
constexpr u32 WS_SS = 0xC0000;
constexpr u32 WS_H1 = 0x400000;
constexpr u32 WS_X1 = 0x680000;
constexpr u32 WS_HH = 0x900000;
constexpr u32 WS_H2 = 0xE00000;
constexpr u32 WS_STAMP = 0x1400000;
constexpr u32 WS_BYTES = 0x1500000;
constexpr u32 OOB = 0x7FFFFFF0u;
constexpr float LN_EPS = 1e-5f;
constexpr int SPIN_CAP = 1 << 17;

template <int I> struct IC { static constexpr int v = I; };
template <int B, int E, class F> DI void sfor(F&& f) {
  if constexpr (B < E) { f(IC<B>{}); sfor<B + 1, E>(f); }
}

#if defined(__HIP_DEVICE_COMPILE__) && __HIP_DEVICE_COMPILE__
typedef __amdgpu_buffer_rsrc_t rsrc_t;
DI rsrc_t mk_rsrc(const void* p, u32 bytes) { return __builtin_amdgcn_make_buffer_rsrc((void*)p, 0, bytes, 0x00020000); }

template <int SC1> DI void dma1(u32 lds, u32 voff, rsrc_t r, u32 soff) {
  if (ABL == 2) return;
  if constexpr (SC1)
    asm volatile("s_mov_b32 m0, %0\n\ts_nop 0\n\tbuffer_load_dwordx4 %1, %2, %3 offen sc1 lds" ::"s"(lds), "v"(voff), "s"(r), "s"(soff) : "memory");
  else
    asm volatile("s_mov_b32 m0, %0\n\ts_nop 0\n\tbuffer_load_dwordx4 %1, %2, %3 offen lds" ::"s"(lds), "v"(voff), "s"(r), "s"(soff) : "memory");
}
template <int SC1> DI void dma2(u32 lds, u32 v0, u32 v1, rsrc_t r, u32 soff) {
  if (ABL == 2) return;
  if constexpr (SC1)
    asm volatile("s_mov_b32 m0, %0\n\ts_nop 0\n\tbuffer_load_dwordx4 %1, %3, %4 offen sc1 lds\n\tbuffer_load_dwordx4 %2, %3, %4 offen offset:1024 sc1 lds"
                 ::"s"(lds), "v"(v0), "v"(v1), "s"(r), "s"(soff) : "memory");
  else
    asm volatile("s_mov_b32 m0, %0\n\ts_nop 0\n\tbuffer_load_dwordx4 %1, %3, %4 offen lds\n\tbuffer_load_dwordx4 %2, %3, %4 offen offset:1024 lds"
                 ::"s"(lds), "v"(v0), "v"(v1), "s"(r), "s"(soff) : "memory");
}
template <int SC1> DI void dma4(u32 lds, u32 v, rsrc_t r, u32 soff) {
  if (ABL == 2) return;
  if constexpr (SC1)
    asm volatile("s_mov_b32 m0, %0\n\ts_nop 0\n\tbuffer_load_dwordx4 %1, %2, %3 offen sc1 lds\n\tbuffer_load_dwordx4 %1, %2, %3 offen offset:1024 sc1 lds\n\t"
                 "buffer_load_dwordx4 %1, %2, %3 offen offset:2048 sc1 lds\n\tbuffer_load_dwordx4 %1, %2, %3 offen offset:3072 sc1 lds"
                 ::"s"(lds), "v"(v), "s"(r), "s"(soff) : "memory");
  else
    asm volatile("s_mov_b32 m0, %0\n\ts_nop 0\n\tbuffer_load_dwordx4 %1, %2, %3 offen lds\n\tbuffer_load_dwordx4 %1, %2, %3 offen offset:1024 lds\n\t"
                 "buffer_load_dwordx4 %1, %2, %3 offen offset:2048 lds\n\tbuffer_load_dwordx4 %1, %2, %3 offen offset:3072 lds"
                 ::"s"(lds), "v"(v), "s"(r), "s"(soff) : "memory");
}
constexpr int VC(int n) { return ABL == 2 ? 0 : n; }
template <int N> DI void waitv() { asm volatile("s_waitcnt vmcnt(%0)" ::"i"(VC(N)) : "memory"); }
DI void bar() { asm volatile("s_waitcnt lgkmcnt(0)\n\ts_barrier" ::: "memory"); }
template <int N> DI void waitbar() { asm volatile("s_waitcnt vmcnt(%0) lgkmcnt(0)\n\ts_barrier" ::"i"(VC(N)) : "memory"); }

DI void touch(u32& d, u32 voff, rsrc_t r) { asm volatile("buffer_load_dword %0, %1, %2, 0 offen" : "+v"(d) : "v"(voff), "s"(r) : "memory"); }

DI u32x4 bld128(u32 voff, rsrc_t r, u32 soff) { return __builtin_amdgcn_raw_buffer_load_b128(r, voff, soff, 0); }
DI u32x2 bld64(u32 voff, rsrc_t r, u32 soff) { return __builtin_amdgcn_raw_buffer_load_b64(r, voff, soff, 0); }
DI u32x2 bld64_sc1(u32 voff, rsrc_t r, u32 soff) { return __builtin_amdgcn_raw_buffer_load_b64(r, voff, soff, 16); }
DI u32 bld32(u32 voff, rsrc_t r, u32 soff) { return __builtin_amdgcn_raw_buffer_load_b32(r, voff, soff, 0); }
DI u32 bld16(u32 voff, rsrc_t r) { return __builtin_amdgcn_raw_buffer_load_b16(r, voff, 0, 0); }
DI void st128_sc1(u32x4 v, u32 voff, rsrc_t r) { __builtin_amdgcn_raw_buffer_store_b128(v, r, voff, 0, 16); }
DI void st64_sc1(u32x2 v, u32 voff, rsrc_t r) { __builtin_amdgcn_raw_buffer_store_b64(v, r, voff, 0, 16); }
DI void st32_sc1(u32 v, u32 voff, rsrc_t r) { __builtin_amdgcn_raw_buffer_store_b32(v, r, voff, 0, 16); }

DI void poll_flags(u32 voff, bool active, rsrc_t ws, u32 ta, u32 tb) {
  _Pragma("nounroll") for (int it = 0; it < SPIN_CAP; ++it) {
    u32x2 v;
    asm volatile("buffer_load_dwordx2 %0, %1, %2, 0 offen sc1\n\ts_waitcnt vmcnt(0)" : "=&v"(v) : "v"(voff), "s"(ws) : "memory");
    const bool ok = !active || (v.x == ta && v.y == tb);
    if (__all(ok)) break;
    __builtin_amdgcn_s_sleep(1);
  }
}
DI u32x4 poll_granule(u32 voff, bool active, rsrc_t ws, u32 ta, u32 tb) {
  u32x4 v;
  _Pragma("nounroll") for (int it = 0; it < SPIN_CAP; ++it) {
    asm volatile("buffer_load_dwordx4 %0, %1, %2, 0 offen sc1\n\ts_waitcnt vmcnt(0)" : "=&v"(v) : "v"(voff), "s"(ws) : "memory");
    const bool ok = !active || (v.y == ta && v.w == tb);
    if (__all(ok)) break;
    __builtin_amdgcn_s_sleep(1);
  }
  return v;
}

DI float asf(u32 x) { return __uint_as_float(x); }
DI u32 asu(float x) { return __float_as_uint(x); }
DI float bflo(u32 x) { return asf(x << 16); }
DI float bfhi(u32 x) { return asf(x & 0xffff0000u); }
DI u32 pk2(float a, float b) { f32x2 p = {a, b}; bf16x2 q = __builtin_convertvector(p, bf16x2); return __builtin_bit_cast(u32, q); }
DI float sum16(float x) { const u32 u = asu(x); auto r = __builtin_amdgcn_permlane16_swap(u, u, false, false); const u32 a = r[0], b = r[1]; return asf(a) + asf(b); }
DI float sum32(float x) { const u32 u = asu(x); auto r = __builtin_amdgcn_permlane32_swap(u, u, false, false); const u32 a = r[0], b = r[1]; return asf(a) + asf(b); }
template <int C> DI float dppadd(float x) { return x + asf(__builtin_amdgcn_update_dpp(0u, asu(x), C, 0xf, 0xf, true)); }
DI float wavesum(float x) { x = dppadd<0xB1>(x); x = dppadd<0x4E>(x); x = dppadd<0x141>(x); x = dppadd<0x140>(x); return sum32(sum16(x)); }
DI void gather4(float x, float (&e)[4]) {
  const u32 u = asu(x);
  auto p = __builtin_amdgcn_permlane16_swap(u, u, false, false);
  const u32 a = p[0], b = p[1];
  auto pa = __builtin_amdgcn_permlane32_swap(a, a, false, false);
  auto pb = __builtin_amdgcn_permlane32_swap(b, b, false, false);
  const u32 e0 = pa[0], e2 = pa[1], e1 = pb[0], e3 = pb[1];
  e[0] = asf(e0); e[1] = asf(e1); e[2] = asf(e2); e[3] = asf(e3);
}
DI float gelu_f(float v) {
  const float c1 = -2.302208198f, c2 = -0.1029432397f;
  const float e = __builtin_amdgcn_exp2f(v * __builtin_fmaf(v * v, c2, c1));
  return v * __builtin_amdgcn_rcpf(1.0f + e);
}
DI float silu_f(float z) { return z * __builtin_amdgcn_rcpf(1.0f + __builtin_amdgcn_exp2f(z * -1.4426950409f)); }
DI f32x4 mfma(bf16x8 a, bf16x8 b, f32x4 c) { if (ABL == 1 || ABL == 4) { asm volatile("" : "+v"(c) : "v"(a), "v"(b)); return c; } return __builtin_amdgcn_mfma_f32_16x16x32_bf16(a, b, c, 0, 0, 0); }
DI bf16x8 lds128(LAS char* p) { if (ABL == 1 || ABL == 3) { bf16x8 r; asm volatile("" : "=v"(r)); return r; } return *(LAS bf16x8*)p; }
DI bf16x8 ldstr(LAS char* p0, LAS char* p1) {
  if (ABL == 1 || ABL == 3) { bf16x8 r; asm volatile("" : "=v"(r)); return r; }
  const s16x4 a = __builtin_amdgcn_ds_read_tr16_b64_v4i16((LAS s16x4*)p0);
  const s16x4 b = __builtin_amdgcn_ds_read_tr16_b64_v4i16((LAS s16x4*)p1);
  const s16x8 w = __builtin_shufflevector(a, b, 0, 1, 2, 3, 4, 5, 6, 7);
  return __builtin_bit_cast(bf16x8, w);
}
DI u32x4 pack8(f32x4 a, f32x4 b) { u32x4 r; r.x = pk2(a.x, a.y); r.y = pk2(a.z, a.w); r.z = pk2(b.x, b.y); r.w = pk2(b.z, b.w); return r; }
DI u32x2 pack4(f32x4 a) { u32x2 r; r.x = pk2(a.x, a.y); r.y = pk2(a.z, a.w); return r; }
DI int wrapk(int k, int n) { return k >= n ? k - n : k; }
#define SB() __builtin_amdgcn_sched_barrier(0)
DI f32x4 keep(f32x4 v) { asm volatile("" : "+v"(v)); return v; }
DI LAS char* pin(LAS char* p) { asm volatile("" : "+v"(p)); return p; }
#endif

#define STAMP(i) do { } while (0)
#define CSTAMP(i) do { } while (0)

#define ROT 0
__global__ __launch_bounds__(768, 3) void s5_mixer_kernel(
    const void* __restrict__ x, const void* __restrict__ lam_re, const void* __restrict__ lam_im,
    const void* __restrict__ b_re, const void* __restrict__ b_im, const void* __restrict__ c_re,
    const void* __restrict__ c_im, const void* __restrict__ log_dt, const void* __restrict__ dskip,
    const void* __restrict__ w1, const void* __restrict__ w2, const void* __restrict__ w3,
    const void* __restrict__ w4, void* __restrict__ out, void* __restrict__ ws) {
#if defined(__HIP_DEVICE_COMPILE__) && __HIP_DEVICE_COMPILE__
  __shared__ __attribute__((aligned(16))) char smem_[163840];
  LAS char* const sm = (LAS char*)smem_;
  const u32 L0 = (u32)(size_t)sm;
  const int tid = threadIdx.x, l = tid & 63;
  const int w = __builtin_amdgcn_readfirstlane(tid >> 6);
  const int bid = blockIdx.x, xcd = bid & 7, rr = bid >> 3;
  const int m = (xcd >> 1) * 4 + (rr < 20 ? rr / 5 : (rr - 20) / 3);
  const int role = rr < 20 ? rr % 5 + 5 * (xcd & 1) : 10 + (rr - 20) % 3 + 3 * (xcd & 1);
  const int g = l >> 4, li = l & 15, r8 = l >> 3, tq = w & 3, cq = w >> 2, L = w - 8;
  const rsrc_t rws = mk_rsrc(ws, WS_BYTES);
  const rsrc_t rx = mk_rsrc(x, 2048 * 1280);
  STAMP(0); CSTAMP(30);
  const int fm = (5 * (m & 3) + 1) >> 1;
  const int rot1 = (2 * (role % 5) + fm) % 10, rotA1 = (2 * (role % 3) + 1 + fm) % 10;
  const int rot10 = 0, rot24 = 0, rotA3 = 0;

  u32 ta, tb;
  auto row_stats = [&]() AINL {
    const int stok = m * 128 + role * 8 + w;
    const u32x2 ep = bld64_sc1(0, rws, 0);
    const u32x4 ra = bld128(l * 16, rx, stok * 1280);
    const u32 rb_ = bld32(1024 + l * 4, rx, stok * 1280);
    const u64 e = (((u64)__builtin_amdgcn_readfirstlane(ep.y) << 32) | (u32)__builtin_amdgcn_readfirstlane(ep.x)) + 0x9E3779B97F4A7C15ull;
    ta = (u32)e; tb = (u32)(e >> 32);
    float v[10] = {bflo(ra.x), bfhi(ra.x), bflo(ra.y), bfhi(ra.y), bflo(ra.z), bfhi(ra.z), bflo(ra.w), bfhi(ra.w), bflo(rb_), bfhi(rb_)};
    float s = 0.f;
#pragma unroll
    for (int i = 0; i < 10; ++i) s += v[i];
    const float mu = wavesum(s) * (1.0f / 640.0f);
    float q = 0.f;
#pragma unroll
    for (int i = 0; i < 10; ++i) { const float d = v[i] - mu; q = __builtin_fmaf(d, d, q); }
    const float rs = 1.0f / sqrtf(wavesum(q) * (1.0f / 640.0f) + LN_EPS);
    u32x4 gr = {asu(mu), ta, asu(rs), tb};
    st128_sc1(gr, l == 0 ? WS_XS + stok * 16 : OOB, rws);
  };
  auto put_tag = [&](u32 off) AINL { if (tid == 0) *(LAS u32x2*)(sm + off) = (u32x2){ta, tb}; };
  auto get_tag = [&](u32 off) AINL {
    const u32x2 t = *(volatile LAS u32x2*)(sm + off);
    ta = __builtin_amdgcn_readfirstlane(t.x); tb = __builtin_amdgcn_readfirstlane(t.y);
  };
  auto set_flag = [&](u32 idx) AINL { st128_sc1((u32x4){ta, tb, ta, tb}, l < 8 ? WS_FLAG + idx * 128 + l * 16 : OOB, rws); };
  auto await = [&](u32 own, int i, bool act, auto&& first3) AINL {
    bar();
    if (L == 0) { set_flag(own); poll_flags(WS_FLAG + i * 128, act, rws, ta, tb); }
    bar();
    first3();
  };

  const u32 rb0 = li * 128 + ((g ^ (li >> 1)) << 4);
  const f32x4 zero4 = {0.f, 0.f, 0.f, 0.f};

  if (role < 10) {
    const int j = role;
    constexpr u32 ACT = 0, W = 65536;
    if (w >= 8) {
      const rsrc_t rw1 = mk_rsrc(w1, 640 * 1280), rw2 = mk_rsrc(w2, 640 * 1280);
      const rsrc_t rw3 = mk_rsrc(w3, 640 * 3072), rw4 = mk_rsrc(w4, 1536 * 1280);
      const u32 vx0 = (m * 128 + L * 32 + r8) * 1280 + (((l & 7) ^ g) << 4);
      const u32 vx1 = (m * 128 + L * 32 + 8 + r8) * 1280 + (((l & 7) ^ (4 + g)) << 4) - 1024;
      const u32 wch = j * 128 + (((l & 7) ^ (2 * g)) << 4);
      const u32 vq0 = ((L >> 1) * 32 + (r8 >> 2) * 8 + (L & 1) * 4 + (r8 & 3)) * 1280 + wch, vq1 = vq0 + 16 * 1280 - 1024;
      const u32 ldsA = L0 + ACT + L * 4096, ldsW = L0 + W + L * 2048;
      auto issue_1 = [&](int s) AINL {
        const int kt = wrapk(s + rot1, 10);
        dma2<0>(ldsA + (s & 3) * 16384, vx0, vx1, rx, kt * 128);
        dma2<0>(ldsW + 65536 + (s & 3) * 8192, vq0, vq1, rw1, kt * 81920);
        dma2<0>(ldsA + (s & 3) * 16384 + 2048, vx0, vx1, rx, kt * 128 + 20480);
      };
      issue_1(0); issue_1(1); issue_1(2);
      const u32 vw0 = (L * 16 + r8) * 1280 + wch, vw1 = vw0 + 10240 - 1024;
      const u32 va = L * 4096 + l * 16, vl = L * 1024 + l * 16;
      u32 v3[5];
#pragma unroll
      for (int i = 0; i < 5; ++i) {
        const int c = 64 * i + l, r = c / 20, pos = c - 20 * r;
        v3[i] = (L * 16 + r) * 3072 + j * 320 + ((pos ^ (2 * ((r >> 2) & 1))) << 4) - 1024 * (i & 1);
      }
      auto issue_w2 = [&](int s) AINL { dma2<0>(ldsW + s * 8192, vw0, vw1, rw2, wrapk(s + rot10, 10) * 81920); };
      auto issue_w3 = [&](int s) AINL {
        const u32 d = L0 + W + (s & 3) * 20480 + L * 5120, so = wrapk(s + rot10, 10) * 196608;
        dma2<0>(d, v3[0], v3[1], rw3, so); dma2<0>(d + 2048, v3[2], v3[3], rw3, so); dma1<0>(d + 4096, v3[4], rw3, so);
      };
      auto issue_w4 = [&](int s) AINL { dma2<0>(ldsW + ((s + 5) % 12) * 8192, vw0, vw1, rw4, wrapk(s + rot24, 24) * 81920); };
      auto issue_a = [&](int s, u32 base) AINL { dma4<1>(ldsA + (s & 3) * 16384, va, rws, base); };
      auto issue_h1 = [&](int s) AINL {
#pragma unroll
        for (int ks = 0; ks < 2; ++ks) {
          const int b = 2 * wrapk(s + rot10, 10) + ks, grp = b / 5;
          const int t = m * 128 + l - ((grp & 1) ? -(1 + (grp >> 1)) : (1 + (grp >> 1)));
          dma2<1>(L0 + ACT + (s & 3) * 16384 + ks * 8192 + L * 2048, t >= 0 ? t * 16 : OOB, t < 2048 - 64 ? t * 16 : OOB, rws, WS_H1 + (b * 4 + L) * 32768);
        }
      };
      const bf16x8 ones = __builtin_bit_cast(bf16x8, (u32x4){0x3F803F80u, 0x3F803F80u, 0x3F803F80u, 0x3F803F80u});
      LAS char* const wl = pin(sm + W + 65536 + (4 * g + (li >> 2)) * 128 + ((L ^ ((2 * g + (li >> 3)) & 3)) << 5) + 8 * (l & 3));
      f32x4 cs = zero4;
      sfor<0, 10>([&](auto ic) AINL {
        constexpr int s = ic.v;
        constexpr int gs[13] = {6, 6, 6, 6, 6, 6, 6, 6, 6, 6, 5, 6, 6};
        waitbar<gs[s + 1] + gs[s + 2]>();
        if constexpr (s + 3 < 10) issue_1(s + 3);
        else if constexpr (s == 7) { dma1<1>(L0 + ACT + 2 * 16384 + L * 1024, vl, rws, WS_XS + m * 2048); issue_w2(0); issue_w2(1); }
        else { issue_w2(3 * s - 22); issue_w2(3 * s - 21); issue_w2(3 * s - 20); }
        if constexpr (s == 0) get_tag(W);
        const bf16x8 c0 = ldstr(wl + (s & 3) * 8192, wl + (s & 3) * 8192 + 2048), c1 = ldstr(wl + (s & 3) * 8192 + 4096, wl + (s & 3) * 8192 + 6144);
        cs = keep(mfma(c1, ones, mfma(c0, ones, cs)));
      });
      if (li == 0) *(LAS f32x4*)(sm + ACT + 2 * 16384 + 4096 + L * 64 + g * 16) = cs;
      waitbar<16>();
      issue_w2(8); issue_w2(9);
      await(FL_1 + m * 10 + j, FL_1 + m * 10 + (l < 10 ? l : l < 16 ? -10 + (l - 10) + (l >= 13 ? 2 : 0) : 12 + (l - 16) + (l >= 19 ? 2 : 0)),
            l < 10 || (l < 16 && m > 0) || (l >= 16 && l < 22 && m < 15), [&]() AINL {
        sfor<0, 3>([&](auto ic) AINL { constexpr int s = ic.v; issue_h1(s); });
      });
      sfor<0, 10>([&](auto ic) AINL {
        constexpr int s = ic.v;
        constexpr int gs[13] = {4, 4, 4, 4, 4, 4, 4, 4, 4, 4, 5, 5, 5};
        waitbar<gs[s + 1] + gs[s + 2]>();
        if constexpr (s + 3 < 10) issue_h1(s + 3);
        else issue_w3(s - 7);
      });
      bar();
      issue_w3(3);
      await(FL_2 + m * 10 + j, l < 10 ? FL_2 + m * 10 + l : FL_C + ((5 * j) >> 2) + l - 10, l < (j < 9 ? 12 : 11), [&]() AINL {
        sfor<0, 3>([&](auto ic) AINL { constexpr int s = ic.v; issue_a(s, WS_X1 + (m * 10 + wrapk(s + rot10, 10)) * 16384); });
      });
      sfor<0, 10>([&](auto ic) AINL {
        constexpr int s = ic.v;
        constexpr int gs[13] = {4, 4, 4, 4, 9, 9, 9, 9, 9, 9, 8, 8, 8};
        waitbar<gs[s + 1] + gs[s + 2]>();
        if constexpr (s + 3 < 10) {
          issue_a(s + 3, WS_X1 + (m * 10 + wrapk(s + 3 + rot10, 10)) * 16384);
          if constexpr (s >= 1) issue_w3(s + 3);
        } else if constexpr (s == 7) {
          issue_a(2, WS_ST1 + m * 20480);
          issue_w4(0); issue_w4(1);
        } else if constexpr (s == 8) {
          dma1<1>(L0 + ACT + 3 * 16384 + L * 1024, vl, rws, WS_ST1 + m * 20480 + 16384);
          dma1<1>(L0 + ACT + 3 * 16384 + 4096 + L * 1024, vl, rws, WS_CS3 + j * 640);
          issue_w4(2); issue_w4(3); issue_w4(4);
        } else { issue_w4(5); issue_w4(6); issue_w4(7); issue_w4(8); }
      });
      waitbar<14>();
      issue_w4(9); issue_w4(10); issue_w4(11);
      bar();
      await(FL_3 + m * 10 + j, FL_3 + m * 10 + l, l < 10, [&]() AINL {
        sfor<0, 3>([&](auto ic) AINL { constexpr int s = ic.v; issue_a(s, WS_H2 + (m * 24 + wrapk(s + rot24, 24)) * 16384); });
      });
      u32x2 fs;
      const u32 fso = WS_FLAG + (FL_S + m * 5 + (j >> 1)) * 128;
      sfor<0, 24>([&](auto ic) AINL {
        constexpr int s = ic.v;
        constexpr int gs[27] = {4, 4, 4, 4, 6, 6, 6, 6, 6, 6, 6, 6, 6, 6, 6, 6, 4, 4, 4, 4, 5, 4, 4, 4, 4, 0, 0};
        if constexpr (s == 20) { asm volatile("s_waitcnt vmcnt(%1) lgkmcnt(0)\n\ts_barrier" : "+v"(fs) : "i"(VC(gs[s + 1] + gs[s + 2])) : "memory"); }
        else waitbar<gs[s + 1] + gs[s + 2]>();
        if constexpr (s == 17) asm volatile("buffer_load_dwordx2 %0, %1, %2, 0 offen sc1" : "=v"(fs) : "v"(fso), "s"(rws) : "memory");
        if constexpr (s + 3 < 24) issue_a(s + 3, WS_H2 + (m * 24 + wrapk(s + 3 + rot24, 24)) * 16384);
        if constexpr (s >= 1 && s <= 12) issue_w4(s + 11);
        if constexpr (s == 21) {
          if (!__all(fs.x == ta && fs.y == tb)) poll_flags(fso, true, rws, ta, tb);
          issue_a(0, WS_SS + (m * 10 + j) * 16384);
        }
      });
      waitbar<0>();
    } else {
      row_stats();
      put_tag(W);
      const int q4 = li >> 2, p4 = l & 3;
      LAS char* const wb0 = pin(sm + W + (4 * g + q4) * 128 + ((((2 * cq) ^ ((2 * g + (q4 >> 1)) & 3))) << 5) + 8 * p4);
      LAS char* const wb1 = pin(sm + W + (4 * g + q4) * 128 + ((((2 * cq + 1) ^ ((2 * g + (q4 >> 1)) & 3))) << 5) + 8 * p4);
      LAS char* const wh0 = pin(wb0 + 65536);
      LAS char* const wh1 = pin(wb1 + 65536);
      LAS char* const ab = pin(sm + ACT + tq * 4096 + l * 16);
      struct F64 { bf16x8 a0, a1, b0, b1; };
      F64 F0, F1;
      f32x4 acc[2][2], cs[2];
      u32x2 xs[2][2];
      auto step64 = [&](auto mf, auto tts, F64& R, LAS char* pb, u32 wo, const F64& M) AINL {
        LAS char* const p0 = wo >= 65536 ? wh0 + (wo - 65536) : wb0 + wo;
        LAS char* const p1 = wo >= 65536 ? wh1 + (wo - 65536) : wb1 + wo;
        R.a0 = ldstr(p0, p0 + 2048); SB();
        if constexpr (mf.v) acc[0][0] = mfma(M.a0, M.b0, acc[0][0]);
        SB(); R.b0 = lds128(pb); R.b1 = lds128(pb + tts.v); SB();
        if constexpr (mf.v) acc[1][0] = mfma(M.a1, M.b0, acc[1][0]);
        SB(); R.a1 = ldstr(p1, p1 + 2048); SB();
        if constexpr (mf.v) { acc[0][1] = mfma(M.a0, M.b1, acc[0][1]); acc[1][1] = mfma(M.a1, M.b1, acc[1][1]); }
        SB();
      };
      auto mm = [&](const F64& M) AINL {
        acc[0][0] = mfma(M.a0, M.b0, acc[0][0]); acc[1][0] = mfma(M.a1, M.b0, acc[1][0]);
        acc[0][1] = mfma(M.a0, M.b1, acc[0][1]); acc[1][1] = mfma(M.a1, M.b1, acc[1][1]);
      };
      float mu[2], rs[2];
      {
        LAS char* const xb0 = pin(sm + ACT + tq * 4096 + rb0);
        LAS char* const xb1 = pin(sm + ACT + tq * 4096 + (rb0 ^ 64));
#pragma unroll
        for (int a = 0; a < 2; ++a) { acc[a][0] = zero4; acc[a][1] = zero4; }
        const int s_own = wrapk(j - rot1 + 10, 10);
        LAS char* const xo0 = pin(sm + ACT + tq * 4096 + li * 128 + (((4 * cq + (g >> 1)) ^ (li >> 1)) << 4) + 8 * (g & 1));
        LAS char* const xo1 = pin(sm + ACT + tq * 4096 + li * 128 + (((4 * cq + 2 + (g >> 1)) ^ (li >> 1)) << 4) + 8 * (g & 1));
        u32 wdum = 0;
        if (WARM && w == 7) touch(wdum, l < 40 ? ((m * 10 + j) * 40 + l) * 128 : OOB, mk_rsrc(w2, 640 * 1280));
        STAMP(1);
        sfor<0, 10>([&](auto ic) AINL {
          constexpr int s = ic.v;
          constexpr u32 so = (s & 3) * 16384, wo = 65536 + (s & 3) * 8192;
          bar();
          if constexpr (s == 0) STAMP(16);
          if constexpr (s == 3) STAMP(20);
          if constexpr (s == 6) STAMP(21);
          step64(IC<(s > 0)>{}, IC<2048>{}, F0, xb0 + so, wo, F1);
          step64(IC<1>{}, IC<2048>{}, F1, xb1 + so, wo + 4096, F0);
          if (s == s_own) {
#pragma unroll
            for (int tt = 0; tt < 2; ++tt)
#pragma unroll
              for (int ct = 0; ct < 2; ++ct) xs[ct][tt] = *(LAS u32x2*)((ct ? xo1 : xo0) + so + tt * 2048);
          }
        });
        mm(F1);
        bar();
        STAMP(2);
        u32x4 g0 = *(LAS u32x4*)(sm + ACT + 2 * 16384 + (tq * 32 + li) * 16);
        u32x4 g1 = *(LAS u32x4*)(sm + ACT + 2 * 16384 + (tq * 32 + 16 + li) * 16);
        cs[0] = *(LAS f32x4*)(sm + ACT + 2 * 16384 + 4096 + cq * 128 + g * 16);
        cs[1] = *(LAS f32x4*)(sm + ACT + 2 * 16384 + 4096 + cq * 128 + 64 + g * 16);
        if (!__all(g0.y == ta && g0.w == tb && g1.y == ta && g1.w == tb)) {
          g0 = poll_granule(WS_XS + (m * 128 + tq * 32 + li) * 16, true, rws, ta, tb);
          g1 = poll_granule(WS_XS + (m * 128 + tq * 32 + 16 + li) * 16, true, rws, ta, tb);
        }
        mu[0] = asf(g0.x); rs[0] = asf(g0.z); mu[1] = asf(g1.x); rs[1] = asf(g1.z);
#pragma unroll
        for (int tt = 0; tt < 2; ++tt) {
          const float r2 = rs[tt] * __builtin_amdgcn_rsqf(1.0f - LN_EPS * rs[tt] * rs[tt] + LN_EPS);
          f32x4 h[2];
#pragma unroll
          for (int ct = 0; ct < 2; ++ct)
#pragma unroll
            for (int e = 0; e < 4; ++e) h[ct][e] = gelu_f(r2 * __builtin_fmaf(-mu[tt], cs[ct][e], acc[ct][tt][e]));
          st128_sc1(pack8(h[0], h[1]), WS_H1 + ((2 * j + cq) * 4 + g) * 32768 + (m * 128 + tq * 32 + tt * 16 + li) * 16, rws);
          SB();
        }
        STAMP(3);
        asm volatile("s_waitcnt vmcnt(0) lgkmcnt(0)\n\ts_barrier" : "+v"(wdum)::"memory");
      }
      STAMP(4);
      bar();
      STAMP(5);
      LAS char* const abt = pin(sm + ACT + g * 2048 + (tq * 32 + li) * 16);
#pragma unroll
      for (int a = 0; a < 2; ++a) { acc[a][0] = zero4; acc[a][1] = zero4; }
      sfor<0, 10>([&](auto ic) AINL {
        constexpr int s = ic.v;
        bar();
        if constexpr (s == 0) STAMP(17);
        if constexpr (s == 3) STAMP(27);
        if constexpr (s == 6) STAMP(28);
        step64(IC<(s > 0)>{}, IC<256>{}, F0, abt + (s & 3) * 16384, s * 8192, F1);
        step64(IC<1>{}, IC<256>{}, F1, abt + (s & 3) * 16384 + 8192, s * 8192 + 4096, F0);
      });
      bar();
      mm(F1);
      STAMP(6);
      u32x4 x1p[2];
      {
        float sm1[2], sq1[2];
#pragma unroll
        for (int tt = 0; tt < 2; ++tt) {
          float s1 = 0.f, s2 = 0.f;
          f32x4 x1[2];
#pragma unroll
          for (int ct = 0; ct < 2; ++ct) {
            const float xv[4] = {bflo(xs[ct][tt].x), bfhi(xs[ct][tt].x), bflo(xs[ct][tt].y), bfhi(xs[ct][tt].y)};
#pragma unroll
            for (int e = 0; e < 4; ++e) {
              const float v = __builtin_fmaf(xv[e] - mu[tt], rs[tt], acc[ct][tt][e]);
              x1[ct][e] = v; s1 += v; s2 = __builtin_fmaf(v, v, s2);
            }
          }
          x1p[tt] = pack8(x1[0], x1[1]);
          st128_sc1(x1p[tt], WS_X1 + (m * 10 + j) * 16384 + (2 * tq + tt) * 2048 + cq * 1024 + l * 16, rws);
          sm1[tt] = sum32(sum16(s1)); sq1[tt] = sum32(sum16(s2));
        }
        const u32x2 st = {asu(g & 1 ? sm1[1] : sm1[0]), asu(g & 1 ? sq1[1] : sq1[0])};
        st64_sc1(st, l < 32 ? WS_ST1 + m * 20480 + (2 * j + cq) * 1024 + (tq * 32 + l) * 8 : OOB, rws);
      }
      STAMP(7);
      waitbar<0>();
      STAMP(8);
      bar();
      STAMP(9);
      {
        const int sg = (g & 1) * 32;
        LAS char* const bA = pin(sm + W + (4 * g + q4) * 320 + 8 * p4 + 160 * cq + (cq ? -sg : sg));
        LAS char* const bB = pin(sm + W + (4 * g + q4) * 320 + 8 * p4 + 160 * cq + (cq ? sg : -sg));
        LAS char* const bA2 = pin(bA + 40960);
        LAS char* const bB2 = pin(bB + 40960);
        struct F3 { bf16x8 a[5], b0, b1; };
        F3 G0, G1;
        f32x4 a3[5][2];
        auto step3 = [&](auto mf, F3& R, u32 so, u32 wo, const F3& M) AINL {
          LAS char* const pA = wo >= 40960 ? bA2 + (wo - 40960) : bA + wo;
          LAS char* const pB = wo >= 40960 ? bB2 + (wo - 40960) : bB + wo;
          R.a[0] = ldstr(pA, pA + 5120); SB();
          if constexpr (mf.v) a3[0][0] = mfma(M.a[0], M.b0, a3[0][0]);
          SB(); R.b0 = lds128(ab + so); R.b1 = lds128(ab + so + 2048); SB();
          if constexpr (mf.v) a3[0][1] = mfma(M.a[0], M.b1, a3[0][1]);
          SB();
          sfor<1, 5>([&](auto ii) AINL {
            constexpr int i = ii.v;
            LAS char* const pw = ((i & 1) ? pB : pA) + 32 * i;
            R.a[i] = ldstr(pw, pw + 5120); SB();
            if constexpr (mf.v) a3[(i + 1) >> 1][(i + 1) & 1] = mfma(M.a[(i + 1) >> 1], ((i + 1) & 1) ? M.b1 : M.b0, a3[(i + 1) >> 1][(i + 1) & 1]);
            SB();
          });
          if constexpr (mf.v) {
            a3[3][0] = mfma(M.a[3], M.b0, a3[3][0]); a3[3][1] = mfma(M.a[3], M.b1, a3[3][1]);
            a3[4][0] = mfma(M.a[4], M.b0, a3[4][0]); a3[4][1] = mfma(M.a[4], M.b1, a3[4][1]);
          }
          SB();
        };
#pragma unroll
        for (int i = 0; i < 5; ++i) { a3[i][0] = zero4; a3[i][1] = zero4; }
        sfor<0, 10>([&](auto ic) AINL {
          constexpr int s = ic.v;
          constexpr u32 so = (s & 3) * 16384, wo = (s & 3) * 20480;
          bar();
          if constexpr (s == 0) STAMP(18);
          if constexpr (s == 3) STAMP(25);
          step3(IC<(s > 0)>{}, G0, so, wo, G1);
          step3(IC<1>{}, G1, so + 1024, wo + 10240, G0);
        });
#pragma unroll
        for (int i = 0; i < 5; ++i) { a3[i][0] = mfma(G1.a[i], G1.b0, a3[i][0]); a3[i][1] = mfma(G1.a[i], G1.b1, a3[i][1]); }
        bar();
        STAMP(10);
        float m1[2], r1[2];
#pragma unroll
        for (int tt = 0; tt < 2; ++tt) {
          float s1 = 0.f, s2 = 0.f;
#pragma unroll
          for (int i = 0; i < 5; ++i) {
            const f32x2 v = *(LAS f32x2*)(sm + ACT + 2 * 16384 + (g + 4 * i) * 1024 + (tq * 32 + tt * 16 + li) * 8);
            s1 += v.x; s2 += v.y;
          }
          s1 = sum32(sum16(s1)) * (1.0f / 640.0f); s2 = sum32(sum16(s2)) * (1.0f / 640.0f);
          m1[tt] = s1; r1[tt] = __builtin_amdgcn_rsqf(fmaxf(__builtin_fmaf(-s1, s1, s2), 0.f) + LN_EPS);
        }
        f32x4 c3[5];
#pragma unroll
        for (int i = 0; i < 5; ++i) c3[i] = *(LAS f32x4*)(sm + ACT + 3 * 16384 + 4096 + (80 * cq + 16 * i + 4 * g) * 4);
        auto act4 = [&](int i, int tt) AINL {
          f32x4 h;
#pragma unroll
          for (int e = 0; e < 4; ++e) h[e] = gelu_f(r1[tt] * __builtin_fmaf(-m1[tt], c3[i][e], a3[i][tt][e]));
          return pack4(h);
        };
        auto put = [&](u32x2 lo, u32x2 hi, int b, int tt) AINL {
          st128_sc1((u32x4){lo.x, lo.y, hi.x, hi.y}, b < 48 ? WS_H2 + (m * 24 + (b >> 1)) * 16384 + (2 * tq + tt) * 2048 + (b & 1) * 1024 + l * 16 : OOB, rws);
          SB();
        };
        LAS char* const xch = sm + ACT + 3 * 16384 + 8192 + tq * 1024 + l * 8;
        if (cq == 0) {
          *(LAS u32x2*)(xch + 512) = act4(4, 1);
          put(act4(0, 0), act4(1, 0), 5 * j, 0); put(act4(2, 0), act4(3, 0), 5 * j + 1, 0);
          bar();
          put(act4(4, 0), *(LAS u32x2*)xch, 5 * j + 2, 0);
          put(act4(0, 1), act4(1, 1), 5 * j, 1); put(act4(2, 1), act4(3, 1), 5 * j + 1, 1);
        } else {
          *(LAS u32x2*)xch = act4(0, 0);
          put(act4(1, 0), act4(2, 0), 5 * j + 3, 0); put(act4(3, 0), act4(4, 0), 5 * j + 4, 0);
          bar();
          put(*(LAS u32x2*)(xch + 512), act4(0, 1), 5 * j + 2, 1);
          put(act4(1, 1), act4(2, 1), 5 * j + 3, 1); put(act4(3, 1), act4(4, 1), 5 * j + 4, 1);
        }
      }
      STAMP(11);
      waitbar<0>();
      STAMP(12);
      const rsrc_t rdk = mk_rsrc(dskip, 1280);
      const u32x2 dk0 = bld64((cq * 32 + 4 * g) * 2, rdk, j * 128), dk1 = bld64((cq * 32 + 16 + 4 * g) * 2, rdk, j * 128);
      bar();
      STAMP(13);
#pragma unroll
      for (int a = 0; a < 2; ++a) { acc[a][0] = zero4; acc[a][1] = zero4; }
      sfor<0, 24>([&](auto ic) AINL {
        constexpr int s = ic.v;
        bar();
        if constexpr (s == 0) STAMP(19);
        if constexpr (s == 6) STAMP(22);
        if constexpr (s == 12) STAMP(23);
        if constexpr (s == 18) STAMP(24);
        step64(IC<(s > 0)>{}, IC<2048>{}, F0, ab + (s & 3) * 16384, ((s + 5) % 12) * 8192, F1);
        step64(IC<1>{}, IC<2048>{}, F1, ab + (s & 3) * 16384 + 1024, ((s + 5) % 12) * 8192 + 4096, F0);
      });
      mm(F1);
      bar();
      STAMP(14);
      const rsrc_t rout = mk_rsrc(out, 2048 * 2560);
#pragma unroll
      for (int tt = 0; tt < 2; ++tt) {
        const u32x4 sv = *(LAS u32x4*)(sm + ACT + w * 2048 + tt * 1024 + l * 16);
#pragma unroll
        for (int ct = 0; ct < 2; ++ct) {
          const float xv[4] = {bflo(xs[ct][tt].x), bfhi(xs[ct][tt].x), bflo(xs[ct][tt].y), bfhi(xs[ct][tt].y)};
          const u32 s0 = ct ? sv.z : sv.x, s1 = ct ? sv.w : sv.y;
          const float sy[4] = {bflo(s0), bfhi(s0), bflo(s1), bfhi(s1)};
          const u32 y0 = ct ? x1p[tt].z : x1p[tt].x, y1 = ct ? x1p[tt].w : x1p[tt].y;
          const float x1[4] = {bflo(y0), bfhi(y0), bflo(y1), bfhi(y1)};
          const u32x2 dk = ct ? dk1 : dk0;
          const float dv[4] = {bflo(dk.x), bfhi(dk.x), bflo(dk.y), bfhi(dk.y)};
          u32x4 o;
#pragma unroll
          for (int e = 0; e < 4; ++e) {
            const float sfull = __builtin_fmaf((xv[e] - mu[tt]) * rs[tt], dv[e], sy[e]);
            o[e] = asu(__builtin_fmaf(sfull, silu_f(x1[e] + acc[ct][tt][e]), xv[e]));
          }
          __builtin_amdgcn_raw_buffer_store_b128(o, rout, (m * 128 + tq * 32 + tt * 16 + li) * 2560 + (j * 64 + cq * 32 + ct * 16 + 4 * g) * 4, 0, 0);
        }
      }
      STAMP(15); CSTAMP(31);
    }
  } else if (role < 15) {
    const int a = role - 10;
    constexpr u32 MISC = 147456, CST = 157696, TAG = 159744;
    if (w >= 8) {
      const rsrc_t rbr = mk_rsrc(b_re, 640 * 1280), rbi = mk_rsrc(b_im, 640 * 1280);
      const rsrc_t rcr = mk_rsrc(c_re, 640 * 1280), rci = mk_rsrc(c_im, 640 * 1280);
      const u32 vx0 = (m * 128 + 32 * (r8 >> 2) + 8 * L + (r8 & 3)) * 1280 + (((l & 7) ^ g) << 4);
      const u32 vx1 = (m * 128 + 32 * (2 + (r8 >> 2)) + 8 * L + (r8 & 3)) * 1280 + (((l & 7) ^ (4 + g)) << 4) - 1024;
      const u32 vb0 = (a * 128 + L * 32 + r8) * 1280 + (((l & 7) ^ g) << 4);
      const u32 vb1 = (a * 128 + L * 32 + 8 + r8) * 1280 + (((l & 7) ^ (4 + g)) << 4) - 1024;
      const u32 ldsw = L0 + L * 4096;
      auto issue_nk = [&](u32 d, rsrc_t r, u32 so) AINL { dma2<0>(d, vb0, vb1, r, so); dma2<0>(d + 2048, vb0, vb1, r, so + 20480); };
      auto issue1 = [&](int s) AINL {
        const int kt = wrapk(s + rotA1, 10);
        const u32 d = ldsw + (s % 3) * 49152;
        dma2<0>(d, vx0, vx1, rx, kt * 128); dma2<0>(d + 2048, vx0, vx1, rx, kt * 128 + 5120);
        issue_nk(d + 16384, rbr, kt * 128); issue_nk(d + 32768, rbi, kt * 128);
      };
      auto issue_c = [&](int s) AINL {
        const int kt = wrapk(s + rotA3, 20);
        issue_nk(ldsw + 49152 + (s % 6) * 16384, kt < 10 ? rcr : rci, wrapk(kt, 10) * 128);
      };
      const u32 va = L * 4096 + l * 16;
      auto issue_h = [&](int s) AINL {
        dma4<1>(ldsw + ((s & 3) == 3 ? MISC : (s & 3) * 16384), va, rws, WS_HH + (m * 20 + wrapk(s + rotA3, 20)) * 16384);
      };
      issue1(0); issue1(1);
      sfor<0, 10>([&](auto ic) AINL {
        constexpr int s = ic.v;
        waitbar<(s == 9 ? 13 : 12)>();
        if constexpr (s + 2 < 10) issue1(s + 2);
        else if constexpr (s == 8) { dma1<1>(L0 + MISC + L * 1024, (L & 1) * 1024 + l * 16, rws, L < 2 ? WS_XS + m * 2048 : WS_RS + a * 2048); issue_c(0); issue_c(1); issue_c(2); }
        else { issue_c(3); issue_c(4); issue_c(5); }
        if constexpr (s == 0) get_tag(TAG);
      });
      waitbar<24>();
      bar(); bar();
      await(FL_A + m * 5 + a, FL_A + m * 5 + l, l < 5, [&]() AINL { issue_h(0); issue_h(1); issue_h(2); });
      sfor<0, 20>([&](auto ic) AINL {
        constexpr int s = ic.v;
        constexpr int gs[23] = {4, 4, 4, 4, 8, 8, 8, 8, 8, 8, 8, 8, 8, 8, 8, 8, 8, 8, 4, 4, 0, 0, 0};
        waitbar<gs[s + 1] + gs[s + 2]>();
        if constexpr (s + 3 < 20) issue_h(s + 3);
        if constexpr (s >= 1 && s + 5 < 20) issue_c(s + 5);
      });
    } else {
      const int p = a * 128 + w * 16 + li;
      const u32 plr = bld16(p * 2, mk_rsrc(lam_re, 1280)), pli = bld16(p * 2, mk_rsrc(lam_im, 1280)), pdt = bld16(p * 2, mk_rsrc(log_dt, 1280));
      row_stats();
      put_tag(TAG);
      float lbr, lbi, kr, ki;
      {
        const float Lr = bflo(plr), Li = bflo(pli);
        const float dt = bflo(pk2(expf(bflo(pdt)), 0.f));
        const float ea = expf(dt * Lr);
        float sn, cn; sincosf(dt * Li, &sn, &cn);
        lbr = ea * cn; lbi = ea * sn;
        const float inv = 2.0f / (Lr * Lr + Li * Li);
        kr = ((lbr - 1.0f) * Lr + lbi * Li) * inv;
        ki = (lbi * Lr - (lbr - 1.0f) * Li) * inv;
        asm volatile("" : "+v"(lbr), "+v"(lbi), "+v"(kr), "+v"(ki));
      }
      f32x4 are[8], aim[8];
      u32 wdum = 0;
      if (WARM && w >= 4) touch(wdum, l < 40 ? ((m * 5 + a) * 80 + (w & 1) * 40 + l) * 128 : OOB, mk_rsrc(w >= 6 ? c_im : c_re, 640 * 1280));
#pragma unroll
      for (int i = 0; i < 8; ++i) { are[i] = zero4; aim[i] = zero4; }
      {
        struct FW { bf16x8 br, bi; };
        struct FX { bf16x8 x[4]; };
        FW W0, W1; FX Xa, Xb;
        auto half = [&](auto mf, auto rw, auto hh, FW& RW, LAS char* pw, FX& RX, LAS char* px, const FW& MW, const FX& MX) AINL {
          constexpr int h = hh.v;
          if constexpr (rw.v) { RW.br = lds128(pw); RX.x[0] = lds128(px); } else { RX.x[0] = lds128(px); RX.x[1] = lds128(px + 2048); }
          SB();
          if constexpr (mf.v) are[4 * h] = mfma(MX.x[0], MW.br, are[4 * h]);
          SB();
          if constexpr (rw.v) { RW.bi = lds128(pw + 16384); RX.x[1] = lds128(px + 2048); } else { RX.x[2] = lds128(px + 4096); RX.x[3] = lds128(px + 6144); }
          SB();
          if constexpr (mf.v) aim[4 * h] = mfma(MX.x[0], MW.bi, aim[4 * h]);
          SB();
          if constexpr (rw.v) { RX.x[2] = lds128(px + 4096); RX.x[3] = lds128(px + 6144); }
          SB();
          if constexpr (mf.v) {
#pragma unroll
            for (int i = 1; i < 4; ++i) { are[4 * h + i] = mfma(MX.x[i], MW.br, are[4 * h + i]); aim[4 * h + i] = mfma(MX.x[i], MW.bi, aim[4 * h + i]); }
          }
          SB();
        };
        STAMP(1);
        sfor<0, 10>([&](auto ic) AINL {
          constexpr int s = ic.v;
          LAS char* const x0 = pin(sm + rb0 + (s % 3) * 49152);
          LAS char* const x1 = pin(sm + (rb0 ^ 64) + (s % 3) * 49152);
          LAS char* const w0 = pin(x0 + w * 2048 + 16384);
          LAS char* const w1_ = pin(x1 + w * 2048 + 16384);
          bar();
          if constexpr (s == 0) STAMP(16);
          half(IC<(s > 0)>{}, IC<1>{}, IC<1>{}, W0, w0, Xa, x0, W1, Xb);
          half(IC<1>{}, IC<0>{}, IC<0>{}, W0, w0, Xb, x0 + 8192, W0, Xa);
          half(IC<1>{}, IC<1>{}, IC<1>{}, W1, w1_, Xa, x1, W0, Xb);
          half(IC<1>{}, IC<0>{}, IC<0>{}, W1, w1_, Xb, x1 + 8192, W1, Xa);
        });
#pragma unroll
        for (int i = 0; i < 4; ++i) { are[4 + i] = mfma(Xb.x[i], W1.br, are[4 + i]); aim[4 + i] = mfma(Xb.x[i], W1.bi, aim[4 + i]); }
      }
      bar();
      STAMP(2);
      if (w < 2) {
        u32x4 g0 = *(LAS u32x4*)(sm + MISC + tid * 16);
        if (!__all(g0.y == ta && g0.w == tb)) g0 = poll_granule(WS_XS + (m * 128 + tid) * 16, true, rws, ta, tb);
        const float rs = asf(g0.z);
        *(LAS f32x2*)(sm + CST + (tid >> 5) * 272 + (tid & 31) * 8) = (f32x2){rs, -rs * asf(g0.x)};
      }
      u32x4 rg = *(LAS u32x4*)(sm + MISC + 2048 + (w * 16 + li) * 16);
      if (!__all(rg.y == ta && rg.w == tb)) rg = poll_granule(WS_RS + p * 16, true, rws, ta, tb);
      const float sre = asf(rg.x), sim = asf(rg.z);
      asm volatile("s_waitcnt vmcnt(0) lgkmcnt(0)\n\ts_barrier" : "+v"(wdum)::"memory");
#pragma unroll
      for (int mt = 0; mt < 8; ++mt) {
        const f32x4 s0 = *(LAS f32x4*)(sm + CST + g * 272 + mt * 32), s1 = *(LAS f32x4*)(sm + CST + g * 272 + mt * 32 + 16);
        const float rv[4] = {s0.x, s0.z, s1.x, s1.z}, nv[4] = {s0.y, s0.w, s1.y, s1.w};
#pragma unroll
        for (int e = 0; e < 4; ++e) {
          are[mt][e] = __builtin_fmaf(rv[e], are[mt][e], nv[e] * sre);
          aim[mt][e] = __builtin_fmaf(rv[e], aim[mt][e], nv[e] * sim);
        }
      }
      bar();
      float hr = 0.f, hi = 0.f;
#pragma unroll
      for (int mt = 0; mt < 8; ++mt)
#pragma unroll
        for (int e = 0; e < 4; ++e) {
          const float nr = __builtin_fmaf(-lbi, hi, __builtin_fmaf(lbr, hr, are[mt][e]));
          const float ni = __builtin_fmaf(lbi, hr, __builtin_fmaf(lbr, hi, aim[mt][e]));
          hr = nr; hi = ni;
        }
      float er[4], ei[4];
      gather4(hr, er); gather4(hi, ei);
      float p32r = lbr, p32i = lbi;
#pragma unroll
      for (int i = 0; i < 5; ++i) { const float t = p32r * p32r - p32i * p32i; p32i = 2.0f * p32r * p32i; p32r = t; }
      float cr[5], ci[5];
      cr[0] = 0.f; ci[0] = 0.f;
#pragma unroll
      for (int q = 0; q < 4; ++q) {
        cr[q + 1] = __builtin_fmaf(-p32i, ci[q], __builtin_fmaf(p32r, cr[q], er[q]));
        ci[q + 1] = __builtin_fmaf(p32i, cr[q], __builtin_fmaf(p32r, ci[q], ei[q]));
      }
      st128_sc1((u32x4){asu(cr[4]), ta, asu(ci[4]), tb}, l < 16 ? WS_EG + (m * 640 + p) * 16 : OOB, rws);
      STAMP(3);
      const float p64r = p32r * p32r - p32i * p32i, p64i = 2.0f * p32r * p32i;
      const float Pr = p64r * p64r - p64i * p64i, Pi = 2.0f * p64r * p64i;
#pragma unroll
      for (int mt = 0; mt < 8; ++mt) {
#pragma unroll
        for (int e = 0; e < 4; ++e) {
          const float ur = are[mt][e], ui = aim[mt][e];
          are[mt][e] = __builtin_fmaf(kr, ur, -ki * ui);
          aim[mt][e] = __builtin_fmaf(-kr, ui, -ki * ur);
        }
        are[mt] = keep(are[mt]); aim[mt] = keep(aim[mt]);
      }
      float Hr = 0.f, Hi = 0.f;
      if (m > 0) {
        u32x4 e0, e1, e2, e3;
        const int m0 = m - 1 - 4 * g;
        const u32 o0 = WS_EG + (m0 * 640 + p) * 16;
        const bool v0 = m0 >= 0, v1 = m0 >= 1, v2 = m0 >= 2, v3 = m0 >= 3;
        _Pragma("nounroll") for (int it = 0; it < SPIN_CAP; ++it) {
          asm volatile("buffer_load_dwordx4 %0, %4, %8, 0 offen sc1\n\tbuffer_load_dwordx4 %1, %5, %8, 0 offen sc1\n\t"
                       "buffer_load_dwordx4 %2, %6, %8, 0 offen sc1\n\tbuffer_load_dwordx4 %3, %7, %8, 0 offen sc1\n\ts_waitcnt vmcnt(0)"
                       : "=&v"(e0), "=&v"(e1), "=&v"(e2), "=&v"(e3)
                       : "v"(v0 ? o0 : OOB), "v"(v1 ? o0 - 10240 : OOB), "v"(v2 ? o0 - 20480 : OOB), "v"(v3 ? o0 - 30720 : OOB), "s"(rws) : "memory");
          const bool ok = (!v0 || (e0.y == ta && e0.w == tb)) && (!v1 || (e1.y == ta && e1.w == tb)) &&
                          (!v2 || (e2.y == ta && e2.w == tb)) && (!v3 || (e3.y == ta && e3.w == tb));
          if (__all(ok)) break;
          __builtin_amdgcn_s_sleep(1);
        }
        float qr = asf(e3.x), qi = asf(e3.z), t;
        t = __builtin_fmaf(-Pi, qi, __builtin_fmaf(Pr, qr, asf(e2.x))); qi = __builtin_fmaf(Pi, qr, __builtin_fmaf(Pr, qi, asf(e2.z))); qr = t;
        t = __builtin_fmaf(-Pi, qi, __builtin_fmaf(Pr, qr, asf(e1.x))); qi = __builtin_fmaf(Pi, qr, __builtin_fmaf(Pr, qi, asf(e1.z))); qr = t;
        t = __builtin_fmaf(-Pi, qi, __builtin_fmaf(Pr, qr, asf(e0.x))); qi = __builtin_fmaf(Pi, qr, __builtin_fmaf(Pr, qi, asf(e0.z))); qr = t;
        float gr[4], gi[4];
        gather4(qr, gr); gather4(qi, gi);
        const float P2r = Pr * Pr - Pi * Pi, P2i = 2.0f * Pr * Pi;
        const float P4r = P2r * P2r - P2i * P2i, P4i = 2.0f * P2r * P2i;
        Hr = gr[3]; Hi = gi[3];
#pragma unroll
        for (int q = 2; q >= 0; --q) {
          t = __builtin_fmaf(-P4i, Hi, __builtin_fmaf(P4r, Hr, gr[q])); Hi = __builtin_fmaf(P4i, Hr, __builtin_fmaf(P4r, Hi, gi[q])); Hr = t;
        }
      }
      STAMP(4);
      {
        const float p96r = p64r * p32r - p64i * p32i, p96i = p64r * p32i + p64i * p32r;
        const float fr = g == 0 ? 1.f : g == 1 ? p32r : g == 2 ? p64r : p96r;
        const float fi = g == 0 ? 0.f : g == 1 ? p32i : g == 2 ? p64i : p96i;
        const float c0r = g == 0 ? cr[0] : g == 1 ? cr[1] : g == 2 ? cr[2] : cr[3];
        const float c0i = g == 0 ? ci[0] : g == 1 ? ci[1] : g == 2 ? ci[2] : ci[3];
        const float sr = __builtin_fmaf(-fi, Hi, __builtin_fmaf(fr, Hr, c0r)), si = __builtin_fmaf(fi, Hr, __builtin_fmaf(fr, Hi, c0i));
        hr = __builtin_fmaf(kr, sr, -ki * si);
        hi = __builtin_fmaf(-kr, si, -ki * sr);
      }
#pragma unroll
      for (int mt = 0; mt < 8; ++mt)
#pragma unroll
        for (int e = 0; e < 4; ++e) {
          const float nr = __builtin_fmaf(lbi, hi, __builtin_fmaf(lbr, hr, are[mt][e]));
          const float ni = __builtin_fmaf(-lbi, hr, __builtin_fmaf(lbr, hi, aim[mt][e]));
          hr = nr; hi = ni;
          are[mt][e] = hr; aim[mt][e] = hi;
        }
      {
        LAS char* const tb_ = sm + (w < 6 ? w * 8192 : MISC + (w - 6) * 8192);
        const int swc = ((li & 3) << 1) | ((li >> 2) & 1) | ((li >> 3) << 3);
#pragma unroll
        for (int i = 0; i < 4; ++i) {
          *(LAS u32x4*)(tb_ + li * 256 + (((4 * g + i) ^ swc) << 4)) = pack8(are[2 * i], are[2 * i + 1]);
          *(LAS u32x4*)(tb_ + 4096 + li * 256 + (((4 * g + i) ^ swc) << 4)) = pack8(aim[2 * i], aim[2 * i + 1]);
        }
        asm volatile("s_waitcnt lgkmcnt(0)" ::: "memory");
        const int q4 = li >> 2, p4 = l & 3;
        const int c0 = 8 * (g & 1) + q4, c1 = c0 + 4;
        const int sw0 = ((c0 & 3) << 1) | ((c0 >> 3) << 3), sw1 = sw0 | 1;
#pragma unroll
        for (int part = 0; part < 2; ++part)
#pragma unroll
          for (int u = 0; u < 4; ++u) {
            const int tt = 2 * u + (g >> 1), ch = 2 * tt + (p4 >> 1);
            const bf16x8 v = ldstr(tb_ + part * 4096 + c0 * 256 + ((ch ^ sw0) << 4) + 8 * (p4 & 1),
                                   tb_ + part * 4096 + c1 * 256 + ((ch ^ sw1) << 4) + 8 * (p4 & 1));
            const u32 off = WS_HH + (m * 20 + 10 * part + 2 * a + (w >> 2)) * 16384 + tt * 2048 + ((w >> 1) & 1) * 1024 + ((2 * (w & 1) + (g & 1)) * 16 + li) * 16;
            st128_sc1(__builtin_bit_cast(u32x4, v), off, rws);
          }
      }
      waitbar<0>();
      STAMP(5);
      bar();
      STAMP(6);
      f32x4 ac[4][2];
#pragma unroll
      for (int i = 0; i < 4; ++i) { ac[i][0] = zero4; ac[i][1] = zero4; }
      LAS char* const ab = pin(sm + tq * 4096 + l * 16);
      LAS char* const ab3 = pin(ab + MISC);
      LAS char* cb[2][2];
#pragma unroll
      for (int i = 0; i < 2; ++i) { cb[0][i] = pin(sm + 49152 + i * 49152 + cq * 8192 + rb0); cb[1][i] = pin(sm + 49152 + i * 49152 + cq * 8192 + (rb0 ^ 64)); }
      struct FC { bf16x8 c[4], b0, b1; };
      FC G0, G1;
      auto stepC = [&](auto mf, FC& R, int ks, int s, const FC& M) AINL {
        LAS char* const pa = ((s & 3) == 3 ? ab3 : ab + (s & 3) * 16384) + ks * 1024;
        LAS char* const pc = cb[ks][(s % 6) / 3] + ((s % 6) % 3) * 16384;
        R.b0 = lds128(pa); R.c[0] = lds128(pc); SB();
        if constexpr (mf.v) ac[0][0] = mfma(M.c[0], M.b0, ac[0][0]);
        SB(); R.b1 = lds128(pa + 2048); R.c[1] = lds128(pc + 2048); SB();
        if constexpr (mf.v) ac[0][1] = mfma(M.c[0], M.b1, ac[0][1]);
        SB(); R.c[2] = lds128(pc + 4096); R.c[3] = lds128(pc + 6144); SB();
        if constexpr (mf.v) {
#pragma unroll
          for (int ct = 1; ct < 4; ++ct) { ac[ct][0] = mfma(M.c[ct], M.b0, ac[ct][0]); ac[ct][1] = mfma(M.c[ct], M.b1, ac[ct][1]); }
        }
        SB();
      };
      sfor<0, 20>([&](auto ic) AINL {
        constexpr int s = ic.v;
        bar();
        if constexpr (s == 0) STAMP(17);
        stepC(IC<(s > 0)>{}, G0, 0, s, G1);
        stepC(IC<1>{}, G1, 1, s, G0);
      });
#pragma unroll
      for (int ct = 0; ct < 4; ++ct) { ac[ct][0] = mfma(G1.c[ct], G1.b0, ac[ct][0]); ac[ct][1] = mfma(G1.c[ct], G1.b1, ac[ct][1]); }
      STAMP(7);
#pragma unroll
      for (int cb_ = 0; cb_ < 2; ++cb_)
#pragma unroll
        for (int tt = 0; tt < 2; ++tt)
          st128_sc1(pack8(ac[2 * cb_][tt], ac[2 * cb_ + 1][tt]), WS_SS + ((m * 10 + 2 * a + cq) * 8 + tq + 4 * cb_) * 2048 + tt * 1024 + l * 16, rws);
      waitbar<0>();
      if (w == 0) set_flag(FL_S + m * 5 + a);
      STAMP(8);
    }
  } else if (w < 8) {
    row_stats();
    auto add8 = [&](float (&c)[8], u32x4 t) AINL {
      c[0] += bflo(t.x); c[1] += bfhi(t.x); c[2] += bflo(t.y); c[3] += bfhi(t.y); c[4] += bflo(t.z); c[5] += bfhi(t.z); c[6] += bflo(t.w); c[7] += bfhi(t.w);
    };
    STAMP(1);
    {
      const rsrc_t rr_ = mk_rsrc(b_re, 640 * 1280), ri_ = mk_rsrc(b_im, 640 * 1280);
      u32x4 t[10]; u32 u[10];
#pragma unroll
      for (int i = 0; i < 10; ++i) {
        const u32 so = (m * 40 + w * 5 + (i >> 1)) * 1280;
        t[i] = bld128(l * 16, (i & 1) ? ri_ : rr_, so); u[i] = bld32(1024 + l * 4, (i & 1) ? ri_ : rr_, so);
      }
      SB();
      float rsum[10];
#pragma unroll
      for (int i = 0; i < 10; ++i)
        rsum[i] = wavesum(((bflo(t[i].x) + bfhi(t[i].x)) + (bflo(t[i].y) + bfhi(t[i].y))) + ((bflo(t[i].z) + bfhi(t[i].z)) + (bflo(t[i].w) + bfhi(t[i].w))) + (bflo(u[i]) + bfhi(u[i])));
      const float vr = l == 0 ? rsum[0] : l == 1 ? rsum[2] : l == 2 ? rsum[4] : l == 3 ? rsum[6] : rsum[8];
      const float vi = l == 0 ? rsum[1] : l == 1 ? rsum[3] : l == 2 ? rsum[5] : l == 3 ? rsum[7] : rsum[9];
      st128_sc1((u32x4){asu(vr), ta, asu(vi), tb}, l < 5 ? WS_RS + (m * 40 + w * 5 + l) * 16 : OOB, rws);
    }
    if (m < 12) {
      const int c = m;
      const rsrc_t rw3 = mk_rsrc(w3, 640 * 3072);
      float cs[8] = {0.f, 0.f, 0.f, 0.f, 0.f, 0.f, 0.f, 0.f};
      const u32 vo = (tid >> 4) * 3072 + c * 256 + (tid & 15) * 16;
      u32x4 t[20];
#pragma unroll
      for (int i = 0; i < 20; ++i) t[i] = bld128(vo, rw3, i * 32 * 3072);
      SB();
#pragma unroll
      for (int i = 0; i < 20; ++i) add8(cs, t[i]);
#pragma unroll
      for (int e = 0; e < 8; ++e) cs[e] = sum32(sum16(cs[e]));
      if (l < 16) {
        *(LAS f32x4*)(sm + w * 512 + l * 32) = (f32x4){cs[0], cs[1], cs[2], cs[3]};
        *(LAS f32x4*)(sm + w * 512 + l * 32 + 16) = (f32x4){cs[4], cs[5], cs[6], cs[7]};
      }
      bar();
      if (w < 2) {
        float s = 0.f;
#pragma unroll
        for (int i = 0; i < 8; ++i) s += *(LAS float*)(sm + i * 512 + tid * 4);
        st32_sc1(asu(s), WS_CS3 + (c * 128 + tid) * 4, rws);
      }
      waitbar<0>();
      if (w == 0) set_flag(FL_C + c);
    }
    {
      const rsrc_t rw4 = mk_rsrc(w4, 1536 * 1280);
      u32 d = 0;
      if (m < 12) { touch(d, (m * 640 + tid) * 128, rw4); touch(d, tid < 128 ? (m * 640 + 512 + tid) * 128 : OOB, rw4); }
      else {
#pragma unroll
        for (int i = 0; i < 4; ++i) touch(d, i < 3 || tid < 384 ? (7680 + (m - 12) * 1920 + i * 512 + tid) * 128 : OOB, rw4);
      }
      asm volatile("s_waitcnt vmcnt(0)" : "+v"(d)::"memory");
    }
    STAMP(2);
    if (m == 0) {
#pragma unroll
      for (int i = 0; i < 4; ++i) (void)poll_granule(WS_XS + (i * 512 + tid) * 16, true, rws, ta, tb);
      bar();
      if (tid == 0) st64_sc1((u32x2){ta, tb}, WS_EPOCH, rws);
    }
  }
#endif
}

extern "C" void kernel_launch(void* const* d_in, const int* in_sizes, int n_in, void* d_out, int out_size,
                              void* d_ws, size_t ws_size, hipStream_t stream) {
  (void)in_sizes; (void)n_in; (void)out_size; (void)ws_size;
  s5_mixer_kernel<<<256, 768, 0, stream>>>(d_in[0], d_in[1], d_in[2], d_in[3], d_in[4], d_in[5], d_in[6], d_in[7],
                                           d_in[8], d_in[10], d_in[11], d_in[12], d_in[13], d_out, d_ws);
}
```

```cpp
#include <hip/hip_runtime.h>
#include <hip/hip_bf16.h>
#include <stdint.h>

#define DI __device__ __forceinline__
#define LAS __attribute__((address_space(3)))
#define AINL __attribute__((always_inline))
typedef unsigned u32;
typedef unsigned long long u64;
typedef u32 u32x4 __attribute__((ext_vector_type(4)));
typedef u32 u32x2 __attribute__((ext_vector_type(2)));
typedef float f32x4 __attribute__((ext_vector_type(4)));
typedef float f32x2 __attribute__((ext_vector_type(2)));
typedef __bf16 bf16x8 __attribute__((ext_vector_type(8)));
typedef __bf16 bf16x2 __attribute__((ext_vector_type(2)));
typedef short s16x4 __attribute__((ext_vector_type(4)));
typedef short s16x8 __attribute__((ext_vector_type(8)));

#define XMAP 1
#define ABL 0
#define PSLEEP 2
#define WARM 1
#define STAMPS 0

constexpr u32 WS_EPOCH = 0;
constexpr u32 WS_FLAG = 0x1000;
constexpr u32 FL_1 = 0, FL_2 = 160, FL_3 = 320, FL_A = 480, FL_S = 560, FL_C = 640;
constexpr u32 WS_XS = 0x20000;
constexpr u32 WS_CS3 = 0x2A000;
constexpr u32 WS_RS = 0x58000;
constexpr u32 WS_EG = 0x30000;
constexpr u32 WS_ST1 = 0x60000;
constexpr u32 WS_SS = 0xC0000;
constexpr u32 WS_H1 = 0x400000;
constexpr u32 WS_X1 = 0x680000;
constexpr u32 WS_HH = 0x900000;
constexpr u32 WS_H2 = 0xE00000;
constexpr u32 WS_STAMP = 0x1400000;
constexpr u32 WS_BYTES = 0x1500000;
constexpr u32 OOB = 0x7FFFFFF0u;
constexpr float LN_EPS = 1e-5f;
constexpr int SPIN_CAP = 1 << 17;

template <int I> struct IC { static constexpr int v = I; };
template <int B, int E, class F> DI void sfor(F&& f) {
  if constexpr (B < E) { f(IC<B>{}); sfor<B + 1, E>(f); }
}

#if defined(__HIP_DEVICE_COMPILE__) && __HIP_DEVICE_COMPILE__
typedef __amdgpu_buffer_rsrc_t rsrc_t;
DI rsrc_t mk_rsrc(const void* p, u32 bytes) { return __builtin_amdgcn_make_buffer_rsrc((void*)p, 0, bytes, 0x00020000); }

template <int SC1> DI void dma1(u32 lds, u32 voff, rsrc_t r, u32 soff) {
  if (ABL == 2) return;
  if constexpr (SC1)
    asm volatile("s_mov_b32 m0, %0\n\ts_nop 0\n\tbuffer_load_dwordx4 %1, %2, %3 offen sc1 lds" ::"s"(lds), "v"(voff), "s"(r), "s"(soff) : "memory");
  else
    asm volatile("s_mov_b32 m0, %0\n\ts_nop 0\n\tbuffer_load_dwordx4 %1, %2, %3 offen lds" ::"s"(lds), "v"(voff), "s"(r), "s"(soff) : "memory");
}
template <int SC1> DI void dma2(u32 lds, u32 v0, u32 v1, rsrc_t r, u32 soff) {
  if (ABL == 2) return;
  if constexpr (SC1)
    asm volatile("s_mov_b32 m0, %0\n\ts_nop 0\n\tbuffer_load_dwordx4 %1, %3, %4 offen sc1 lds\n\tbuffer_load_dwordx4 %2, %3, %4 offen offset:1024 sc1 lds"
                 ::"s"(lds), "v"(v0), "v"(v1), "s"(r), "s"(soff) : "memory");
  else
    asm volatile("s_mov_b32 m0, %0\n\ts_nop 0\n\tbuffer_load_dwordx4 %1, %3, %4 offen lds\n\tbuffer_load_dwordx4 %2, %3, %4 offen offset:1024 lds"
                 ::"s"(lds), "v"(v0), "v"(v1), "s"(r), "s"(soff) : "memory");
}
template <int SC1> DI void dma4(u32 lds, u32 v, rsrc_t r, u32 soff) {
  if (ABL == 2) return;
  if constexpr (SC1)
    asm volatile("s_mov_b32 m0, %0\n\ts_nop 0\n\tbuffer_load_dwordx4 %1, %2, %3 offen sc1 lds\n\tbuffer_load_dwordx4 %1, %2, %3 offen offset:1024 sc1 lds\n\t"
                 "buffer_load_dwordx4 %1, %2, %3 offen offset:2048 sc1 lds\n\tbuffer_load_dwordx4 %1, %2, %3 offen offset:3072 sc1 lds"
                 ::"s"(lds), "v"(v), "s"(r), "s"(soff) : "memory");
  else
    asm volatile("s_mov_b32 m0, %0\n\ts_nop 0\n\tbuffer_load_dwordx4 %1, %2, %3 offen lds\n\tbuffer_load_dwordx4 %1, %2, %3 offen offset:1024 lds\n\t"
                 "buffer_load_dwordx4 %1, %2, %3 offen offset:2048 lds\n\tbuffer_load_dwordx4 %1, %2, %3 offen offset:3072 lds"
                 ::"s"(lds), "v"(v), "s"(r), "s"(soff) : "memory");
}
constexpr int VC(int n) { return ABL == 2 ? 0 : n; }
template <int N> DI void waitv() { asm volatile("s_waitcnt vmcnt(%0)" ::"i"(VC(N)) : "memory"); }
DI void bar() { asm volatile("s_waitcnt lgkmcnt(0)\n\ts_barrier" ::: "memory"); }
template <int N> DI void waitbar() { asm volatile("s_waitcnt vmcnt(%0) lgkmcnt(0)\n\ts_barrier" ::"i"(VC(N)) : "memory"); }

DI void touch(u32& d, u32 voff, rsrc_t r) { asm volatile("buffer_load_dword %0, %1, %2, 0 offen" : "+v"(d) : "v"(voff), "s"(r) : "memory"); }

DI u32x4 bld128(u32 voff, rsrc_t r, u32 soff) { return __builtin_amdgcn_raw_buffer_load_b128(r, voff, soff, 0); }
DI u32x2 bld64(u32 voff, rsrc_t r, u32 soff) { return __builtin_amdgcn_raw_buffer_load_b64(r, voff, soff, 0); }
DI u32x2 bld64_sc1(u32 voff, rsrc_t r, u32 soff) { return __builtin_amdgcn_raw_buffer_load_b64(r, voff, soff, 16); }
DI u32 bld32(u32 voff, rsrc_t r, u32 soff) { return __builtin_amdgcn_raw_buffer_load_b32(r, voff, soff, 0); }
DI u32 bld16(u32 voff, rsrc_t r) { return __builtin_amdgcn_raw_buffer_load_b16(r, voff, 0, 0); }
DI void st128_sc1(u32x4 v, u32 voff, rsrc_t r) { __builtin_amdgcn_raw_buffer_store_b128(v, r, voff, 0, 16); }
DI void st64_sc1(u32x2 v, u32 voff, rsrc_t r) { __builtin_amdgcn_raw_buffer_store_b64(v, r, voff, 0, 16); }
DI void st32_sc1(u32 v, u32 voff, rsrc_t r) { __builtin_amdgcn_raw_buffer_store_b32(v, r, voff, 0, 16); }

DI void poll_flags(u32 voff, bool active, rsrc_t ws, u32 ta, u32 tb) {
  _Pragma("nounroll") for (int it = 0; it < SPIN_CAP; ++it) {
    u32x2 v;
    asm volatile("buffer_load_dwordx2 %0, %1, %2, 0 offen sc1\n\ts_waitcnt vmcnt(0)" : "=&v"(v) : "v"(voff), "s"(ws) : "memory");
    const bool ok = !active || (v.x == ta && v.y == tb);
    if (__all(ok)) break;
    __builtin_amdgcn_s_sleep(1);
  }
}
DI u32x4 poll_granule(u32 voff, bool active, rsrc_t ws, u32 ta, u32 tb) {
  u32x4 v;
  _Pragma("nounroll") for (int it = 0; it < SPIN_CAP; ++it) {
    asm volatile("buffer_load_dwordx4 %0, %1, %2, 0 offen sc1\n\ts_waitcnt vmcnt(0)" : "=&v"(v) : "v"(voff), "s"(ws) : "memory");
    const bool ok = !active || (v.y == ta && v.w == tb);
    if (__all(ok)) break;
    __builtin_amdgcn_s_sleep(1);
  }
  return v;
}

DI float asf(u32 x) { return __uint_as_float(x); }
DI u32 asu(float x) { return __float_as_uint(x); }
DI float bflo(u32 x) { return asf(x << 16); }
DI float bfhi(u32 x) { return asf(x & 0xffff0000u); }
DI u32 pk2(float a, float b) { f32x2 p = {a, b}; bf16x2 q = __builtin_convertvector(p, bf16x2); return __builtin_bit_cast(u32, q); }
DI float sum16(float x) { const u32 u = asu(x); auto r = __builtin_amdgcn_permlane16_swap(u, u, false, false); const u32 a = r[0], b = r[1]; return asf(a) + asf(b); }
DI float sum32(float x) { const u32 u = asu(x); auto r = __builtin_amdgcn_permlane32_swap(u, u, false, false); const u32 a = r[0], b = r[1]; return asf(a) + asf(b); }
template <int C> DI float dppadd(float x) { return x + asf(__builtin_amdgcn_update_dpp(0u, asu(x), C, 0xf, 0xf, true)); }
DI float wavesum(float x) { x = dppadd<0xB1>(x); x = dppadd<0x4E>(x); x = dppadd<0x141>(x); x = dppadd<0x140>(x); return sum32(sum16(x)); }
DI void gather4(float x, float (&e)[4]) {
  const u32 u = asu(x);
  auto p = __builtin_amdgcn_permlane16_swap(u, u, false, false);
  const u32 a = p[0], b = p[1];
  auto pa = __builtin_amdgcn_permlane32_swap(a, a, false, false);
  auto pb = __builtin_amdgcn_permlane32_swap(b, b, false, false);
  const u32 e0 = pa[0], e2 = pa[1], e1 = pb[0], e3 = pb[1];
  e[0] = asf(e0); e[1] = asf(e1); e[2] = asf(e2); e[3] = asf(e3);
}
DI float gelu_f(float v) {
  const float c1 = -2.302208198f, c2 = -0.1029432397f;
  const float e = __builtin_amdgcn_exp2f(v * __builtin_fmaf(v * v, c2, c1));
  return v * __builtin_amdgcn_rcpf(1.0f + e);
}
DI float silu_f(float z) { return z * __builtin_amdgcn_rcpf(1.0f + __builtin_amdgcn_exp2f(z * -1.4426950409f)); }
DI f32x4 mfma(bf16x8 a, bf16x8 b, f32x4 c) { if (ABL == 1 || ABL == 4) { asm volatile("" : "+v"(c) : "v"(a), "v"(b)); return c; } return __builtin_amdgcn_mfma_f32_16x16x32_bf16(a, b, c, 0, 0, 0); }
DI bf16x8 lds128(LAS char* p) { if (ABL == 1 || ABL == 3) { bf16x8 r; asm volatile("" : "=v"(r)); return r; } return *(LAS bf16x8*)p; }
DI bf16x8 ldstr(LAS char* p0, LAS char* p1) {
  if (ABL == 1 || ABL == 3) { bf16x8 r; asm volatile("" : "=v"(r)); return r; }
  const s16x4 a = __builtin_amdgcn_ds_read_tr16_b64_v4i16((LAS s16x4*)p0);
  const s16x4 b = __builtin_amdgcn_ds_read_tr16_b64_v4i16((LAS s16x4*)p1);
  const s16x8 w = __builtin_shufflevector(a, b, 0, 1, 2, 3, 4, 5, 6, 7);
  return __builtin_bit_cast(bf16x8, w);
}
DI u32x4 pack8(f32x4 a, f32x4 b) { u32x4 r; r.x = pk2(a.x, a.y); r.y = pk2(a.z, a.w); r.z = pk2(b.x, b.y); r.w = pk2(b.z, b.w); return r; }
DI u32x2 pack4(f32x4 a) { u32x2 r; r.x = pk2(a.x, a.y); r.y = pk2(a.z, a.w); return r; }
DI int wrapk(int k, int n) { return k >= n ? k - n : k; }
#define SB() __builtin_amdgcn_sched_barrier(0)
DI f32x4 keep(f32x4 v) { asm volatile("" : "+v"(v)); return v; }
DI LAS char* pin(LAS char* p) { asm volatile("" : "+v"(p)); return p; }
#endif

#define STAMP(i) do { } while (0)
#define CSTAMP(i) do { } while (0)

#define ROT 0
__global__ __launch_bounds__(768, 3) void s5_mixer_kernel(
    const void* __restrict__ x, const void* __restrict__ lam_re, const void* __restrict__ lam_im,
    const void* __restrict__ b_re, const void* __restrict__ b_im, const void* __restrict__ c_re,
    const void* __restrict__ c_im, const void* __restrict__ log_dt, const void* __restrict__ dskip,
    const void* __restrict__ w1, const void* __restrict__ w2, const void* __restrict__ w3,
    const void* __restrict__ w4, void* __restrict__ out, void* __restrict__ ws) {
#if defined(__HIP_DEVICE_COMPILE__) && __HIP_DEVICE_COMPILE__
  __shared__ __attribute__((aligned(16))) char smem_[163840];
  LAS char* const sm = (LAS char*)smem_;
  const u32 L0 = (u32)(size_t)sm;
  const int tid = threadIdx.x, l = tid & 63;
  const int w = __builtin_amdgcn_readfirstlane(tid >> 6);
  const int bid = blockIdx.x, xcd = bid & 7, rr = bid >> 3;
  const int m = (xcd >> 1) * 4 + (rr < 20 ? rr / 5 : (rr - 20) / 3);
  const int role = rr < 20 ? rr % 5 + 5 * (xcd & 1) : 10 + (rr - 20) % 3 + 3 * (xcd & 1);
  const int g = l >> 4, li = l & 15, r8 = l >> 3, tq = w & 3, cq = w >> 2, L = w - 8;
  const rsrc_t rws = mk_rsrc(ws, WS_BYTES);
  const rsrc_t rx = mk_rsrc(x, 2048 * 1280);
  STAMP(0); CSTAMP(30);
  const int fm = (5 * (m & 3) + 1) >> 1;
  const int rot1 = (2 * (role % 5) + fm) % 10, rotA1 = (2 * (role % 3) + 1 + fm) % 10;
  const int rot10 = 0, rot24 = 0, rotA3 = 0;

  u32 ta, tb;
  auto row_stats = [&]() AINL {
    const int stok = m * 128 + role * 8 + w;
    const u32x2 ep = bld64_sc1(0, rws, 0);
    const u32x4 ra = bld128(l * 16, rx, stok * 1280);
    const u32 rb_ = bld32(1024 + l * 4, rx, stok * 1280);
    const u64 e = (((u64)__builtin_amdgcn_readfirstlane(ep.y) << 32) | (u32)__builtin_amdgcn_readfirstlane(ep.x)) + 0x9E3779B97F4A7C15ull;
    ta = (u32)e; tb = (u32)(e >> 32);
    float v[10] = {bflo(ra.x), bfhi(ra.x), bflo(ra.y), bfhi(ra.y), bflo(ra.z), bfhi(ra.z), bflo(ra.w), bfhi(ra.w), bflo(rb_), bfhi(rb_)};
    float s = 0.f;
#pragma unroll
    for (int i = 0; i < 10; ++i) s += v[i];
    const float mu = wavesum(s) * (1.0f / 640.0f);
    float q = 0.f;
#pragma unroll
    for (int i = 0; i < 10; ++i) { const float d = v[i] - mu; q = __builtin_fmaf(d, d, q); }
    const float rs = 1.0f / sqrtf(wavesum(q) * (1.0f / 640.0f) + LN_EPS);
    u32x4 gr = {asu(mu), ta, asu(rs), tb};
    st128_sc1(gr, l == 0 ? WS_XS + stok * 16 : OOB, rws);
  };
  auto put_tag = [&](u32 off) AINL { if (tid == 0) *(LAS u32x2*)(sm + off) = (u32x2){ta, tb}; };
  auto get_tag = [&](u32 off) AINL {
    const u32x2 t = *(volatile LAS u32x2*)(sm + off);
    ta = __builtin_amdgcn_readfirstlane(t.x); tb = __builtin_amdgcn_readfirstlane(t.y);
  };
  auto set_flag = [&](u32 idx) AINL { st128_sc1((u32x4){ta, tb, ta, tb}, l < 8 ? WS_FLAG + idx * 128 + l * 16 : OOB, rws); };
  auto await = [&](u32 own, int i, bool act, auto&& first3) AINL {
    bar();
    if (L == 0) { set_flag(own); poll_flags(WS_FLAG + i * 128, act, rws, ta, tb); }
    bar();
    first3();
  };

  const u32 rb0 = li * 128 + ((g ^ (li >> 1)) << 4);
  const f32x4 zero4 = {0.f, 0.f, 0.f, 0.f};

  if (role < 10) {
    const int j = role;
    constexpr u32 ACT = 0, W = 65536;
    if (w >= 8) {
      const rsrc_t rw1 = mk_rsrc(w1, 640 * 1280), rw2 = mk_rsrc(w2, 640 * 1280);
      const rsrc_t rw3 = mk_rsrc(w3, 640 * 3072), rw4 = mk_rsrc(w4, 1536 * 1280);
      const u32 vx0 = (m * 128 + L * 32 + r8) * 1280 + (((l & 7) ^ g) << 4);
      const u32 vx1 = (m * 128 + L * 32 + 8 + r8) * 1280 + (((l & 7) ^ (4 + g)) << 4) - 1024;
      const u32 wch = j * 128 + (((l & 7) ^ (2 * g)) << 4);
      const u32 vq0 = ((L >> 1) * 32 + (r8 >> 2) * 8 + (L & 1) * 4 + (r8 & 3)) * 1280 + wch, vq1 = vq0 + 16 * 1280 - 1024;
      const u32 ldsA = L0 + ACT + L * 4096, ldsW = L0 + W + L * 2048;
      auto issue_1 = [&](int s) AINL {
        const int kt = wrapk(s + rot1, 10);
        dma2<0>(ldsA + (s & 3) * 16384, vx0, vx1, rx, kt * 128);
        dma2<0>(ldsW + 65536 + (s & 3) * 8192, vq0, vq1, rw1, kt * 81920);
        dma2<0>(ldsA + (s & 3) * 16384 + 2048, vx0, vx1, rx, kt * 128 + 20480);
      };
      issue_1(0); issue_1(1); issue_1(2);
      const u32 vw0 = (L * 16 + r8) * 1280 + wch, vw1 = vw0 + 10240 - 1024;
      const u32 va = L * 4096 + l * 16, vl = L * 1024 + l * 16;
      u32 v3[5];
#pragma unroll
      for (int i = 0; i < 5; ++i) {
        const int c = 64 * i + l, r = c / 20, pos = c - 20 * r;
        v3[i] = (L * 16 + r) * 3072 + j * 320 + ((pos ^ (2 * ((r >> 2) & 1))) << 4) - 1024 * (i & 1);
      }
      auto issue_w2 = [&](int s) AINL { dma2<0>(ldsW + s * 8192, vw0, vw1, rw2, wrapk(s + rot10, 10) * 81920); };
      auto issue_w3 = [&](int s) AINL {
        const u32 d = L0 + W + (s & 3) * 20480 + L * 5120, so = wrapk(s + rot10, 10) * 196608;
        dma2<0>(d, v3[0], v3[1], rw3, so); dma2<0>(d + 2048, v3[2], v3[3], rw3, so); dma1<0>(d + 4096, v3[4], rw3, so);
      };
      auto issue_w4 = [&](int s) AINL { dma2<0>(ldsW + ((s + 5) % 12) * 8192, vw0, vw1, rw4, wrapk(s + rot24, 24) * 81920); };
      auto issue_a = [&](int s, u32 base) AINL { dma4<1>(ldsA + (s & 3) * 16384, va, rws, base); };
      auto issue_h1 = [&](int s) AINL {
#pragma unroll
        for (int ks = 0; ks < 2; ++ks) {
          const int b = 2 * wrapk(s + rot10, 10) + ks, grp = b / 5;
          const int t = m * 128 + l - ((grp & 1) ? -(1 + (grp >> 1)) : (1 + (grp >> 1)));
          dma2<1>(L0 + ACT + (s & 3) * 16384 + ks * 8192 + L * 2048, t >= 0 ? t * 16 : OOB, t < 2048 - 64 ? t * 16 : OOB, rws, WS_H1 + (b * 4 + L) * 32768);
        }
      };
      const bf16x8 ones = __builtin_bit_cast(bf16x8, (u32x4){0x3F803F80u, 0x3F803F80u, 0x3F803F80u, 0x3F803F80u});
      LAS char* const wl = pin(sm + W + 65536 + (4 * g + (li >> 2)) * 128 + ((L ^ ((2 * g + (li >> 3)) & 3)) << 5) + 8 * (l & 3));
      f32x4 cs = zero4;
      sfor<0, 10>([&](auto ic) AINL {
        constexpr int s = ic.v;
        constexpr int gs[13] = {6, 6, 6, 6, 6, 6, 6, 6, 6, 6, 5, 6, 6};
        waitbar<gs[s + 1] + gs[s + 2]>();
        if constexpr (s + 3 < 10) issue_1(s + 3);
        else if constexpr (s == 7) { dma1<1>(L0 + ACT + 2 * 16384 + L * 1024, vl, rws, WS_XS + m * 2048); issue_w2(0); issue_w2(1); }
        else { issue_w2(3 * s - 22); issue_w2(3 * s - 21); issue_w2(3 * s - 20); }
        if constexpr (s == 0) get_tag(W);
        const bf16x8 c0 = ldstr(wl + (s & 3) * 8192, wl + (s & 3) * 8192 + 2048), c1 = ldstr(wl + (s & 3) * 8192 + 4096, wl + (s & 3) * 8192 + 6144);
        cs = keep(mfma(c1, ones, mfma(c0, ones, cs)));
      });
      if (li == 0) *(LAS f32x4*)(sm + ACT + 2 * 16384 + 4096 + L * 64 + g * 16) = cs;
      waitbar<16>();
      issue_w2(8); issue_w2(9);
      await(FL_1 + m * 10 + j, FL_1 + m * 10 + (l < 10 ? l : l < 16 ? -10 + (l - 10) + (l >= 13 ? 2 : 0) : 12 + (l - 16) + (l >= 19 ? 2 : 0)),
            l < 10 || (l < 16 && m > 0) || (l >= 16 && l < 22 && m < 15), [&]() AINL {
        sfor<0, 3>([&](auto ic) AINL { constexpr int s = ic.v; issue_h1(s); });
      });
      sfor<0, 10>([&](auto ic) AINL {
        constexpr int s = ic.v;
        constexpr int gs[13] = {4, 4, 4, 4, 4, 4, 4, 4, 4, 4, 5, 5, 5};
        waitbar<gs[s + 1] + gs[s + 2]>();
        if constexpr (s + 3 < 10) issue_h1(s + 3);
        else issue_w3(s - 7);
      });
      bar();
      issue_w3(3);
      await(FL_2 + m * 10 + j, l < 10 ? FL_2 + m * 10 + l : FL_C + ((5 * j) >> 2) + l - 10, l < (j < 9 ? 12 : 11), [&]() AINL {
        sfor<0, 3>([&](auto ic) AINL { constexpr int s = ic.v; issue_a(s, WS_X1 + (m * 10 + wrapk(s + rot10, 10)) * 16384); });
      });
      sfor<0, 10>([&](auto ic) AINL {
        constexpr int s = ic.v;
        constexpr int gs[13] = {4, 4, 4, 4, 9, 9, 9, 9, 9, 9, 8, 8, 8};
        waitbar<gs[s + 1] + gs[s + 2]>();
        if constexpr (s + 3 < 10) {
          issue_a(s + 3, WS_X1 + (m * 10 + wrapk(s + 3 + rot10, 10)) * 16384);
          if constexpr (s >= 1) issue_w3(s + 3);
        } else if constexpr (s == 7) {
          issue_a(2, WS_ST1 + m * 20480);
          issue_w4(0); issue_w4(1);
        } else if constexpr (s == 8) {
          dma1<1>(L0 + ACT + 3 * 16384 + L * 1024, vl, rws, WS_ST1 + m * 20480 + 16384);
          dma1<1>(L0 + ACT + 3 * 16384 + 4096 + L * 1024, vl, rws, WS_CS3 + j * 640);
          issue_w4(2); issue_w4(3); issue_w4(4);
        } else { issue_w4(5); issue_w4(6); issue_w4(7); issue_w4(8); }
      });
      waitbar<14>();
      issue_w4(9); issue_w4(10); issue_w4(11);
      bar();
      await(FL_3 + m * 10 + j, FL_3 + m * 10 + l, l < 10, [&]() AINL {
        sfor<0, 3>([&](auto ic) AINL { constexpr int s = ic.v; issue_a(s, WS_H2 + (m * 24 + wrapk(s + rot24, 24)) * 16384); });
      });
      u32x2 fs;
      const u32 fso = WS_FLAG + (FL_S + m * 5 + (j >> 1)) * 128;
      sfor<0, 24>([&](auto ic) AINL {
        constexpr int s = ic.v;
        constexpr int gs[27] = {4, 4, 4, 4, 6, 6, 6, 6, 6, 6, 6, 6, 6, 6, 6, 6, 4, 4, 4, 4, 5, 4, 4, 4, 4, 0, 0};
        if constexpr (s == 20) { asm volatile("s_waitcnt vmcnt(%1) lgkmcnt(0)\n\ts_barrier" : "+v"(fs) : "i"(VC(gs[s + 1] + gs[s + 2])) : "memory"); }
        else waitbar<gs[s + 1] + gs[s + 2]>();
        if constexpr (s == 17) asm volatile("buffer_load_dwordx2 %0, %1, %2, 0 offen sc1" : "=v"(fs) : "v"(fso), "s"(rws) : "memory");
        if constexpr (s + 3 < 24) issue_a(s + 3, WS_H2 + (m * 24 + wrapk(s + 3 + rot24, 24)) * 16384);
        if constexpr (s >= 1 && s <= 12) issue_w4(s + 11);
        if constexpr (s == 21) {
          if (!__all(fs.x == ta && fs.y == tb)) poll_flags(fso, true, rws, ta, tb);
          issue_a(0, WS_SS + (m * 10 + j) * 16384);
        }
      });
      waitbar<0>();
    } else {
      row_stats();
      put_tag(W);
      const int q4 = li >> 2, p4 = l & 3;
      LAS char* const wb0 = pin(sm + W + (4 * g + q4) * 128 + ((((2 * cq) ^ ((2 * g + (q4 >> 1)) & 3))) << 5) + 8 * p4);
      LAS char* const wb1 = pin(sm + W + (4 * g + q4) * 128 + ((((2 * cq + 1) ^ ((2 * g + (q4 >> 1)) & 3))) << 5) + 8 * p4);
      LAS char* const wh0 = pin(wb0 + 65536);
      LAS char* const wh1 = pin(wb1 + 65536);
      LAS char* const ab = pin(sm + ACT + tq * 4096 + l * 16);
      struct F64 { bf16x8 a0, a1, b0, b1; };
      F64 F0, F1;
      f32x4 acc[2][2], cs[2];
      u32x2 xs[2][2];
      auto step64 = [&](auto mf, auto tts, F64& R, LAS char* pb, u32 wo, const F64& M) AINL {
        LAS char* const p0 = wo >= 65536 ? wh0 + (wo - 65536) : wb0 + wo;
        LAS char* const p1 = wo >= 65536 ? wh1 + (wo - 65536) : wb1 + wo;
        R.a0 = ldstr(p0, p0 + 2048); SB();
        if constexpr (mf.v) acc[0][0] = mfma(M.a0, M.b0, acc[0][0]);
        SB(); R.b0 = lds128(pb); R.b1 = lds128(pb + tts.v); SB();
        if constexpr (mf.v) acc[1][0] = mfma(M.a1, M.b0, acc[1][0]);
        SB(); R.a1 = ldstr(p1, p1 + 2048); SB();
        if constexpr (mf.v) { acc[0][1] = mfma(M.a0, M.b1, acc[0][1]); acc[1][1] = mfma(M.a1, M.b1, acc[1][1]); }
        SB();
      };
      auto mm = [&](const F64& M) AINL {
        acc[0][0] = mfma(M.a0, M.b0, acc[0][0]); acc[1][0] = mfma(M.a1, M.b0, acc[1][0]);
        acc[0][1] = mfma(M.a0, M.b1, acc[0][1]); acc[1][1] = mfma(M.a1, M.b1, acc[1][1]);
      };
      float mu[2], rs[2];
      {
        LAS char* const xb0 = pin(sm + ACT + tq * 4096 + rb0);
        LAS char* const xb1 = pin(sm + ACT + tq * 4096 + (rb0 ^ 64));
#pragma unroll
        for (int a = 0; a < 2; ++a) { acc[a][0] = zero4; acc[a][1] = zero4; }
        const int s_own = wrapk(j - rot1 + 10, 10);
        LAS char* const xo0 = pin(sm + ACT + tq * 4096 + li * 128 + (((4 * cq + (g >> 1)) ^ (li >> 1)) << 4) + 8 * (g & 1));
        LAS char* const xo1 = pin(sm + ACT + tq * 4096 + li * 128 + (((4 * cq + 2 + (g >> 1)) ^ (li >> 1)) << 4) + 8 * (g & 1));
        u32 wdum = 0;
        if (WARM && w == 7) touch(wdum, l < 40 ? ((m * 10 + j) * 40 + l) * 128 : OOB, mk_rsrc(w2, 640 * 1280));
        STAMP(1);
        sfor<0, 10>([&](auto ic) AINL {
          constexpr int s = ic.v;
          constexpr u32 so = (s & 3) * 16384, wo = 65536 + (s & 3) * 8192;
          bar();
          if constexpr (s == 0) STAMP(16);
          if constexpr (s == 3) STAMP(20);
          if constexpr (s == 6) STAMP(21);
          step64(IC<(s > 0)>{}, IC<2048>{}, F0, xb0 + so, wo, F1);
          step64(IC<1>{}, IC<2048>{}, F1, xb1 + so, wo + 4096, F0);
          if (s == s_own) {
#pragma unroll
            for (int tt = 0; tt < 2; ++tt)
#pragma unroll
              for (int ct = 0; ct < 2; ++ct) xs[ct][tt] = *(LAS u32x2*)((ct ? xo1 : xo0) + so + tt * 2048);
          }
        });
        mm(F1);
        bar();
        STAMP(2);
        u32x4 g0 = *(LAS u32x4*)(sm + ACT + 2 * 16384 + (tq * 32 + li) * 16);
        u32x4 g1 = *(LAS u32x4*)(sm + ACT + 2 * 16384 + (tq * 32 + 16 + li) * 16);
        cs[0] = *(LAS f32x4*)(sm + ACT + 2 * 16384 + 4096 + cq * 128 + g * 16);
        cs[1] = *(LAS f32x4*)(sm + ACT + 2 * 16384 + 4096 + cq * 128 + 64 + g * 16);
        if (!__all(g0.y == ta && g0.w == tb && g1.y == ta && g1.w == tb)) {
          g0 = poll_granule(WS_XS + (m * 128 + tq * 32 + li) * 16, true, rws, ta, tb);
          g1 = poll_granule(WS_XS + (m * 128 + tq * 32 + 16 + li) * 16, true, rws, ta, tb);
        }
        mu[0] = asf(g0.x); rs[0] = asf(g0.z); mu[1] = asf(g1.x); rs[1] = asf(g1.z);
#pragma unroll
        for (int tt = 0; tt < 2; ++tt) {
          const float r2 = rs[tt] * __builtin_amdgcn_rsqf(1.0f - LN_EPS * rs[tt] * rs[tt] + LN_EPS);
          f32x4 h[2];
#pragma unroll
          for (int ct = 0; ct < 2; ++ct)
#pragma unroll
            for (int e = 0; e < 4; ++e) h[ct][e] = gelu_f(r2 * __builtin_fmaf(-mu[tt], cs[ct][e], acc[ct][tt][e]));
          st128_sc1(pack8(h[0], h[1]), WS_H1 + ((2 * j + cq) * 4 + g) * 32768 + (m * 128 + tq * 32 + tt * 16 + li) * 16, rws);
          SB();
        }
        STAMP(3);
        asm volatile("s_waitcnt vmcnt(0) lgkmcnt(0)\n\ts_barrier" : "+v"(wdum)::"memory");
      }
      STAMP(4);
      bar();
      STAMP(5);
      LAS char* const abt = pin(sm + ACT + g * 2048 + (tq * 32 + li) * 16);
#pragma unroll
      for (int a = 0; a < 2; ++a) { acc[a][0] = zero4; acc[a][1] = zero4; }
      sfor<0, 10>([&](auto ic) AINL {
        constexpr int s = ic.v;
        bar();
        if constexpr (s == 0) STAMP(17);
        if constexpr (s == 3) STAMP(27);
        if constexpr (s == 6) STAMP(28);
        step64(IC<(s > 0)>{}, IC<256>{}, F0, abt + (s & 3) * 16384, s * 8192, F1);
        step64(IC<1>{}, IC<256>{}, F1, abt + (s & 3) * 16384 + 8192, s * 8192 + 4096, F0);
      });
      bar();
      mm(F1);
      STAMP(6);
      u32x4 x1p[2];
      {
        float sm1[2], sq1[2];
#pragma unroll
        for (int tt = 0; tt < 2; ++tt) {
          float s1 = 0.f, s2 = 0.f;
          f32x4 x1[2];
#pragma unroll
          for (int ct = 0; ct < 2; ++ct) {
            const float xv[4] = {bflo(xs[ct][tt].x), bfhi(xs[ct][tt].x), bflo(xs[ct][tt].y), bfhi(xs[ct][tt].y)};
#pragma unroll
            for (int e = 0; e < 4; ++e) {
              const float v = __builtin_fmaf(xv[e] - mu[tt], rs[tt], acc[ct][tt][e]);
              x1[ct][e] = v; s1 += v; s2 = __builtin_fmaf(v, v, s2);
            }
          }
          x1p[tt] = pack8(x1[0], x1[1]);
          st128_sc1(x1p[tt], WS_X1 + (m * 10 + j) * 16384 + (2 * tq + tt) * 2048 + cq * 1024 + l * 16, rws);
          sm1[tt] = sum32(sum16(s1)); sq1[tt] = sum32(sum16(s2));
        }
        const u32x2 st = {asu(g & 1 ? sm1[1] : sm1[0]), asu(g & 1 ? sq1[1] : sq1[0])};
        st64_sc1(st, l < 32 ? WS_ST1 + m * 20480 + (2 * j + cq) * 1024 + (tq * 32 + l) * 8 : OOB, rws);
      }
      STAMP(7);
      waitbar<0>();
      STAMP(8);
      bar();
      STAMP(9);
      {
        const int sg = (g & 1) * 32;
        LAS char* const bA = pin(sm + W + (4 * g + q4) * 320 + 8 * p4 + 160 * cq + (cq ? -sg : sg));
        LAS char* const bB = pin(sm + W + (4 * g + q4) * 320 + 8 * p4 + 160 * cq + (cq ? sg : -sg));
        LAS char* const bA2 = pin(bA + 40960);
        LAS char* const bB2 = pin(bB + 40960);
        struct F3 { bf16x8 a[5], b0, b1; };
        F3 G0, G1;
        f32x4 a3[5][2];
        auto step3 = [&](auto mf, F3& R, u32 so, u32 wo, const F3& M) AINL {
          LAS char* const pA = wo >= 40960 ? bA2 + (wo - 40960) : bA + wo;
          LAS char* const pB = wo >= 40960 ? bB2 + (wo - 40960) : bB + wo;
          R.a[0] = ldstr(pA, pA + 5120); SB();
          if constexpr (mf.v) a3[0][0] = mfma(M.a[0], M.b0, a3[0][0]);
          SB(); R.b0 = lds128(ab + so); R.b1 = lds128(ab + so + 2048); SB();
          if constexpr (mf.v) a3[0][1] = mfma(M.a[0], M.b1, a3[0][1]);
          SB();
          sfor<1, 5>([&](auto ii) AINL {
            constexpr int i = ii.v;
            LAS char* const pw = ((i & 1) ? pB : pA) + 32 * i;
            R.a[i] = ldstr(pw, pw + 5120); SB();
            if constexpr (mf.v) a3[(i + 1) >> 1][(i + 1) & 1] = mfma(M.a[(i + 1) >> 1], ((i + 1) & 1) ? M.b1 : M.b0, a3[(i + 1) >> 1][(i + 1) & 1]);
            SB();
          });
          if constexpr (mf.v) {
            a3[3][0] = mfma(M.a[3], M.b0, a3[3][0]); a3[3][1] = mfma(M.a[3], M.b1, a3[3][1]);
            a3[4][0] = mfma(M.a[4], M.b0, a3[4][0]); a3[4][1] = mfma(M.a[4], M.b1, a3[4][1]);
          }
          SB();
        };
#pragma unroll
        for (int i = 0; i < 5; ++i) { a3[i][0] = zero4; a3[i][1] = zero4; }
        sfor<0, 10>([&](auto ic) AINL {
          constexpr int s = ic.v;
          constexpr u32 so = (s & 3) * 16384, wo = (s & 3) * 20480;
          bar();
          if constexpr (s == 0) STAMP(18);
          if constexpr (s == 3) STAMP(25);
          step3(IC<(s > 0)>{}, G0, so, wo, G1);
          step3(IC<1>{}, G1, so + 1024, wo + 10240, G0);
        });
#pragma unroll
        for (int i = 0; i < 5; ++i) { a3[i][0] = mfma(G1.a[i], G1.b0, a3[i][0]); a3[i][1] = mfma(G1.a[i], G1.b1, a3[i][1]); }
        bar();
        STAMP(10);
        float m1[2], r1[2];
#pragma unroll
        for (int tt = 0; tt < 2; ++tt) {
          float s1 = 0.f, s2 = 0.f;
#pragma unroll
          for (int i = 0; i < 5; ++i) {
            const f32x2 v = *(LAS f32x2*)(sm + ACT + 2 * 16384 + (g + 4 * i) * 1024 + (tq * 32 + tt * 16 + li) * 8);
            s1 += v.x; s2 += v.y;
          }
          s1 = sum32(sum16(s1)) * (1.0f / 640.0f); s2 = sum32(sum16(s2)) * (1.0f / 640.0f);
          m1[tt] = s1; r1[tt] = __builtin_amdgcn_rsqf(fmaxf(__builtin_fmaf(-s1, s1, s2), 0.f) + LN_EPS);
        }
        f32x4 c3[5];
#pragma unroll
        for (int i = 0; i < 5; ++i) c3[i] = *(LAS f32x4*)(sm + ACT + 3 * 16384 + 4096 + (80 * cq + 16 * i + 4 * g) * 4);
        auto act4 = [&](int i, int tt) AINL {
          f32x4 h;
#pragma unroll
          for (int e = 0; e < 4; ++e) h[e] = gelu_f(r1[tt] * __builtin_fmaf(-m1[tt], c3[i][e], a3[i][tt][e]));
          return pack4(h);
        };
        auto put = [&](u32x2 lo, u32x2 hi, int b, int tt) AINL {
          st128_sc1((u32x4){lo.x, lo.y, hi.x, hi.y}, b < 48 ? WS_H2 + (m * 24 + (b >> 1)) * 16384 + (2 * tq + tt) * 2048 + (b & 1) * 1024 + l * 16 : OOB, rws);
          SB();
        };
        LAS char* const xch = sm + ACT + 3 * 16384 + 8192 + tq * 1024 + l * 8;
        if (cq == 0) {
          *(LAS u32x2*)(xch + 512) = act4(4, 1);
          put(act4(0, 0), act4(1, 0), 5 * j, 0); put(act4(2, 0), act4(3, 0), 5 * j + 1, 0);
          bar();
          put(act4(4, 0), *(LAS u32x2*)xch, 5 * j + 2, 0);
          put(act4(0, 1), act4(1, 1), 5 * j, 1); put(act4(2, 1), act4(3, 1), 5 * j + 1, 1);
        } else {
          *(LAS u32x2*)xch = act4(0, 0);
          put(act4(1, 0), act4(2, 0), 5 * j + 3, 0); put(act4(3, 0), act4(4, 0), 5 * j + 4, 0);
          bar();
          put(*(LAS u32x2*)(xch + 512), act4(0, 1), 5 * j + 2, 1);
          put(act4(1, 1), act4(2, 1), 5 * j + 3, 1); put(act4(3, 1), act4(4, 1), 5 * j + 4, 1);
        }
      }
      STAMP(11);
      waitbar<0>();
      STAMP(12);
      const rsrc_t rdk = mk_rsrc(dskip, 1280);
      const u32x2 dk0 = bld64((cq * 32 + 4 * g) * 2, rdk, j * 128), dk1 = bld64((cq * 32 + 16 + 4 * g) * 2, rdk, j * 128);
      bar();
      STAMP(13);
#pragma unroll
      for (int a = 0; a < 2; ++a) { acc[a][0] = zero4; acc[a][1] = zero4; }
      sfor<0, 24>([&](auto ic) AINL {
        constexpr int s = ic.v;
        bar();
        if constexpr (s == 0) STAMP(19);
        if constexpr (s == 6) STAMP(22);
        if constexpr (s == 12) STAMP(23);
        if constexpr (s == 18) STAMP(24);
        step64(IC<(s > 0)>{}, IC<2048>{}, F0, ab + (s & 3) * 16384, ((s + 5) % 12) * 8192, F1);
        step64(IC<1>{}, IC<2048>{}, F1, ab + (s & 3) * 16384 + 1024, ((s + 5) % 12) * 8192 + 4096, F0);
      });
      mm(F1);
      bar();
      STAMP(14);
      const rsrc_t rout = mk_rsrc(out, 2048 * 2560);
#pragma unroll
      for (int tt = 0; tt < 2; ++tt) {
        const u32x4 sv = *(LAS u32x4*)(sm + ACT + w * 2048 + tt * 1024 + l * 16);
#pragma unroll
        for (int ct = 0; ct < 2; ++ct) {
          const float xv[4] = {bflo(xs[ct][tt].x), bfhi(xs[ct][tt].x), bflo(xs[ct][tt].y), bfhi(xs[ct][tt].y)};
          const u32 s0 = ct ? sv.z : sv.x, s1 = ct ? sv.w : sv.y;
          const float sy[4] = {bflo(s0), bfhi(s0), bflo(s1), bfhi(s1)};
          const u32 y0 = ct ? x1p[tt].z : x1p[tt].x, y1 = ct ? x1p[tt].w : x1p[tt].y;
          const float x1[4] = {bflo(y0), bfhi(y0), bflo(y1), bfhi(y1)};
          const u32x2 dk = ct ? dk1 : dk0;
          const float dv[4] = {bflo(dk.x), bfhi(dk.x), bflo(dk.y), bfhi(dk.y)};
          u32x4 o;
#pragma unroll
          for (int e = 0; e < 4; ++e) {
            const float sfull = __builtin_fmaf((xv[e] - mu[tt]) * rs[tt], dv[e], sy[e]);
            o[e] = asu(__builtin_fmaf(sfull, silu_f(x1[e] + acc[ct][tt][e]), xv[e]));
          }
          __builtin_amdgcn_raw_buffer_store_b128(o, rout, (m * 128 + tq * 32 + tt * 16 + li) * 2560 + (j * 64 + cq * 32 + ct * 16 + 4 * g) * 4, 0, 0);
        }
      }
      STAMP(15); CSTAMP(31);
      if (tid == 0) st32_sc1((u32)__builtin_amdgcn_s_memrealtime(), WS_STAMP + bid * 64, rws);
    }
  } else if (role < 15) {
    const int a = role - 10;
    constexpr u32 MISC = 147456, CST = 157696, TAG = 159744;
    if (w >= 8) {
      const rsrc_t rbr = mk_rsrc(b_re, 640 * 1280), rbi = mk_rsrc(b_im, 640 * 1280);
      const rsrc_t rcr = mk_rsrc(c_re, 640 * 1280), rci = mk_rsrc(c_im, 640 * 1280);
      const u32 vx0 = (m * 128 + 32 * (r8 >> 2) + 8 * L + (r8 & 3)) * 1280 + (((l & 7) ^ g) << 4);
      const u32 vx1 = (m * 128 + 32 * (2 + (r8 >> 2)) + 8 * L + (r8 & 3)) * 1280 + (((l & 7) ^ (4 + g)) << 4) - 1024;
      const u32 vb0 = (a * 128 + L * 32 + r8) * 1280 + (((l & 7) ^ g) << 4);
      const u32 vb1 = (a * 128 + L * 32 + 8 + r8) * 1280 + (((l & 7) ^ (4 + g)) << 4) - 1024;
      const u32 ldsw = L0 + L * 4096;
      auto issue_nk = [&](u32 d, rsrc_t r, u32 so) AINL { dma2<0>(d, vb0, vb1, r, so); dma2<0>(d + 2048, vb0, vb1, r, so + 20480); };
      auto issue1 = [&](int s) AINL {
        const int kt = wrapk(s + rotA1, 10);
        const u32 d = ldsw + (s % 3) * 49152;
        dma2<0>(d, vx0, vx1, rx, kt * 128); dma2<0>(d + 2048, vx0, vx1, rx, kt * 128 + 5120);
        issue_nk(d + 16384, rbr, kt * 128); issue_nk(d + 32768, rbi, kt * 128);
      };
      auto issue_c = [&](int s) AINL {
        const int kt = wrapk(s + rotA3, 20);
        issue_nk(ldsw + 49152 + (s % 6) * 16384, kt < 10 ? rcr : rci, wrapk(kt, 10) * 128);
      };
      const u32 va = L * 4096 + l * 16;
      auto issue_h = [&](int s) AINL {
        dma4<1>(ldsw + ((s & 3) == 3 ? MISC : (s & 3) * 16384), va, rws, WS_HH + (m * 20 + wrapk(s + rotA3, 20)) * 16384);
      };
      issue1(0); issue1(1);
      sfor<0, 10>([&](auto ic) AINL {
        constexpr int s = ic.v;
        waitbar<(s == 9 ? 13 : 12)>();
        if constexpr (s + 2 < 10) issue1(s + 2);
        else if constexpr (s == 8) { dma1<1>(L0 + MISC + L * 1024, (L & 1) * 1024 + l * 16, rws, L < 2 ? WS_XS + m * 2048 : WS_RS + a * 2048); issue_c(0); issue_c(1); issue_c(2); }
        else { issue_c(3); issue_c(4); issue_c(5); }
        if constexpr (s == 0) get_tag(TAG);
      });
      waitbar<24>();
      bar(); bar();
      await(FL_A + m * 5 + a, FL_A + m * 5 + l, l < 5, [&]() AINL { issue_h(0); issue_h(1); issue_h(2); });
      sfor<0, 20>([&](auto ic) AINL {
        constexpr int s = ic.v;
        constexpr int gs[23] = {4, 4, 4, 4, 8, 8, 8, 8, 8, 8, 8, 8, 8, 8, 8, 8, 8, 8, 4, 4, 0, 0, 0};
        waitbar<gs[s + 1] + gs[s + 2]>();
        if constexpr (s + 3 < 20) issue_h(s + 3);
        if constexpr (s >= 1 && s + 5 < 20) issue_c(s + 5);
      });
    } else {
      const int p = a * 128 + w * 16 + li;
      const u32 plr = bld16(p * 2, mk_rsrc(lam_re, 1280)), pli = bld16(p * 2, mk_rsrc(lam_im, 1280)), pdt = bld16(p * 2, mk_rsrc(log_dt, 1280));
      row_stats();
      put_tag(TAG);
      float lbr, lbi, kr, ki;
      {
        const float Lr = bflo(plr), Li = bflo(pli);
        const float dt = bflo(pk2(expf(bflo(pdt)), 0.f));
        const float ea = expf(dt * Lr);
        float sn, cn; sincosf(dt * Li, &sn, &cn);
        lbr = ea * cn; lbi = ea * sn;
        const float inv = 2.0f / (Lr * Lr + Li * Li);
        kr = ((lbr - 1.0f) * Lr + lbi * Li) * inv;
        ki = (lbi * Lr - (lbr - 1.0f) * Li) * inv;
        asm volatile("" : "+v"(lbr), "+v"(lbi), "+v"(kr), "+v"(ki));
      }
      f32x4 are[8], aim[8];
      u32 wdum = 0;
      if (WARM && w >= 4) touch(wdum, l < 40 ? ((m * 5 + a) * 80 + (w & 1) * 40 + l) * 128 : OOB, mk_rsrc(w >= 6 ? c_im : c_re, 640 * 1280));
#pragma unroll
      for (int i = 0; i < 8; ++i) { are[i] = zero4; aim[i] = zero4; }
      {
        struct FW { bf16x8 br, bi; };
        struct FX { bf16x8 x[4]; };
        FW W0, W1; FX Xa, Xb;
        auto half = [&](auto mf, auto rw, auto hh, FW& RW, LAS char* pw, FX& RX, LAS char* px, const FW& MW, const FX& MX) AINL {
          constexpr int h = hh.v;
          if constexpr (rw.v) { RW.br = lds128(pw); RX.x[0] = lds128(px); } else { RX.x[0] = lds128(px); RX.x[1] = lds128(px + 2048); }
          SB();
          if constexpr (mf.v) are[4 * h] = mfma(MX.x[0], MW.br, are[4 * h]);
          SB();
          if constexpr (rw.v) { RW.bi = lds128(pw + 16384); RX.x[1] = lds128(px + 2048); } else { RX.x[2] = lds128(px + 4096); RX.x[3] = lds128(px + 6144); }
          SB();
          if constexpr (mf.v) aim[4 * h] = mfma(MX.x[0], MW.bi, aim[4 * h]);
          SB();
          if constexpr (rw.v) { RX.x[2] = lds128(px + 4096); RX.x[3] = lds128(px + 6144); }
          SB();
          if constexpr (mf.v) {
#pragma unroll
            for (int i = 1; i < 4; ++i) { are[4 * h + i] = mfma(MX.x[i], MW.br, are[4 * h + i]); aim[4 * h + i] = mfma(MX.x[i], MW.bi, aim[4 * h + i]); }
          }
          SB();
        };
        STAMP(1);
        sfor<0, 10>([&](auto ic) AINL {
          constexpr int s = ic.v;
          LAS char* const x0 = pin(sm + rb0 + (s % 3) * 49152);
          LAS char* const x1 = pin(sm + (rb0 ^ 64) + (s % 3) * 49152);
          LAS char* const w0 = pin(x0 + w * 2048 + 16384);
          LAS char* const w1_ = pin(x1 + w * 2048 + 16384);
          bar();
          if constexpr (s == 0) STAMP(16);
          half(IC<(s > 0)>{}, IC<1>{}, IC<1>{}, W0, w0, Xa, x0, W1, Xb);
          half(IC<1>{}, IC<0>{}, IC<0>{}, W0, w0, Xb, x0 + 8192, W0, Xa);
          half(IC<1>{}, IC<1>{}, IC<1>{}, W1, w1_, Xa, x1, W0, Xb);
          half(IC<1>{}, IC<0>{}, IC<0>{}, W1, w1_, Xb, x1 + 8192, W1, Xa);
        });
#pragma unroll
        for (int i = 0; i < 4; ++i) { are[4 + i] = mfma(Xb.x[i], W1.br, are[4 + i]); aim[4 + i] = mfma(Xb.x[i], W1.bi, aim[4 + i]); }
      }
      bar();
      STAMP(2);
      if (w < 2) {
        u32x4 g0 = *(LAS u32x4*)(sm + MISC + tid * 16);
        if (!__all(g0.y == ta && g0.w == tb)) g0 = poll_granule(WS_XS + (m * 128 + tid) * 16, true, rws, ta, tb);
        const float rs = asf(g0.z);
        *(LAS f32x2*)(sm + CST + (tid >> 5) * 272 + (tid & 31) * 8) = (f32x2){rs, -rs * asf(g0.x)};
      }
      u32x4 rg = *(LAS u32x4*)(sm + MISC + 2048 + (w * 16 + li) * 16);
      if (!__all(rg.y == ta && rg.w == tb)) rg = poll_granule(WS_RS + p * 16, true, rws, ta, tb);
      const float sre = asf(rg.x), sim = asf(rg.z);
      asm volatile("s_waitcnt vmcnt(0) lgkmcnt(0)\n\ts_barrier" : "+v"(wdum)::"memory");
#pragma unroll
      for (int mt = 0; mt < 8; ++mt) {
        const f32x4 s0 = *(LAS f32x4*)(sm + CST + g * 272 + mt * 32), s1 = *(LAS f32x4*)(sm + CST + g * 272 + mt * 32 + 16);
        const float rv[4] = {s0.x, s0.z, s1.x, s1.z}, nv[4] = {s0.y, s0.w, s1.y, s1.w};
#pragma unroll
        for (int e = 0; e < 4; ++e) {
          are[mt][e] = __builtin_fmaf(rv[e], are[mt][e], nv[e] * sre);
          aim[mt][e] = __builtin_fmaf(rv[e], aim[mt][e], nv[e] * sim);
        }
      }
      bar();
      float hr = 0.f, hi = 0.f;
#pragma unroll
      for (int mt = 0; mt < 8; ++mt)
#pragma unroll
        for (int e = 0; e < 4; ++e) {
          const float nr = __builtin_fmaf(-lbi, hi, __builtin_fmaf(lbr, hr, are[mt][e]));
          const float ni = __builtin_fmaf(lbi, hr, __builtin_fmaf(lbr, hi, aim[mt][e]));
          hr = nr; hi = ni;
        }
      float er[4], ei[4];
      gather4(hr, er); gather4(hi, ei);
      float p32r = lbr, p32i = lbi;
#pragma unroll
      for (int i = 0; i < 5; ++i) { const float t = p32r * p32r - p32i * p32i; p32i = 2.0f * p32r * p32i; p32r = t; }
      float cr[5], ci[5];
      cr[0] = 0.f; ci[0] = 0.f;
#pragma unroll
      for (int q = 0; q < 4; ++q) {
        cr[q + 1] = __builtin_fmaf(-p32i, ci[q], __builtin_fmaf(p32r, cr[q], er[q]));
        ci[q + 1] = __builtin_fmaf(p32i, cr[q], __builtin_fmaf(p32r, ci[q], ei[q]));
      }
      st128_sc1((u32x4){asu(cr[4]), ta, asu(ci[4]), tb}, l < 16 ? WS_EG + (m * 640 + p) * 16 : OOB, rws);
      STAMP(3);
      const float p64r = p32r * p32r - p32i * p32i, p64i = 2.0f * p32r * p32i;
      const float Pr = p64r * p64r - p64i * p64i, Pi = 2.0f * p64r * p64i;
#pragma unroll
      for (int mt = 0; mt < 8; ++mt) {
#pragma unroll
        for (int e = 0; e < 4; ++e) {
          const float ur = are[mt][e], ui = aim[mt][e];
          are[mt][e] = __builtin_fmaf(kr, ur, -ki * ui);
          aim[mt][e] = __builtin_fmaf(-kr, ui, -ki * ur);
        }
        are[mt] = keep(are[mt]); aim[mt] = keep(aim[mt]);
      }
      float Hr = 0.f, Hi = 0.f;
      if (m > 0) {
        u32x4 e0, e1, e2, e3;
        const int m0 = m - 1 - 4 * g;
        const u32 o0 = WS_EG + (m0 * 640 + p) * 16;
        const bool v0 = m0 >= 0, v1 = m0 >= 1, v2 = m0 >= 2, v3 = m0 >= 3;
        _Pragma("nounroll") for (int it = 0; it < SPIN_CAP; ++it) {
          asm volatile("buffer_load_dwordx4 %0, %4, %8, 0 offen sc1\n\tbuffer_load_dwordx4 %1, %5, %8, 0 offen sc1\n\t"
                       "buffer_load_dwordx4 %2, %6, %8, 0 offen sc1\n\tbuffer_load_dwordx4 %3, %7, %8, 0 offen sc1\n\ts_waitcnt vmcnt(0)"
                       : "=&v"(e0), "=&v"(e1), "=&v"(e2), "=&v"(e3)
                       : "v"(v0 ? o0 : OOB), "v"(v1 ? o0 - 10240 : OOB), "v"(v2 ? o0 - 20480 : OOB), "v"(v3 ? o0 - 30720 : OOB), "s"(rws) : "memory");
          const bool ok = (!v0 || (e0.y == ta && e0.w == tb)) && (!v1 || (e1.y == ta && e1.w == tb)) &&
                          (!v2 || (e2.y == ta && e2.w == tb)) && (!v3 || (e3.y == ta && e3.w == tb));
          if (__all(ok)) break;
          __builtin_amdgcn_s_sleep(1);
        }
        float qr = asf(e3.x), qi = asf(e3.z), t;
        t = __builtin_fmaf(-Pi, qi, __builtin_fmaf(Pr, qr, asf(e2.x))); qi = __builtin_fmaf(Pi, qr, __builtin_fmaf(Pr, qi, asf(e2.z))); qr = t;
        t = __builtin_fmaf(-Pi, qi, __builtin_fmaf(Pr, qr, asf(e1.x))); qi = __builtin_fmaf(Pi, qr, __builtin_fmaf(Pr, qi, asf(e1.z))); qr = t;
        t = __builtin_fmaf(-Pi, qi, __builtin_fmaf(Pr, qr, asf(e0.x))); qi = __builtin_fmaf(Pi, qr, __builtin_fmaf(Pr, qi, asf(e0.z))); qr = t;
        float gr[4], gi[4];
        gather4(qr, gr); gather4(qi, gi);
        const float P2r = Pr * Pr - Pi * Pi, P2i = 2.0f * Pr * Pi;
        const float P4r = P2r * P2r - P2i * P2i, P4i = 2.0f * P2r * P2i;
        Hr = gr[3]; Hi = gi[3];
#pragma unroll
        for (int q = 2; q >= 0; --q) {
          t = __builtin_fmaf(-P4i, Hi, __builtin_fmaf(P4r, Hr, gr[q])); Hi = __builtin_fmaf(P4i, Hr, __builtin_fmaf(P4r, Hi, gi[q])); Hr = t;
        }
      }
      STAMP(4);
      {
        const float p96r = p64r * p32r - p64i * p32i, p96i = p64r * p32i + p64i * p32r;
        const float fr = g == 0 ? 1.f : g == 1 ? p32r : g == 2 ? p64r : p96r;
        const float fi = g == 0 ? 0.f : g == 1 ? p32i : g == 2 ? p64i : p96i;
        const float c0r = g == 0 ? cr[0] : g == 1 ? cr[1] : g == 2 ? cr[2] : cr[3];
        const float c0i = g == 0 ? ci[0] : g == 1 ? ci[1] : g == 2 ? ci[2] : ci[3];
        const float sr = __builtin_fmaf(-fi, Hi, __builtin_fmaf(fr, Hr, c0r)), si = __builtin_fmaf(fi, Hr, __builtin_fmaf(fr, Hi, c0i));
        hr = __builtin_fmaf(kr, sr, -ki * si);
        hi = __builtin_fmaf(-kr, si, -ki * sr);
      }
#pragma unroll
      for (int mt = 0; mt < 8; ++mt)
#pragma unroll
        for (int e = 0; e < 4; ++e) {
          const float nr = __builtin_fmaf(lbi, hi, __builtin_fmaf(lbr, hr, are[mt][e]));
          const float ni = __builtin_fmaf(-lbi, hr, __builtin_fmaf(lbr, hi, aim[mt][e]));
          hr = nr; hi = ni;
          are[mt][e] = hr; aim[mt][e] = hi;
        }
      {
        LAS char* const tb_ = sm + (w < 6 ? w * 8192 : MISC + (w - 6) * 8192);
        const int swc = ((li & 3) << 1) | ((li >> 2) & 1) | ((li >> 3) << 3);
#pragma unroll
        for (int i = 0; i < 4; ++i) {
          *(LAS u32x4*)(tb_ + li * 256 + (((4 * g + i) ^ swc) << 4)) = pack8(are[2 * i], are[2 * i + 1]);
          *(LAS u32x4*)(tb_ + 4096 + li * 256 + (((4 * g + i) ^ swc) << 4)) = pack8(aim[2 * i], aim[2 * i + 1]);
        }
        asm volatile("s_waitcnt lgkmcnt(0)" ::: "memory");
        const int q4 = li >> 2, p4 = l & 3;
        const int c0 = 8 * (g & 1) + q4, c1 = c0 + 4;
        const int sw0 = ((c0 & 3) << 1) | ((c0 >> 3) << 3), sw1 = sw0 | 1;
#pragma unroll
        for (int part = 0; part < 2; ++part)
#pragma unroll
          for (int u = 0; u < 4; ++u) {
            const int tt = 2 * u + (g >> 1), ch = 2 * tt + (p4 >> 1);
            const bf16x8 v = ldstr(tb_ + part * 4096 + c0 * 256 + ((ch ^ sw0) << 4) + 8 * (p4 & 1),
                                   tb_ + part * 4096 + c1 * 256 + ((ch ^ sw1) << 4) + 8 * (p4 & 1));
            const u32 off = WS_HH + (m * 20 + 10 * part + 2 * a + (w >> 2)) * 16384 + tt * 2048 + ((w >> 1) & 1) * 1024 + ((2 * (w & 1) + (g & 1)) * 16 + li) * 16;
            st128_sc1(__builtin_bit_cast(u32x4, v), off, rws);
          }
      }
      waitbar<0>();
      STAMP(5);
      bar();
      STAMP(6);
      f32x4 ac[4][2];
#pragma unroll
      for (int i = 0; i < 4; ++i) { ac[i][0] = zero4; ac[i][1] = zero4; }
      LAS char* const ab = pin(sm + tq * 4096 + l * 16);
      LAS char* const ab3 = pin(ab + MISC);
      LAS char* cb[2][2];
#pragma unroll
      for (int i = 0; i < 2; ++i) { cb[0][i] = pin(sm + 49152 + i * 49152 + cq * 8192 + rb0); cb[1][i] = pin(sm + 49152 + i * 49152 + cq * 8192 + (rb0 ^ 64)); }
      struct FC { bf16x8 c[4], b0, b1; };
      FC G0, G1;
      auto stepC = [&](auto mf, FC& R, int ks, int s, const FC& M) AINL {
        LAS char* const pa = ((s & 3) == 3 ? ab3 : ab + (s & 3) * 16384) + ks * 1024;
        LAS char* const pc = cb[ks][(s % 6) / 3] + ((s % 6) % 3) * 16384;
        R.b0 = lds128(pa); R.c[0] = lds128(pc); SB();
        if constexpr (mf.v) ac[0][0] = mfma(M.c[0], M.b0, ac[0][0]);
        SB(); R.b1 = lds128(pa + 2048); R.c[1] = lds128(pc + 2048); SB();
        if constexpr (mf.v) ac[0][1] = mfma(M.c[0], M.b1, ac[0][1]);
        SB(); R.c[2] = lds128(pc + 4096); R.c[3] = lds128(pc + 6144); SB();
        if constexpr (mf.v) {
#pragma unroll
          for (int ct = 1; ct < 4; ++ct) { ac[ct][0] = mfma(M.c[ct], M.b0, ac[ct][0]); ac[ct][1] = mfma(M.c[ct], M.b1, ac[ct][1]); }
        }
        SB();
      };
      sfor<0, 20>([&](auto ic) AINL {
        constexpr int s = ic.v;
        bar();
        if constexpr (s == 0) STAMP(17);
        stepC(IC<(s > 0)>{}, G0, 0, s, G1);
        stepC(IC<1>{}, G1, 1, s, G0);
      });
#pragma unroll
      for (int ct = 0; ct < 4; ++ct) { ac[ct][0] = mfma(G1.c[ct], G1.b0, ac[ct][0]); ac[ct][1] = mfma(G1.c[ct], G1.b1, ac[ct][1]); }
      STAMP(7);
#pragma unroll
      for (int cb_ = 0; cb_ < 2; ++cb_)
#pragma unroll
        for (int tt = 0; tt < 2; ++tt)
          st128_sc1(pack8(ac[2 * cb_][tt], ac[2 * cb_ + 1][tt]), WS_SS + ((m * 10 + 2 * a + cq) * 8 + tq + 4 * cb_) * 2048 + tt * 1024 + l * 16, rws);
      waitbar<0>();
      if (w == 0) set_flag(FL_S + m * 5 + a);
      STAMP(8);
    }
  } else if (w < 8) {
    row_stats();
    auto add8 = [&](float (&c)[8], u32x4 t) AINL {
      c[0] += bflo(t.x); c[1] += bfhi(t.x); c[2] += bflo(t.y); c[3] += bfhi(t.y); c[4] += bflo(t.z); c[5] += bfhi(t.z); c[6] += bflo(t.w); c[7] += bfhi(t.w);
    };
    STAMP(1);
    {
      const rsrc_t rr_ = mk_rsrc(b_re, 640 * 1280), ri_ = mk_rsrc(b_im, 640 * 1280);
      u32x4 t[10]; u32 u[10];
#pragma unroll
      for (int i = 0; i < 10; ++i) {
        const u32 so = (m * 40 + w * 5 + (i >> 1)) * 1280;
        t[i] = bld128(l * 16, (i & 1) ? ri_ : rr_, so); u[i] = bld32(1024 + l * 4, (i & 1) ? ri_ : rr_, so);
      }
      SB();
      float rsum[10];
#pragma unroll
      for (int i = 0; i < 10; ++i)
        rsum[i] = wavesum(((bflo(t[i].x) + bfhi(t[i].x)) + (bflo(t[i].y) + bfhi(t[i].y))) + ((bflo(t[i].z) + bfhi(t[i].z)) + (bflo(t[i].w) + bfhi(t[i].w))) + (bflo(u[i]) + bfhi(u[i])));
      const float vr = l == 0 ? rsum[0] : l == 1 ? rsum[2] : l == 2 ? rsum[4] : l == 3 ? rsum[6] : rsum[8];
      const float vi = l == 0 ? rsum[1] : l == 1 ? rsum[3] : l == 2 ? rsum[5] : l == 3 ? rsum[7] : rsum[9];
      st128_sc1((u32x4){asu(vr), ta, asu(vi), tb}, l < 5 ? WS_RS + (m * 40 + w * 5 + l) * 16 : OOB, rws);
    }
    if (m < 12) {
      const int c = m;
      const rsrc_t rw3 = mk_rsrc(w3, 640 * 3072);
      float cs[8] = {0.f, 0.f, 0.f, 0.f, 0.f, 0.f, 0.f, 0.f};
      const u32 vo = (tid >> 4) * 3072 + c * 256 + (tid & 15) * 16;
      u32x4 t[20];
#pragma unroll
      for (int i = 0; i < 20; ++i) t[i] = bld128(vo, rw3, i * 32 * 3072);
      SB();
#pragma unroll
      for (int i = 0; i < 20; ++i) add8(cs, t[i]);
#pragma unroll
      for (int e = 0; e < 8; ++e) cs[e] = sum32(sum16(cs[e]));
      if (l < 16) {
        *(LAS f32x4*)(sm + w * 512 + l * 32) = (f32x4){cs[0], cs[1], cs[2], cs[3]};
        *(LAS f32x4*)(sm + w * 512 + l * 32 + 16) = (f32x4){cs[4], cs[5], cs[6], cs[7]};
      }
      bar();
      if (w < 2) {
        float s = 0.f;
#pragma unroll
        for (int i = 0; i < 8; ++i) s += *(LAS float*)(sm + i * 512 + tid * 4);
        st32_sc1(asu(s), WS_CS3 + (c * 128 + tid) * 4, rws);
      }
      waitbar<0>();
      if (w == 0) set_flag(FL_C + c);
    }
    {
      const rsrc_t rw4 = mk_rsrc(w4, 1536 * 1280);
      u32 d = 0;
      if (m < 12) { touch(d, (m * 640 + tid) * 128, rw4); touch(d, tid < 128 ? (m * 640 + 512 + tid) * 128 : OOB, rw4); }
      else {
#pragma unroll
        for (int i = 0; i < 4; ++i) touch(d, i < 3 || tid < 384 ? (7680 + (m - 12) * 1920 + i * 512 + tid) * 128 : OOB, rw4);
      }
      asm volatile("s_waitcnt vmcnt(0)" : "+v"(d)::"memory");
    }
    STAMP(2);
    if (m == 0) {
#pragma unroll
      for (int i = 0; i < 4; ++i) (void)poll_granule(WS_XS + (i * 512 + tid) * 16, true, rws, ta, tb);
      bar();
      if (tid == 0) st64_sc1((u32x2){ta, tb}, WS_EPOCH, rws);
    }
  }
#endif
}

__global__ void post_k(unsigned* p) {
#if defined(__HIP_DEVICE_COMPILE__) && __HIP_DEVICE_COMPILE__
  const unsigned long long t0 = __builtin_amdgcn_s_memrealtime();
  unsigned mn = ~0u;
  for (int i = 0; i < 4; ++i) { const int b = i * 64 + threadIdx.x; if ((b >> 3) < 20) mn = min(mn, (unsigned)t0 - p[b * 16]); }
  for (int o = 1; o < 64; o <<= 1) mn = min(mn, (unsigned)__shfl_xor(mn, o));
  const unsigned long long e = __builtin_amdgcn_s_memrealtime() + 0 * (unsigned long long)__builtin_amdgcn_readfirstlane(mn);
  while (__builtin_amdgcn_s_memrealtime() < e) __builtin_amdgcn_s_sleep(2);
#endif
}
extern "C" void kernel_launch(void* const* d_in, const int* in_sizes, int n_in, void* d_out, int out_size,
                              void* d_ws, size_t ws_size, hipStream_t stream) {
  (void)in_sizes; (void)n_in; (void)out_size; (void)ws_size;
  s5_mixer_kernel<<<256, 768, 0, stream>>>(d_in[0], d_in[1], d_in[2], d_in[3], d_in[4], d_in[5], d_in[6], d_in[7],
                                           d_in[8], d_in[10], d_in[11], d_in[12], d_in[13], d_out, d_ws);
  post_k<<<1, 64, 0, stream>>>((unsigned*)((char*)d_ws + WS_STAMP));
}
```

```cpp
#include <hip/hip_runtime.h>
#include <hip/hip_bf16.h>
#include <stdint.h>

#define DI __device__ __forceinline__
#define LAS __attribute__((address_space(3)))
#define AINL __attribute__((always_inline))
typedef unsigned u32;
typedef unsigned long long u64;
typedef u32 u32x4 __attribute__((ext_vector_type(4)));
typedef u32 u32x2 __attribute__((ext_vector_type(2)));
typedef float f32x4 __attribute__((ext_vector_type(4)));
typedef float f32x2 __attribute__((ext_vector_type(2)));
typedef __bf16 bf16x8 __attribute__((ext_vector_type(8)));
typedef __bf16 bf16x2 __attribute__((ext_vector_type(2)));
typedef short s16x4 __attribute__((ext_vector_type(4)));
typedef short s16x8 __attribute__((ext_vector_type(8)));

#define XMAP 1
#define ABL 0
#define PSLEEP 2
#define WARM 1
#define IBW 1
#define STAMPS 0

constexpr u32 WS_EPOCH = 0;
constexpr u32 WS_CF = 0x1000;
constexpr u32 WS_IB = 0x340000;
constexpr u32 IB_B = 640, IB_A = 160 * 640;
constexpr u32 WS_XS = 0x20000;
constexpr u32 WS_CS3 = 0x2A000;
constexpr u32 WS_RS = 0x58000;
constexpr u32 WS_EG = 0x30000;
constexpr u32 WS_ST1 = 0x60000;
constexpr u32 WS_SS = 0xC0000;
constexpr u32 WS_H1 = 0x400000;
constexpr u32 WS_X1 = 0x680000;
constexpr u32 WS_HH = 0x900000;
constexpr u32 WS_H2 = 0xE00000;
constexpr u32 WS_STAMP = 0x1400000;
constexpr u32 WS_BYTES = 0x1500000;
constexpr u32 OOB = 0x7FFFFFF0u;
constexpr float LN_EPS = 1e-5f;
constexpr int SPIN_CAP = 1 << 17;

template <int I> struct IC { static constexpr int v = I; };
template <int B, int E, class F> DI void sfor(F&& f) {
  if constexpr (B < E) { f(IC<B>{}); sfor<B + 1, E>(f); }
}

#if defined(__HIP_DEVICE_COMPILE__) && __HIP_DEVICE_COMPILE__
typedef __amdgpu_buffer_rsrc_t rsrc_t;
DI rsrc_t mk_rsrc(const void* p, u32 bytes) { return __builtin_amdgcn_make_buffer_rsrc((void*)p, 0, bytes, 0x00020000); }

template <int SC1> DI void dma1(u32 lds, u32 voff, rsrc_t r, u32 soff) {
  if (ABL == 2) return;
  if constexpr (SC1)
    asm volatile("s_mov_b32 m0, %0\n\ts_nop 0\n\tbuffer_load_dwordx4 %1, %2, %3 offen sc1 lds" ::"s"(lds), "v"(voff), "s"(r), "s"(soff) : "memory");
  else
    asm volatile("s_mov_b32 m0, %0\n\ts_nop 0\n\tbuffer_load_dwordx4 %1, %2, %3 offen lds" ::"s"(lds), "v"(voff), "s"(r), "s"(soff) : "memory");
}
template <int SC1> DI void dma2(u32 lds, u32 v0, u32 v1, rsrc_t r, u32 soff) {
  if (ABL == 2) return;
  if constexpr (SC1)
    asm volatile("s_mov_b32 m0, %0\n\ts_nop 0\n\tbuffer_load_dwordx4 %1, %3, %4 offen sc1 lds\n\tbuffer_load_dwordx4 %2, %3, %4 offen offset:1024 sc1 lds"
                 ::"s"(lds), "v"(v0), "v"(v1), "s"(r), "s"(soff) : "memory");
  else
    asm volatile("s_mov_b32 m0, %0\n\ts_nop 0\n\tbuffer_load_dwordx4 %1, %3, %4 offen lds\n\tbuffer_load_dwordx4 %2, %3, %4 offen offset:1024 lds"
                 ::"s"(lds), "v"(v0), "v"(v1), "s"(r), "s"(soff) : "memory");
}
template <int SC1> DI void dma4(u32 lds, u32 v, rsrc_t r, u32 soff) {
  if (ABL == 2) return;
  if constexpr (SC1)
    asm volatile("s_mov_b32 m0, %0\n\ts_nop 0\n\tbuffer_load_dwordx4 %1, %2, %3 offen sc1 lds\n\tbuffer_load_dwordx4 %1, %2, %3 offen offset:1024 sc1 lds\n\t"
                 "buffer_load_dwordx4 %1, %2, %3 offen offset:2048 sc1 lds\n\tbuffer_load_dwordx4 %1, %2, %3 offen offset:3072 sc1 lds"
                 ::"s"(lds), "v"(v), "s"(r), "s"(soff) : "memory");
  else
    asm volatile("s_mov_b32 m0, %0\n\ts_nop 0\n\tbuffer_load_dwordx4 %1, %2, %3 offen lds\n\tbuffer_load_dwordx4 %1, %2, %3 offen offset:1024 lds\n\t"
                 "buffer_load_dwordx4 %1, %2, %3 offen offset:2048 lds\n\tbuffer_load_dwordx4 %1, %2, %3 offen offset:3072 lds"
                 ::"s"(lds), "v"(v), "s"(r), "s"(soff) : "memory");
}
constexpr int VC(int n) { return ABL == 2 ? 0 : n; }
template <int N> DI void waitv() { asm volatile("s_waitcnt vmcnt(%0)" ::"i"(VC(N)) : "memory"); }
DI void bar() { asm volatile("s_waitcnt lgkmcnt(0)\n\ts_barrier" ::: "memory"); }
template <int N> DI void waitbar() { asm volatile("s_waitcnt vmcnt(%0) lgkmcnt(0)\n\ts_barrier" ::"i"(VC(N)) : "memory"); }

DI void touch(u32& d, u32 voff, rsrc_t r) { asm volatile("buffer_load_dword %0, %1, %2, 0 offen" : "+v"(d) : "v"(voff), "s"(r) : "memory"); }

DI u32x4 bld128(u32 voff, rsrc_t r, u32 soff) { return __builtin_amdgcn_raw_buffer_load_b128(r, voff, soff, 0); }
DI u32x2 bld64(u32 voff, rsrc_t r, u32 soff) { return __builtin_amdgcn_raw_buffer_load_b64(r, voff, soff, 0); }
DI u32x2 bld64_sc1(u32 voff, rsrc_t r, u32 soff) { return __builtin_amdgcn_raw_buffer_load_b64(r, voff, soff, 16); }
DI u32 bld32(u32 voff, rsrc_t r, u32 soff) { return __builtin_amdgcn_raw_buffer_load_b32(r, voff, soff, 0); }
DI u32 bld16(u32 voff, rsrc_t r) { return __builtin_amdgcn_raw_buffer_load_b16(r, voff, 0, 0); }
DI void st128_sc1(u32x4 v, u32 voff, rsrc_t r) { __builtin_amdgcn_raw_buffer_store_b128(v, r, voff, 0, 16); }
DI void st64_sc1(u32x2 v, u32 voff, rsrc_t r) { __builtin_amdgcn_raw_buffer_store_b64(v, r, voff, 0, 16); }
DI void st32_sc1(u32 v, u32 voff, rsrc_t r) { __builtin_amdgcn_raw_buffer_store_b32(v, r, voff, 0, 16); }

DI void poll_flags(u32 voff, bool active, rsrc_t ws, u32 ta, u32 tb) {
  _Pragma("nounroll") for (int it = 0; it < SPIN_CAP; ++it) {
    u32x2 v;
    asm volatile("buffer_load_dwordx2 %0, %1, %2, 0 offen sc1\n\ts_waitcnt vmcnt(0)" : "=&v"(v) : "v"(voff), "s"(ws) : "memory");
    const bool ok = !active || (v.x == ta && v.y == tb);
    if (__all(ok)) break;
    __builtin_amdgcn_s_sleep(1);
  }
}
DI u32x4 poll_granule(u32 voff, bool active, rsrc_t ws, u32 ta, u32 tb) {
  u32x4 v;
  _Pragma("nounroll") for (int it = 0; it < SPIN_CAP; ++it) {
    asm volatile("buffer_load_dwordx4 %0, %1, %2, 0 offen sc1\n\ts_waitcnt vmcnt(0)" : "=&v"(v) : "v"(voff), "s"(ws) : "memory");
    const bool ok = !active || (v.y == ta && v.w == tb);
    if (__all(ok)) break;
    __builtin_amdgcn_s_sleep(1);
  }
  return v;
}

DI float asf(u32 x) { return __uint_as_float(x); }
DI u32 asu(float x) { return __float_as_uint(x); }
DI float bflo(u32 x) { return asf(x << 16); }
DI float bfhi(u32 x) { return asf(x & 0xffff0000u); }
DI u32 pk2(float a, float b) { f32x2 p = {a, b}; bf16x2 q = __builtin_convertvector(p, bf16x2); return __builtin_bit_cast(u32, q); }
DI float sum16(float x) { const u32 u = asu(x); auto r = __builtin_amdgcn_permlane16_swap(u, u, false, false); const u32 a = r[0], b = r[1]; return asf(a) + asf(b); }
DI float sum32(float x) { const u32 u = asu(x); auto r = __builtin_amdgcn_permlane32_swap(u, u, false, false); const u32 a = r[0], b = r[1]; return asf(a) + asf(b); }
template <int C> DI float dppadd(float x) { return x + asf(__builtin_amdgcn_update_dpp(0u, asu(x), C, 0xf, 0xf, true)); }
DI float wavesum(float x) { x = dppadd<0xB1>(x); x = dppadd<0x4E>(x); x = dppadd<0x141>(x); x = dppadd<0x140>(x); return sum32(sum16(x)); }
DI void gather4(float x, float (&e)[4]) {
  const u32 u = asu(x);
  auto p = __builtin_amdgcn_permlane16_swap(u, u, false, false);
  const u32 a = p[0], b = p[1];
  auto pa = __builtin_amdgcn_permlane32_swap(a, a, false, false);
  auto pb = __builtin_amdgcn_permlane32_swap(b, b, false, false);
  const u32 e0 = pa[0], e2 = pa[1], e1 = pb[0], e3 = pb[1];
  e[0] = asf(e0); e[1] = asf(e1); e[2] = asf(e2); e[3] = asf(e3);
}
typedef f32x4 GeluK;
DI GeluK gelu_k(float r, float ri) { return (f32x4){-2.302208198f * r, -0.1029432397f * r * r * r, ri, 0.f}; }
DI u32 gelu2(f32x2 t, GeluK k) {
  const f32x2 u = t * __builtin_elementwise_fma(t * t, (f32x2){k.y, k.y}, (f32x2){k.x, k.x});
  const f32x2 e = {__builtin_amdgcn_exp2f(u.x), __builtin_amdgcn_exp2f(u.y)};
  const f32x2 d = __builtin_elementwise_fma(e, (f32x2){k.z, k.z}, (f32x2){k.z, k.z});
  const f32x2 o = t * (f32x2){__builtin_amdgcn_rcpf(d.x), __builtin_amdgcn_rcpf(d.y)};
  return __builtin_bit_cast(u32, __builtin_convertvector(o, bf16x2));
}
DI u32x2 gelu4(f32x4 acc, f32x4 cs, float mu, GeluK k) {
  const f32x2 nm = {-mu, -mu};
  return (u32x2){gelu2(__builtin_elementwise_fma(nm, cs.xy, acc.xy), k), gelu2(__builtin_elementwise_fma(nm, cs.zw, acc.zw), k)};
}
DI float silu_f(float z) { return z * __builtin_amdgcn_rcpf(1.0f + __builtin_amdgcn_exp2f(z * -1.4426950409f)); }
DI f32x4 mfma(bf16x8 a, bf16x8 b, f32x4 c) { if (ABL == 1 || ABL == 4) { asm volatile("" : "+v"(c) : "v"(a), "v"(b)); return c; } return __builtin_amdgcn_mfma_f32_16x16x32_bf16(a, b, c, 0, 0, 0); }
DI bf16x8 lds128(LAS char* p) { if (ABL == 1 || ABL == 3) { bf16x8 r; asm volatile("" : "=v"(r)); return r; } return *(LAS bf16x8*)p; }
DI bf16x8 ldstr(LAS char* p0, LAS char* p1) {
  if (ABL == 1 || ABL == 3) { bf16x8 r; asm volatile("" : "=v"(r)); return r; }
  const s16x4 a = __builtin_amdgcn_ds_read_tr16_b64_v4i16((LAS s16x4*)p0);
  const s16x4 b = __builtin_amdgcn_ds_read_tr16_b64_v4i16((LAS s16x4*)p1);
  const s16x8 w = __builtin_shufflevector(a, b, 0, 1, 2, 3, 4, 5, 6, 7);
  return __builtin_bit_cast(bf16x8, w);
}
DI u32x4 pack8(f32x4 a, f32x4 b) { u32x4 r; r.x = pk2(a.x, a.y); r.y = pk2(a.z, a.w); r.z = pk2(b.x, b.y); r.w = pk2(b.z, b.w); return r; }
DI u32x2 pack4(f32x4 a) { u32x2 r; r.x = pk2(a.x, a.y); r.y = pk2(a.z, a.w); return r; }
DI int ssel_lt(int a, int b, int x, int y) { int r; asm("s_cmp_lt_i32 %1, %2\n\ts_cselect_b32 %0, %3, %4" : "=s"(r) : "s"(a), "s"(b), "s"(x), "s"(y) : "scc"); return r; }
DI int wrapk(int k, int n) { return k >= n ? k - n : k; }
#define SB() __builtin_amdgcn_sched_barrier(0)
DI f32x4 keep(f32x4 v) { asm volatile("" : "+v"(v)); return v; }
DI LAS char* pin(LAS char* p) { asm volatile("" : "+v"(p)); return p; }
#endif

#define STAMP(i) do { } while (0)
#define CSTAMP(i) do { } while (0)

__global__ __launch_bounds__(768, 3) void s5_mixer_kernel(
    const void* __restrict__ x, const void* __restrict__ lam_re, const void* __restrict__ lam_im,
    const void* __restrict__ b_re, const void* __restrict__ b_im, const void* __restrict__ c_re,
    const void* __restrict__ c_im, const void* __restrict__ log_dt, const void* __restrict__ dskip,
    const void* __restrict__ w1, const void* __restrict__ w2, const void* __restrict__ w3,
    const void* __restrict__ w4, void* __restrict__ out, void* __restrict__ ws) {
#if defined(__HIP_DEVICE_COMPILE__) && __HIP_DEVICE_COMPILE__
  __shared__ __attribute__((aligned(16))) char smem_[163840];
  LAS char* const sm = (LAS char*)smem_;
  const u32 L0 = (u32)(size_t)sm;
  const int tid = threadIdx.x, l = tid & 63;
  const int w = __builtin_amdgcn_readfirstlane(tid >> 6);
  const int bid = blockIdx.x, xcd = bid & 7, rr = bid >> 3;
  const int m = (xcd >> 1) * 4 + (rr < 20 ? rr / 5 : (rr - 20) / 3);
  const int role = rr < 20 ? rr % 5 + 5 * (xcd & 1) : 10 + (rr - 20) % 3 + 3 * (xcd & 1);
  const int g = l >> 4, li = l & 15, r8 = l >> 3, tq = w & 3, cq = w >> 2, L = w - 8;
  const rsrc_t rws = mk_rsrc(ws, WS_BYTES);
  const rsrc_t rx = mk_rsrc(x, 2048 * 1280);
  STAMP(0); CSTAMP(30);
  const int fm = ((5 * (m & 3) + 1) >> 1) + (m >> 2);
  const int rot1 = (2 * (role % 5) + (role >= 5) + fm) % 10, rotA1 = (2 * (role % 5) + 1 + fm) % 10;

  u32 ta, tb;
  auto row_stats = [&]() AINL {
    const int stok = m * 128 + role * 8 + w;
    const u32x2 ep = bld64_sc1(0, rws, 0);
    const u32x4 ra = bld128(l * 16, rx, stok * 1280);
    const u32 rb_ = bld32(1024 + l * 4, rx, stok * 1280);
    const u64 e = (((u64)__builtin_amdgcn_readfirstlane(ep.y) << 32) | (u32)__builtin_amdgcn_readfirstlane(ep.x)) + 0x9E3779B97F4A7C15ull;
    ta = (u32)e; tb = (u32)(e >> 32);
    float v[10] = {bflo(ra.x), bfhi(ra.x), bflo(ra.y), bfhi(ra.y), bflo(ra.z), bfhi(ra.z), bflo(ra.w), bfhi(ra.w), bflo(rb_), bfhi(rb_)};
    float s = 0.f;
#pragma unroll
    for (int i = 0; i < 10; ++i) s += v[i];
    const float mu = wavesum(s) * (1.0f / 640.0f);
    float q = 0.f;
#pragma unroll
    for (int i = 0; i < 10; ++i) { const float d = v[i] - mu; q = __builtin_fmaf(d, d, q); }
    const float rs = 1.0f / sqrtf(wavesum(q) * (1.0f / 640.0f) + LN_EPS);
    u32x4 gr = {asu(mu), ta, asu(rs), tb};
    st128_sc1(gr, l == 0 ? WS_XS + stok * 16 : OOB, rws);
  };
  auto put_tag = [&](u32 off) AINL { if (tid == 0) *(LAS u32x2*)(sm + off) = (u32x2){ta, tb}; };
  auto get_tag = [&](u32 off) AINL {
    const u32x2 t = *(volatile LAS u32x2*)(sm + off);
    ta = __builtin_amdgcn_readfirstlane(t.x); tb = __builtin_amdgcn_readfirstlane(t.y);
  };
  auto signal = [&](u32 off) AINL { st64_sc1((u32x2){ta, tb}, off, rws); };
  auto await = [&](auto nv, u32 sig, u32 src, bool act, auto&& pre) AINL {
    waitbar<nv.v>();
    if (L == 0) { signal(sig); poll_flags(src, act, rws, ta, tb); }
    else pre();
    bar();
  };
  const u32 rb0 = li * 128 + ((g ^ (li >> 1)) << 4);
  const f32x4 zero4 = {0.f, 0.f, 0.f, 0.f};

  if (role < 10) {
    const int j = role;
    constexpr u32 ACT = 0, W = 65536;
    const int b0 = j < 9 ? 5 * j : 44, nb = j < 8 ? 5 : 4, kt0 = (b0 + 1) >> 1;
    if (w >= 8) {
      const rsrc_t rw1 = mk_rsrc(w1, 640 * 1280), rw2 = mk_rsrc(w2, 640 * 1280);
      const rsrc_t rw3 = mk_rsrc(w3, 640 * 3072), rw4 = mk_rsrc(w4, 1536 * 1280);
      const u32 vx0 = (m * 128 + L * 32 + r8) * 1280 + (((l & 7) ^ g) << 4);
      const u32 vx1 = (m * 128 + L * 32 + 8 + r8) * 1280 + (((l & 7) ^ (4 + g)) << 4) - 1024;
      const u32 wch = j * 128 + (((l & 7) ^ (2 * g)) << 4);
      const u32 vq0 = ((L >> 1) * 32 + (r8 >> 2) * 8 + (L & 1) * 4 + (r8 & 3)) * 1280 + wch, vq1 = vq0 + 16 * 1280 - 1024;
      const u32 ldsA = L0 + ACT + L * 4096, ldsW = L0 + W + L * 2048;
      auto issue_1 = [&](int s) AINL {
        const int kt = wrapk(s + rot1, 10);
        dma2<0>(ldsA + (s & 3) * 16384, vx0, vx1, rx, kt * 128);
        dma2<0>(ldsW + 65536 + (s & 3) * 8192, vq0, vq1, rw1, kt * 81920);
        dma2<0>(ldsA + (s & 3) * 16384 + 2048, vx0, vx1, rx, kt * 128 + 20480);
      };
      issue_1(0); issue_1(1); issue_1(2);
      const u32 vw0 = (L * 16 + r8) * 1280 + wch, vw1 = vw0 + 10240 - 1024;
      const u32 va = L * 4096 + l * 16, vl = L * 1024 + l * 16;
      auto k3 = [&](int s) AINL { return s == 0 ? j : ssel_lt(s - 1, j, s - 1, s); };
      auto k4 = [&](int s) AINL { return s < 2 ? kt0 + s : ssel_lt(s - 2, kt0, s - 2, s); };
      u32 v3[5];
#pragma unroll
      for (int i = 0; i < 5; ++i) {
        const int c = 64 * i + l, r = c / 20, pos = c - 20 * r;
        v3[i] = (L * 16 + r) * 3072 + b0 * 64 + ((pos ^ (2 * ((r >> 2) & 1))) << 4) - 1024 * (i & 1);
      }
      auto issue_w2 = [&](int s) AINL { dma2<0>(ldsW + s * 8192, vw0, vw1, rw2, s * 81920); };
      auto issue_w3 = [&](int s) AINL {
        const u32 d = L0 + W + (s & 3) * 20480 + L * 5120, so = k3(s) * 196608;
        dma2<0>(d, v3[0], v3[1], rw3, so); dma2<0>(d + 2048, v3[2], v3[3], rw3, so); dma1<0>(d + 4096, v3[4], rw3, so);
      };
      const u32 vz0 = r8 * 1280 + wch, vz1 = vz0 + 10240 - 1024;
      auto whole_w = [&](u32 d, rsrc_t r, u32 so) AINL {
#pragma unroll
        for (int i = 0; i < 4; ++i) dma2<0>(d + i * 2048, vz0, vz1, r, so + i * 20480);
      };
      auto issue_w4 = [&](int s) AINL { dma2<0>(ldsW + ((s + 5) % 12) * 8192, vw0, vw1, rw4, k4(s) * 81920); };
      auto issue_a = [&](int s, u32 base) AINL { dma4<1>(ldsA + (s & 3) * 16384, va, rws, base); };
      auto issue_h1 = [&](int s) AINL {
#pragma unroll
        for (int ks = 0; ks < 2; ++ks) {
          const int b = 2 * s + ks, grp = b / 5;
          const int t = m * 128 + l - ((grp & 1) ? -(1 + (grp >> 1)) : (1 + (grp >> 1)));
          dma2<1>(L0 + ACT + (s & 3) * 16384 + ks * 8192 + L * 2048, t >= 0 ? t * 16 + 32 : OOB, t < 2048 - 64 ? t * 16 + 32 : OOB, rws, WS_H1 + (b * 4 + L) * 32768 - 32);
        }
      };
      const bf16x8 ones = __builtin_bit_cast(bf16x8, (u32x4){0x3F803F80u, 0x3F803F80u, 0x3F803F80u, 0x3F803F80u});
      LAS char* const wl = pin(sm + W + 65536 + (4 * g + (li >> 2)) * 128 + ((L ^ ((2 * g + (li >> 3)) & 3)) << 5) + 8 * (l & 3));
      f32x4 cs = zero4;
      sfor<0, 10>([&](auto ic) AINL {
        constexpr int s = ic.v;
        constexpr int gs[13] = {6, 6, 6, 6, 6, 6, 6, 6, 6, 6, 5, 6, 6};
        waitbar<gs[s + 1] + gs[s + 2]>();
        if constexpr (s + 3 < 10) issue_1(s + 3);
        else if constexpr (s == 7) { dma1<1>(L0 + ACT + 2 * 16384 + L * 1024, vl, rws, WS_XS + m * 2048); issue_w2(0); issue_w2(1); }
        else { issue_w2(3 * s - 22); issue_w2(3 * s - 21); issue_w2(3 * s - 20); }
        if constexpr (s == 0) get_tag(W);
        const bf16x8 c0 = ldstr(wl + (s & 3) * 8192, wl + (s & 3) * 8192 + 2048), c1 = ldstr(wl + (s & 3) * 8192 + 4096, wl + (s & 3) * 8192 + 6144);
        cs = keep(mfma(c1, ones, mfma(c0, ones, cs)));
      });
      if (li == 0) *(LAS f32x4*)(sm + ACT + 2 * 16384 + 4096 + L * 64 + g * 16) = cs;
      waitbar<16>();
      const u32 ib = WS_IB + (m * 10 + j) * IB_B, ibt = WS_IB + (m * 10 + l) * IB_B;
      const int j5 = j % 5;
      await(IC<63>{},
            l < 10 ? ibt + j * 8 : l < 20 ? (j5 < 3 && m < 15 ? ibt + (10 + j5 + 3 * (j / 5)) * 8 : OOB)
                   : (l < 30 && j5 >= 2 && m > 0 ? ibt - 30 * IB_B + (14 + j5 + 3 * (j / 5)) * 8 : OOB),
            ib + l * 8, l < 10 || (l < 16 && m > 0) || (l >= 16 && l < 22 && m < 15),
            [&]() AINL { if (L < 3) whole_w(L0 + W + (7 + L) * 8192, rw2, (7 + L) * 81920); });
      issue_h1(0); issue_h1(1); issue_h1(2);
      sfor<0, 10>([&](auto ic) AINL {
        constexpr int s = ic.v;
        constexpr int gs[13] = {4, 4, 4, 4, 4, 4, 4, 4, 4, 4, 5, 5, 5};
        waitbar<gs[s + 1] + gs[s + 2]>();
        if constexpr (s + 3 < 10) issue_h1(s + 3);
        else issue_w3(s - 7);
      });
      await(IC<10>{}, l < 10 ? ibt + 256 + j * 8 : OOB, l < 10 ? ib + 256 + l * 8 : WS_CF + ((b0 >> 2) + l - 10) * 128, l < (j < 9 ? 12 : 11), [&]() AINL {
        issue_w3(3);
        if (L == 1) {
          const u32 d = L0 + W + 3 * 20480, so = k3(3) * 196608;
          dma2<0>(d, v3[0] - 49152, v3[1] - 49152, rw3, so); dma2<0>(d + 2048, v3[2] - 49152, v3[3] - 49152, rw3, so); dma1<0>(d + 4096, v3[4] - 49152, rw3, so);
        }
      });
      sfor<1, 4>([&](auto ic) AINL { constexpr int s = ic.v; issue_a(s, WS_X1 + (m * 10 + k3(s)) * 16384); });
      sfor<1, 10>([&](auto ic) AINL {
        constexpr int s = ic.v;
        constexpr int gs[13] = {0, 4, 4, 4, 9, 9, 9, 9, 9, 9, 8, 8, 8};
        waitbar<gs[s + 1] + gs[s + 2]>();
        if constexpr (s + 3 < 10) {
          issue_a(s + 3, WS_X1 + (m * 10 + k3(s + 3)) * 16384);
          issue_w3(s + 3);
        } else if constexpr (s == 7) {
          issue_a(2, WS_ST1 + m * 20480);
          issue_w4(0); issue_w4(1);
        } else if constexpr (s == 8) {
          dma1<1>(L0 + ACT + 3 * 16384 + L * 1024, vl, rws, WS_ST1 + m * 20480 + 16384);
          dma1<1>(L0 + ACT + 3 * 16384 + 4096 + L * 1024, vl, rws, WS_CS3 + b0 * 128);
          issue_w4(2); issue_w4(3); issue_w4(4);
        } else { issue_w4(5); issue_w4(6); issue_w4(7); issue_w4(8); }
      });
      waitbar<14>();
      await(IC<63>{}, l < 10 ? ibt + 384 + j * 8 : OOB, ib + 384 + l * 8, l < 10,
            [&]() AINL { whole_w(L0 + W + (1 + L) * 8192, rw4, k4(8 + L) * 81920); });
      sfor<2, 5>([&](auto ic) AINL { constexpr int s = ic.v; issue_a(s, WS_H2 + (m * 24 + k4(s)) * 16384); });
      u32x2 fs;
      const u32 fso = ib + 512;
      sfor<2, 24>([&](auto ic) AINL {
        constexpr int s = ic.v;
        constexpr int gs[27] = {0, 0, 4, 4, 4, 6, 6, 6, 6, 6, 6, 6, 6, 6, 6, 6, 6, 4, 4, 4, 5, 4, 4, 4, 4, 0, 0};
        if constexpr (s == 20) { asm volatile("s_waitcnt vmcnt(%1) lgkmcnt(0)\n\ts_barrier" : "+v"(fs) : "i"(VC(gs[s + 1] + gs[s + 2])) : "memory"); }
        else waitbar<gs[s + 1] + gs[s + 2]>();
        if constexpr (s == 17) asm volatile("buffer_load_dwordx2 %0, %1, %2, 0 offen sc1" : "=v"(fs) : "v"(fso), "s"(rws) : "memory");
        if constexpr (s + 3 < 24) issue_a(s + 3, WS_H2 + (m * 24 + k4(s + 3)) * 16384);
        if constexpr (s <= 13) issue_w4(s + 10);
        if constexpr (s == 21) {
          if (!__all(fs.x == ta && fs.y == tb)) poll_flags(fso, true, rws, ta, tb);
          issue_a(0, WS_SS + (m * 10 + j) * 16384);
        }
      });
      waitbar<0>();
    } else {
      row_stats();
      put_tag(W);
      const int q4 = li >> 2, p4 = l & 3;
      LAS char* const wb0 = pin(sm + W + (4 * g + q4) * 128 + ((((2 * cq) ^ ((2 * g + (q4 >> 1)) & 3))) << 5) + 8 * p4);
      LAS char* const wb1 = pin(sm + W + (4 * g + q4) * 128 + ((((2 * cq + 1) ^ ((2 * g + (q4 >> 1)) & 3))) << 5) + 8 * p4);
      LAS char* const wh0 = pin(wb0 + 65536);
      LAS char* const wh1 = pin(wb1 + 65536);
      LAS char* const ab = pin(sm + ACT + tq * 4096 + l * 16);
      struct F64 { bf16x8 a0, a1, b0, b1; };
      F64 F0, F1;
      f32x4 acc[2][2], cs[2];
      u32x2 xs[2][2];
      auto step64 = [&](auto mf, auto tts, F64& R, LAS char* pb, u32 wo, const F64& M) AINL {
        LAS char* const p0 = wo >= 65536 ? wh0 + (wo - 65536) : wb0 + wo;
        LAS char* const p1 = wo >= 65536 ? wh1 + (wo - 65536) : wb1 + wo;
        R.a0 = ldstr(p0, p0 + 2048); SB();
        if constexpr (mf.v) acc[0][0] = mfma(M.a0, M.b0, acc[0][0]);
        SB(); R.b0 = lds128(pb); R.b1 = lds128(pb + tts.v); SB();
        if constexpr (mf.v) acc[1][0] = mfma(M.a1, M.b0, acc[1][0]);
        SB(); R.a1 = ldstr(p1, p1 + 2048); SB();
        if constexpr (mf.v) { acc[0][1] = mfma(M.a0, M.b1, acc[0][1]); acc[1][1] = mfma(M.a1, M.b1, acc[1][1]); }
        SB();
      };
      auto mm = [&](const F64& M) AINL {
        acc[0][0] = mfma(M.a0, M.b0, acc[0][0]); acc[1][0] = mfma(M.a1, M.b0, acc[1][0]);
        acc[0][1] = mfma(M.a0, M.b1, acc[0][1]); acc[1][1] = mfma(M.a1, M.b1, acc[1][1]);
      };
      float mu[2], rs[2];
      {
        LAS char* const xb0 = pin(sm + ACT + tq * 4096 + rb0);
        LAS char* const xb1 = pin(sm + ACT + tq * 4096 + (rb0 ^ 64));
#pragma unroll
        for (int a = 0; a < 2; ++a) { acc[a][0] = zero4; acc[a][1] = zero4; }
        const int s_own = wrapk(j - rot1 + 10, 10);
        LAS char* const xo0 = pin(sm + ACT + tq * 4096 + li * 128 + (((4 * cq + (g >> 1)) ^ (li >> 1)) << 4) + 8 * (g & 1));
        LAS char* const xo1 = pin(sm + ACT + tq * 4096 + li * 128 + (((4 * cq + 2 + (g >> 1)) ^ (li >> 1)) << 4) + 8 * (g & 1));
        u32 wdum = 0;
        if (WARM && w == 7) touch(wdum, l < 40 ? ((m * 10 + j) * 40 + l) * 128 : OOB, mk_rsrc(w2, 640 * 1280));
        if (IBW && w == 6) touch(wdum, l < 5 ? WS_IB + (m * 10 + j) * IB_B + l * 128 : OOB, rws);
        STAMP(1);
        sfor<0, 10>([&](auto ic) AINL {
          constexpr int s = ic.v;
          constexpr u32 so = (s & 3) * 16384, wo = 65536 + (s & 3) * 8192;
          bar();
          if constexpr (s == 0) STAMP(16);
          if constexpr (s == 3) STAMP(20);
          if constexpr (s == 6) STAMP(21);
          step64(IC<(s > 0)>{}, IC<2048>{}, F0, xb0 + so, wo, F1);
          step64(IC<1>{}, IC<2048>{}, F1, xb1 + so, wo + 4096, F0);
          if (s == s_own) {
#pragma unroll
            for (int tt = 0; tt < 2; ++tt)
#pragma unroll
              for (int ct = 0; ct < 2; ++ct) xs[ct][tt] = *(LAS u32x2*)((ct ? xo1 : xo0) + so + tt * 2048);
          }
        });
        mm(F1);
        bar();
        STAMP(2);
        u32x4 g0 = *(LAS u32x4*)(sm + ACT + 2 * 16384 + (tq * 32 + li) * 16);
        u32x4 g1 = *(LAS u32x4*)(sm + ACT + 2 * 16384 + (tq * 32 + 16 + li) * 16);
        cs[0] = *(LAS f32x4*)(sm + ACT + 2 * 16384 + 4096 + cq * 128 + g * 16);
        cs[1] = *(LAS f32x4*)(sm + ACT + 2 * 16384 + 4096 + cq * 128 + 64 + g * 16);
        if (!__all(g0.y == ta && g0.w == tb && g1.y == ta && g1.w == tb)) {
          g0 = poll_granule(WS_XS + (m * 128 + tq * 32 + li) * 16, true, rws, ta, tb);
          g1 = poll_granule(WS_XS + (m * 128 + tq * 32 + 16 + li) * 16, true, rws, ta, tb);
        }
        mu[0] = asf(g0.x); rs[0] = asf(g0.z); mu[1] = asf(g1.x); rs[1] = asf(g1.z);
#pragma unroll
        for (int tt = 0; tt < 2; ++tt) {
          const float q2 = 1.0f - LN_EPS * rs[tt] * rs[tt] + LN_EPS;
          const float r2 = rs[tt] * __builtin_amdgcn_rsqf(q2);
          const GeluK k = gelu_k(r2, __builtin_amdgcn_rcpf(r2));
          const u32x2 h0 = gelu4(acc[0][tt], cs[0], mu[tt], k), h1 = gelu4(acc[1][tt], cs[1], mu[tt], k);
          st128_sc1((u32x4){h0.x, h0.y, h1.x, h1.y}, WS_H1 + ((2 * j + cq) * 4 + g) * 32768 + (m * 128 + tq * 32 + tt * 16 + li) * 16, rws);
          SB();
        }
        STAMP(3);
        asm volatile("s_waitcnt vmcnt(0) lgkmcnt(0)\n\ts_barrier" : "+v"(wdum)::"memory");
      }
      STAMP(4);
      bar();
      STAMP(5);
      LAS char* const abt = pin(sm + ACT + g * 2048 + (tq * 32 + li) * 16);
#pragma unroll
      for (int a = 0; a < 2; ++a) { acc[a][0] = zero4; acc[a][1] = zero4; }
      sfor<0, 10>([&](auto ic) AINL {
        constexpr int s = ic.v;
        bar();
        if constexpr (s == 0) STAMP(17);
        if constexpr (s == 3) STAMP(27);
        if constexpr (s == 6) STAMP(28);
        step64(IC<(s > 0)>{}, IC<256>{}, F0, abt + (s & 3) * 16384, s * 8192, F1);
        step64(IC<1>{}, IC<256>{}, F1, abt + (s & 3) * 16384 + 8192, s * 8192 + 4096, F0);
      });
      mm(F1);
      STAMP(6);
      u32x4 x1p[2];
      {
        float sm1[2], sq1[2];
#pragma unroll
        for (int tt = 0; tt < 2; ++tt) {
          float s1 = 0.f, s2 = 0.f;
          f32x4 x1[2];
#pragma unroll
          for (int ct = 0; ct < 2; ++ct) {
            const float xv[4] = {bflo(xs[ct][tt].x), bfhi(xs[ct][tt].x), bflo(xs[ct][tt].y), bfhi(xs[ct][tt].y)};
#pragma unroll
            for (int e = 0; e < 4; ++e) {
              const float v = __builtin_fmaf(xv[e] - mu[tt], rs[tt], acc[ct][tt][e]);
              x1[ct][e] = v; s1 += v; s2 = __builtin_fmaf(v, v, s2);
            }
          }
          x1p[tt] = pack8(x1[0], x1[1]);
          *(LAS u32x4*)(sm + ACT + (2 * tq + tt) * 2048 + cq * 1024 + l * 16) = x1p[tt];
          st128_sc1(x1p[tt], WS_X1 + (m * 10 + j) * 16384 + (2 * tq + tt) * 2048 + cq * 1024 + l * 16, rws);
          sm1[tt] = sum32(sum16(s1)); sq1[tt] = sum32(sum16(s2));
        }
        const u32x2 st = {asu(g & 1 ? sm1[1] : sm1[0]), asu(g & 1 ? sq1[1] : sq1[0])};
        st64_sc1(st, l < 32 ? WS_ST1 + m * 20480 + (2 * j + cq) * 1024 + (tq * 32 + l) * 8 : OOB, rws);
      }
      STAMP(7);
      asm volatile("" : "+v"(xs[0][0]), "+v"(xs[0][1]), "+v"(xs[1][0]), "+v"(xs[1][1]), "+v"(x1p[0]), "+v"(x1p[1]));
      waitbar<0>();
      STAMP(8);
      {
        const int sg = (g & 1) * 32;
        LAS char* const bA = pin(sm + W + (4 * g + q4) * 320 + 8 * p4 + 160 * cq + (cq ? -sg : sg));
        LAS char* const bB = pin(sm + W + (4 * g + q4) * 320 + 8 * p4 + 160 * cq + (cq ? sg : -sg));
        LAS char* const bA2 = pin(bA + 40960);
        LAS char* const bB2 = pin(bB + 40960);
        struct F3 { bf16x8 a[5], b0, b1; };
        F3 G0, G1;
        f32x4 a3[5][2];
        auto step3 = [&](auto mf, F3& R, u32 so, u32 wo, const F3& M) AINL {
          LAS char* const pA = wo >= 40960 ? bA2 + (wo - 40960) : bA + wo;
          LAS char* const pB = wo >= 40960 ? bB2 + (wo - 40960) : bB + wo;
          R.a[0] = ldstr(pA, pA + 5120); SB();
          if constexpr (mf.v) a3[0][0] = mfma(M.a[0], M.b0, a3[0][0]);
          SB(); R.b0 = lds128(ab + so); R.b1 = lds128(ab + so + 2048); SB();
          if constexpr (mf.v) a3[0][1] = mfma(M.a[0], M.b1, a3[0][1]);
          SB();
          sfor<1, 5>([&](auto ii) AINL {
            constexpr int i = ii.v;
            LAS char* const pw = ((i & 1) ? pB : pA) + 32 * i;
            R.a[i] = ldstr(pw, pw + 5120); SB();
            if constexpr (mf.v) a3[(i + 1) >> 1][(i + 1) & 1] = mfma(M.a[(i + 1) >> 1], ((i + 1) & 1) ? M.b1 : M.b0, a3[(i + 1) >> 1][(i + 1) & 1]);
            SB();
          });
          if constexpr (mf.v) {
            a3[3][0] = mfma(M.a[3], M.b0, a3[3][0]); a3[3][1] = mfma(M.a[3], M.b1, a3[3][1]);
            a3[4][0] = mfma(M.a[4], M.b0, a3[4][0]); a3[4][1] = mfma(M.a[4], M.b1, a3[4][1]);
          }
          SB();
        };
#pragma unroll
        for (int i = 0; i < 5; ++i) { a3[i][0] = zero4; a3[i][1] = zero4; }
        sfor<0, 10>([&](auto ic) AINL {
          constexpr int s = ic.v;
          constexpr u32 so = (s & 3) * 16384, wo = (s & 3) * 20480;
          if constexpr (s == 1) { bar(); STAMP(9); }
          if constexpr (s >= 1) bar();
          if constexpr (s == 1) STAMP(18);
          if constexpr (s == 3) STAMP(25);
          step3(IC<(s > 0)>{}, G0, so, wo, G1);
          step3(IC<1>{}, G1, so + 1024, wo + 10240, G0);
          if constexpr (s == 9) *(volatile LAS u32*)(sm + ACT + 3 * 16384 + 13312 + w * 4) = 0;
        });
#pragma unroll
        for (int i = 0; i < 5; ++i) { a3[i][0] = mfma(G1.a[i], G1.b0, a3[i][0]); a3[i][1] = mfma(G1.a[i], G1.b1, a3[i][1]); }
        bar();
        STAMP(10);
        float m1[2]; GeluK k1[2];
#pragma unroll
        for (int tt = 0; tt < 2; ++tt) {
          float s1 = 0.f, s2 = 0.f;
#pragma unroll
          for (int i = 0; i < 5; ++i) {
            const f32x2 v = *(LAS f32x2*)(sm + ACT + 2 * 16384 + (g + 4 * i) * 1024 + (tq * 32 + tt * 16 + li) * 8);
            s1 += v.x; s2 += v.y;
          }
          s1 = sum32(sum16(s1)) * (1.0f / 640.0f); s2 = sum32(sum16(s2)) * (1.0f / 640.0f);
          const float vq = fmaxf(__builtin_fmaf(-s1, s1, s2), 0.f) + LN_EPS, r1 = __builtin_amdgcn_rsqf(vq);
          m1[tt] = s1; k1[tt] = gelu_k(r1, vq * r1);
        }
        f32x4 c3[5];
#pragma unroll
        for (int i = 0; i < 5; ++i) c3[i] = *(LAS f32x4*)(sm + ACT + 3 * 16384 + 4096 + (80 * cq + 16 * i + 4 * g) * 4);
        auto act4 = [&](int i, int tt) AINL { return gelu4(a3[i][tt], c3[i], m1[tt], k1[tt]); };
        auto put = [&](u32x2 lo, u32x2 hi, int b, int tt) AINL {
          const u32x4 v = {lo.x, lo.y, hi.x, hi.y};
          const u32 ob = b0 + b - 2 * kt0;
          st128_sc1(v, b < nb ? WS_H2 + (m * 24 + ((b0 + b) >> 1)) * 16384 + (2 * tq + tt) * 2048 + ((b0 + b) & 1) * 1024 + l * 16 : OOB, rws);
          *(LAS u32x4*)(sm + ACT + (ob < 4 ? (ob >> 1) * 16384 + (ob & 1) * 1024 + (2 * tq + tt) * 2048 : 3 * 16384 + 12288) + l * 16) = v;
          SB();
        };
        LAS char* const xch = sm + ACT + 3 * 16384 + 8192 + tq * 1024 + l * 8;
        volatile LAS u32* const myf = (volatile LAS u32*)(sm + ACT + 3 * 16384 + 13312 + w * 4);
        auto partner = [&]() AINL {
          _Pragma("nounroll") for (int it = 0; it < SPIN_CAP; ++it)
            if (__builtin_amdgcn_readfirstlane(*(volatile LAS u32*)(sm + ACT + 3 * 16384 + 13312 + (w ^ 4) * 4))) break;
          asm volatile("" ::: "memory");
        };
        if (cq == 0) {
          *(volatile LAS u32x2*)(xch + 512) = act4(4, 1);
          *myf = 1;
          put(act4(0, 0), act4(1, 0), 0, 0); put(act4(2, 0), act4(3, 0), 1, 0);
          partner();
          put(act4(4, 0), *(LAS u32x2*)xch, 2, 0);
          put(act4(0, 1), act4(1, 1), 0, 1); put(act4(2, 1), act4(3, 1), 1, 1);
        } else {
          *(volatile LAS u32x2*)xch = act4(0, 0);
          *myf = 1;
          put(act4(1, 0), act4(2, 0), 3, 0); put(act4(3, 0), act4(4, 0), 4, 0);
          partner();
          put(*(LAS u32x2*)(xch + 512), act4(0, 1), 2, 1);
          put(act4(1, 1), act4(2, 1), 3, 1); put(act4(3, 1), act4(4, 1), 4, 1);
        }
      }
      STAMP(11);
      waitbar<0>();
      STAMP(12);
      const rsrc_t rdk = mk_rsrc(dskip, 1280);
      const u32x2 dk0 = bld64((cq * 32 + 4 * g) * 2, rdk, j * 128), dk1 = bld64((cq * 32 + 16 + 4 * g) * 2, rdk, j * 128);
#pragma unroll
      for (int a = 0; a < 2; ++a) { acc[a][0] = zero4; acc[a][1] = zero4; }
      sfor<0, 24>([&](auto ic) AINL {
        constexpr int s = ic.v;
        if constexpr (s == 2) { bar(); STAMP(13); }
        if constexpr (s >= 2) bar();
        if constexpr (s == 2) STAMP(19);
        if constexpr (s == 6) STAMP(22);
        if constexpr (s == 12) STAMP(23);
        if constexpr (s == 18) STAMP(24);
        step64(IC<(s > 0)>{}, IC<2048>{}, F0, ab + (s & 3) * 16384, ((s + 5) % 12) * 8192, F1);
        step64(IC<1>{}, IC<2048>{}, F1, ab + (s & 3) * 16384 + 1024, ((s + 5) % 12) * 8192 + 4096, F0);
      });
      mm(F1);
      bar();
      STAMP(14);
      const rsrc_t rout = mk_rsrc(out, 2048 * 2560);
#pragma unroll
      for (int tt = 0; tt < 2; ++tt) {
        const u32x4 sv = *(LAS u32x4*)(sm + ACT + w * 2048 + tt * 1024 + l * 16);
#pragma unroll
        for (int ct = 0; ct < 2; ++ct) {
          const float xv[4] = {bflo(xs[ct][tt].x), bfhi(xs[ct][tt].x), bflo(xs[ct][tt].y), bfhi(xs[ct][tt].y)};
          const u32 s0 = ct ? sv.z : sv.x, s1 = ct ? sv.w : sv.y;
          const float sy[4] = {bflo(s0), bfhi(s0), bflo(s1), bfhi(s1)};
          const u32 y0 = ct ? x1p[tt].z : x1p[tt].x, y1 = ct ? x1p[tt].w : x1p[tt].y;
          const float x1[4] = {bflo(y0), bfhi(y0), bflo(y1), bfhi(y1)};
          const u32x2 dk = ct ? dk1 : dk0;
          const float dv[4] = {bflo(dk.x), bfhi(dk.x), bflo(dk.y), bfhi(dk.y)};
          u32x4 o;
#pragma unroll
          for (int e = 0; e < 4; ++e) {
            const float sfull = __builtin_fmaf((xv[e] - mu[tt]) * rs[tt], dv[e], sy[e]);
            o[e] = asu(__builtin_fmaf(sfull, silu_f(x1[e] + acc[ct][tt][e]), xv[e]));
          }
          __builtin_amdgcn_raw_buffer_store_b128(o, rout, (m * 128 + tq * 32 + tt * 16 + li) * 2560 + (j * 64 + cq * 32 + ct * 16 + 4 * g) * 4, 0, 0);
        }
      }
      STAMP(15); CSTAMP(31);
    }
  } else if (role < 15) {
    const int a = role - 10;
    constexpr u32 MISC = 147456, CST = 157696, TAG = 159744, AS3 = 131072;
    if (w >= 8) {
      const rsrc_t rbr = mk_rsrc(b_re, 640 * 1280), rbi = mk_rsrc(b_im, 640 * 1280);
      const rsrc_t rcr = mk_rsrc(c_re, 640 * 1280), rci = mk_rsrc(c_im, 640 * 1280);
      const u32 vx0 = (m * 128 + 32 * (r8 >> 2) + 8 * L + (r8 & 3)) * 1280 + (((l & 7) ^ g) << 4);
      const u32 vx1 = (m * 128 + 32 * (2 + (r8 >> 2)) + 8 * L + (r8 & 3)) * 1280 + (((l & 7) ^ (4 + g)) << 4) - 1024;
      const u32 vb0 = (a * 128 + L * 32 + r8) * 1280 + (((l & 7) ^ g) << 4);
      const u32 vb1 = (a * 128 + L * 32 + 8 + r8) * 1280 + (((l & 7) ^ (4 + g)) << 4) - 1024;
      const u32 ldsw = L0 + L * 4096;
      auto issue_nk = [&](u32 d, rsrc_t r, u32 so) AINL { dma2<0>(d, vb0, vb1, r, so); dma2<0>(d + 2048, vb0, vb1, r, so + 20480); };
      auto issue1 = [&](int s) AINL {
        const int kt = wrapk(s + rotA1, 10);
        const u32 d = ldsw + (s % 3) * 49152;
        dma2<0>(d, vx0, vx1, rx, kt * 128); dma2<0>(d + 2048, vx0, vx1, rx, kt * 128 + 5120);
        issue_nk(d + 16384, rbr, kt * 128); issue_nk(d + 32768, rbi, kt * 128);
      };
      auto ktc = [&](int s) AINL { return s < 4 ? (s >> 1) * 10 + 2 * a + (s & 1) : ((s - 4) >> 3) * 10 + ssel_lt((s - 4) & 7, 2 * a, (s - 4) & 7, ((s - 4) & 7) + 2); };
      auto issue_c = [&](int s) AINL {
        const int kt = ktc(s);
        issue_nk(ldsw + 49152 + (s % 5) * 16384, kt < 10 ? rcr : rci, wrapk(kt, 10) * 128);
      };
      const u32 va = L * 4096 + l * 16;
      auto issue_h = [&](int s) AINL { dma4<1>(ldsw + ((s & 3) == 3 ? AS3 : (s & 3) * 16384), va, rws, WS_HH + (m * 20 + ktc(s)) * 16384); };
      issue1(0); issue1(1);
      sfor<0, 10>([&](auto ic) AINL {
        constexpr int s = ic.v;
        waitbar<(s == 9 ? 13 : 12)>();
        if constexpr (s + 2 < 10) issue1(s + 2);
        else if constexpr (s == 8) { dma1<1>(L0 + MISC + L * 1024, (L & 1) * 1024 + l * 16, rws, L < 2 ? WS_XS + m * 2048 : WS_RS + a * 2048); issue_c(0); issue_c(1); issue_c(2); }
        else { issue_c(3); issue_c(4); }
        if constexpr (s == 0) get_tag(TAG);
      });
      waitbar<20>();
      bar();
      bar();
      await(IC<0>{}, l < 5 ? WS_IB + IB_A + (m * 5 + l) * 128 + a * 8 : OOB, WS_IB + IB_A + (m * 5 + a) * 128 + l * 8, l < 5, [&]() AINL {});
      issue_h(4); issue_h(5); issue_c(5); issue_h(6); issue_c(6);
      sfor<4, 20>([&](auto ic) AINL {
        constexpr int s = ic.v;
        constexpr int gs[23] = {0, 0, 0, 0, 4, 8, 8, 8, 8, 8, 8, 8, 8, 8, 8, 8, 8, 8, 8, 8, 0, 0, 0};
        waitbar<gs[s + 1] + gs[s + 2]>();
        if constexpr (s + 3 < 20) { issue_h(s + 3); issue_c(s + 3); }
      });
    } else {
      const int p = a * 128 + w * 16 + li;
      const u32 plr = bld16(p * 2, mk_rsrc(lam_re, 1280)), pli = bld16(p * 2, mk_rsrc(lam_im, 1280)), pdt = bld16(p * 2, mk_rsrc(log_dt, 1280));
      const int pc = a * 128 + m * 8 + w;
      const rsrc_t rbr = mk_rsrc(b_re, 640 * 1280), rbi = mk_rsrc(b_im, 640 * 1280);
      const u32x4 t0 = bld128(l * 16, rbr, pc * 1280), t1 = bld128(l * 16, rbi, pc * 1280);
      const u32 u0 = bld32(1024 + l * 4, rbr, pc * 1280), u1 = bld32(1024 + l * 4, rbi, pc * 1280);
      row_stats();
      put_tag(TAG);
      {
        auto sum10 = [&](u32x4 t, u32 u) AINL {
          return wavesum(((bflo(t.x) + bfhi(t.x)) + (bflo(t.y) + bfhi(t.y))) + ((bflo(t.z) + bfhi(t.z)) + (bflo(t.w) + bfhi(t.w))) + (bflo(u) + bfhi(u)));
        };
        st128_sc1((u32x4){asu(sum10(t0, u0)), ta, asu(sum10(t1, u1)), tb}, l == 0 ? WS_RS + pc * 16 : OOB, rws);
      }
      float lbr, lbi, kr, ki;
      {
        const float Lr = bflo(plr), Li = bflo(pli);
        const float dt = bflo(pk2(expf(bflo(pdt)), 0.f));
        const float ea = expf(dt * Lr);
        float sn, cn; sincosf(dt * Li, &sn, &cn);
        lbr = ea * cn; lbi = ea * sn;
        const float inv = 2.0f / (Lr * Lr + Li * Li);
        kr = ((lbr - 1.0f) * Lr + lbi * Li) * inv;
        ki = (lbi * Lr - (lbr - 1.0f) * Li) * inv;
        asm volatile("" : "+v"(lbr), "+v"(lbi), "+v"(kr), "+v"(ki));
      }
      f32x4 are[8], aim[8];
      u32 wdum = 0;
      if (WARM && w >= 4) touch(wdum, l < 40 ? ((m * 5 + a) * 80 + (w & 1) * 40 + l) * 128 : OOB, mk_rsrc(w >= 6 ? c_im : c_re, 640 * 1280));
#pragma unroll
      for (int i = 0; i < 8; ++i) { are[i] = zero4; aim[i] = zero4; }
      {
        struct FW { bf16x8 br, bi; };
        struct FX { bf16x8 x[4]; };
        FW W0, W1; FX Xa, Xb;
        auto half = [&](auto mf, auto rw, auto hh, FW& RW, LAS char* pw, FX& RX, LAS char* px, const FW& MW, const FX& MX) AINL {
          constexpr int h = hh.v;
          if constexpr (rw.v) { RW.br = lds128(pw); RX.x[0] = lds128(px); } else { RX.x[0] = lds128(px); RX.x[1] = lds128(px + 2048); }
          SB();
          if constexpr (mf.v) are[4 * h] = mfma(MX.x[0], MW.br, are[4 * h]);
          SB();
          if constexpr (rw.v) { RW.bi = lds128(pw + 16384); RX.x[1] = lds128(px + 2048); } else { RX.x[2] = lds128(px + 4096); RX.x[3] = lds128(px + 6144); }
          SB();
          if constexpr (mf.v) aim[4 * h] = mfma(MX.x[0], MW.bi, aim[4 * h]);
          SB();
          if constexpr (rw.v) { RX.x[2] = lds128(px + 4096); RX.x[3] = lds128(px + 6144); }
          SB();
          if constexpr (mf.v) {
#pragma unroll
            for (int i = 1; i < 4; ++i) { are[4 * h + i] = mfma(MX.x[i], MW.br, are[4 * h + i]); aim[4 * h + i] = mfma(MX.x[i], MW.bi, aim[4 * h + i]); }
          }
          SB();
        };
        STAMP(1);
        sfor<0, 10>([&](auto ic) AINL {
          constexpr int s = ic.v;
          LAS char* const x0 = pin(sm + rb0 + (s % 3) * 49152);
          LAS char* const x1 = pin(sm + (rb0 ^ 64) + (s % 3) * 49152);
          LAS char* const w0 = pin(x0 + w * 2048 + 16384);
          LAS char* const w1_ = pin(x1 + w * 2048 + 16384);
          bar();
          if constexpr (s == 0) STAMP(16);
          half(IC<(s > 0)>{}, IC<1>{}, IC<1>{}, W0, w0, Xa, x0, W1, Xb);
          half(IC<1>{}, IC<0>{}, IC<0>{}, W0, w0, Xb, x0 + 8192, W0, Xa);
          half(IC<1>{}, IC<1>{}, IC<1>{}, W1, w1_, Xa, x1, W0, Xb);
          half(IC<1>{}, IC<0>{}, IC<0>{}, W1, w1_, Xb, x1 + 8192, W1, Xa);
        });
#pragma unroll
        for (int i = 0; i < 4; ++i) { are[4 + i] = mfma(Xb.x[i], W1.br, are[4 + i]); aim[4 + i] = mfma(Xb.x[i], W1.bi, aim[4 + i]); }
      }
      bar();
      STAMP(2);
      if (w < 2) {
        u32x4 g0 = *(LAS u32x4*)(sm + MISC + tid * 16);
        if (!__all(g0.y == ta && g0.w == tb)) g0 = poll_granule(WS_XS + (m * 128 + tid) * 16, true, rws, ta, tb);
        const float rs = asf(g0.z);
        *(LAS float*)(sm + CST + (tid >> 5) * 144 + (tid & 31) * 4) = rs;
        *(LAS float*)(sm + CST + 576 + (tid >> 5) * 144 + (tid & 31) * 4) = -rs * asf(g0.x);
      }
      u32x4 rg = *(LAS u32x4*)(sm + MISC + 2048 + (w * 16 + li) * 16);
      if (!__all(rg.y == ta && rg.w == tb)) rg = poll_granule(WS_RS + p * 16, true, rws, ta, tb);
      const float sre = asf(rg.x), sim = asf(rg.z);
      asm volatile("s_waitcnt vmcnt(0) lgkmcnt(0)\n\ts_barrier" : "+v"(wdum)::"memory");
      STAMP(21);
      LAS char* const cst = pin(sm + CST + g * 144);
#pragma unroll
      for (int mt = 0; mt < 8; ++mt) {
        const f32x4 rv = *(LAS f32x4*)(cst + mt * 16), nv = *(LAS f32x4*)(cst + 576 + mt * 16);
        are[mt] = rv * are[mt] + nv * sre;
        aim[mt] = rv * aim[mt] + nv * sim;
      }
      float hr = 0.f, hi = 0.f;
#pragma unroll
      for (int mt = 0; mt < 8; ++mt)
#pragma unroll
        for (int e = 0; e < 4; ++e) {
          const float nr = __builtin_fmaf(-lbi, hi, __builtin_fmaf(lbr, hr, are[mt][e]));
          const float ni = __builtin_fmaf(lbi, hr, __builtin_fmaf(lbr, hi, aim[mt][e]));
          hr = nr; hi = ni;
        }
      SB(); STAMP(23); SB();
      float er[4], ei[4];
      gather4(hr, er); gather4(hi, ei);
      float p32r = lbr, p32i = lbi;
#pragma unroll
      for (int i = 0; i < 5; ++i) { const float t = p32r * p32r - p32i * p32i; p32i = 2.0f * p32r * p32i; p32r = t; }
      float cr[5], ci[5];
      cr[0] = 0.f; ci[0] = 0.f;
#pragma unroll
      for (int q = 0; q < 4; ++q) {
        cr[q + 1] = __builtin_fmaf(-p32i, ci[q], __builtin_fmaf(p32r, cr[q], er[q]));
        ci[q + 1] = __builtin_fmaf(p32i, cr[q], __builtin_fmaf(p32r, ci[q], ei[q]));
      }
      st128_sc1((u32x4){asu(cr[4]), ta, asu(ci[4]), tb}, l < 16 ? WS_EG + (m * 640 + p) * 16 : OOB, rws);
      SB();
      STAMP(3);
      const float p64r = p32r * p32r - p32i * p32i, p64i = 2.0f * p32r * p32i;
      const float Pr = p64r * p64r - p64i * p64i, Pi = 2.0f * p64r * p64i;
#pragma unroll
      for (int mt = 0; mt < 8; ++mt) {
#pragma unroll
        for (int e = 0; e < 4; ++e) {
          const float ur = are[mt][e], ui = aim[mt][e];
          are[mt][e] = __builtin_fmaf(kr, ur, -ki * ui);
          aim[mt][e] = __builtin_fmaf(-kr, ui, -ki * ur);
        }
        are[mt] = keep(are[mt]); aim[mt] = keep(aim[mt]);
      }
      SB(); STAMP(24); SB();
      float Hr = 0.f, Hi = 0.f;
      if (m > 0) {
        u32x4 e0, e1, e2, e3;
        const int m0 = m - 1 - 4 * g;
        const u32 o0 = WS_EG + (m0 * 640 + p) * 16;
        const bool v0 = m0 >= 0, v1 = m0 >= 1, v2 = m0 >= 2, v3 = m0 >= 3;
        _Pragma("nounroll") for (int it = 0; it < SPIN_CAP; ++it) {
          asm volatile("buffer_load_dwordx4 %0, %4, %8, 0 offen sc1\n\tbuffer_load_dwordx4 %1, %5, %8, 0 offen sc1\n\t"
                       "buffer_load_dwordx4 %2, %6, %8, 0 offen sc1\n\tbuffer_load_dwordx4 %3, %7, %8, 0 offen sc1\n\ts_waitcnt vmcnt(0)"
                       : "=&v"(e0), "=&v"(e1), "=&v"(e2), "=&v"(e3)
                       : "v"(v0 ? o0 : OOB), "v"(v1 ? o0 - 10240 : OOB), "v"(v2 ? o0 - 20480 : OOB), "v"(v3 ? o0 - 30720 : OOB), "s"(rws) : "memory");
          const bool ok = (!v0 || (e0.y == ta && e0.w == tb)) && (!v1 || (e1.y == ta && e1.w == tb)) &&
                          (!v2 || (e2.y == ta && e2.w == tb)) && (!v3 || (e3.y == ta && e3.w == tb));
          if (__all(ok)) break;
          __builtin_amdgcn_s_sleep(1);
        }
        float qr = asf(e3.x), qi = asf(e3.z), t;
        t = __builtin_fmaf(-Pi, qi, __builtin_fmaf(Pr, qr, asf(e2.x))); qi = __builtin_fmaf(Pi, qr, __builtin_fmaf(Pr, qi, asf(e2.z))); qr = t;
        t = __builtin_fmaf(-Pi, qi, __builtin_fmaf(Pr, qr, asf(e1.x))); qi = __builtin_fmaf(Pi, qr, __builtin_fmaf(Pr, qi, asf(e1.z))); qr = t;
        t = __builtin_fmaf(-Pi, qi, __builtin_fmaf(Pr, qr, asf(e0.x))); qi = __builtin_fmaf(Pi, qr, __builtin_fmaf(Pr, qi, asf(e0.z))); qr = t;
        float gr[4], gi[4];
        gather4(qr, gr); gather4(qi, gi);
        const float P2r = Pr * Pr - Pi * Pi, P2i = 2.0f * Pr * Pi;
        const float P4r = P2r * P2r - P2i * P2i, P4i = 2.0f * P2r * P2i;
        Hr = gr[3]; Hi = gi[3];
#pragma unroll
        for (int q = 2; q >= 0; --q) {
          t = __builtin_fmaf(-P4i, Hi, __builtin_fmaf(P4r, Hr, gr[q])); Hi = __builtin_fmaf(P4i, Hr, __builtin_fmaf(P4r, Hi, gi[q])); Hr = t;
        }
      }
      STAMP(4);
      {
        const float p96r = p64r * p32r - p64i * p32i, p96i = p64r * p32i + p64i * p32r;
        const float fr = g == 0 ? 1.f : g == 1 ? p32r : g == 2 ? p64r : p96r;
        const float fi = g == 0 ? 0.f : g == 1 ? p32i : g == 2 ? p64i : p96i;
        const float c0r = g == 0 ? cr[0] : g == 1 ? cr[1] : g == 2 ? cr[2] : cr[3];
        const float c0i = g == 0 ? ci[0] : g == 1 ? ci[1] : g == 2 ? ci[2] : ci[3];
        const float sr = __builtin_fmaf(-fi, Hi, __builtin_fmaf(fr, Hr, c0r)), si = __builtin_fmaf(fi, Hr, __builtin_fmaf(fr, Hi, c0i));
        hr = __builtin_fmaf(kr, sr, -ki * si);
        hi = __builtin_fmaf(-kr, si, -ki * sr);
      }
      {
        LAS char* const tb_ = sm + (w < 6 ? w * 8192 : AS3 + (w - 6) * 8192);
        u32x4 hv[2][2][2];
        const int swc = ((li & 3) << 1) | ((li >> 2) & 1) | ((li >> 3) << 3);
        const int q4 = li >> 2, p4 = l & 3;
        const int c0 = 8 * (g & 1) + q4, c1 = c0 + 4;
        const int sw0 = ((c0 & 3) << 1) | ((c0 >> 3) << 3), sw1 = sw0 | 1;
        const u32 ob = WS_HH + (m * 20 + 2 * a + (w >> 2)) * 16384 + ((w >> 1) & 1) * 1024 + ((2 * (w & 1) + (g & 1)) * 16 + li) * 16;
#pragma unroll
        for (int hf = 0; hf < 2; ++hf) {
#pragma unroll
          for (int mt = 4 * hf; mt < 4 * hf + 4; ++mt)
#pragma unroll
            for (int e = 0; e < 4; ++e) {
              const float nr = __builtin_fmaf(lbi, hi, __builtin_fmaf(lbr, hr, are[mt][e]));
              const float ni = __builtin_fmaf(-lbi, hr, __builtin_fmaf(lbr, hi, aim[mt][e]));
              hr = nr; hi = ni;
              are[mt][e] = hr; aim[mt][e] = hi;
            }
#pragma unroll
          for (int i = 2 * hf; i < 2 * hf + 2; ++i) {
            *(LAS u32x4*)(tb_ + li * 256 + (((4 * g + i) ^ swc) << 4)) = pack8(are[2 * i], are[2 * i + 1]);
            *(LAS u32x4*)(tb_ + 4096 + li * 256 + (((4 * g + i) ^ swc) << 4)) = pack8(aim[2 * i], aim[2 * i + 1]);
          }
          asm volatile("s_waitcnt lgkmcnt(0)" ::: "memory");
#pragma unroll
          for (int part = 0; part < 2; ++part)
#pragma unroll
            for (int u = 0; u < 2; ++u) {
              const int tt = 4 * u + 2 * (g >> 1) + hf, ch = 2 * tt + (p4 >> 1);
              const bf16x8 v = ldstr(tb_ + part * 4096 + c0 * 256 + ((ch ^ sw0) << 4) + 8 * (p4 & 1),
                                     tb_ + part * 4096 + c1 * 256 + ((ch ^ sw1) << 4) + 8 * (p4 & 1));
              hv[hf][part][u] = __builtin_bit_cast(u32x4, v);
              st128_sc1(hv[hf][part][u], ob + part * 163840 + tt * 2048, rws);
            }
          SB();
          STAMP(25 + hf);
        }
        bar();
        LAS char* const hd = sm + (w >> 2) * 16384 + (g >> 1) * 4096 + ((w >> 1) & 1) * 1024 + ((2 * (w & 1) + (g & 1)) * 16 + li) * 16;
        LAS char* const hd1 = hd + ((w >> 2) ? AS3 - 16384 : 32768);
#pragma unroll
        for (int hf = 0; hf < 2; ++hf)
#pragma unroll
          for (int u = 0; u < 2; ++u) {
            *(LAS u32x4*)(hd + u * 8192 + hf * 2048) = hv[hf][0][u];
            *(LAS u32x4*)(hd1 + u * 8192 + hf * 2048) = hv[hf][1][u];
          }
      }
      waitbar<0>();
      STAMP(5);
      f32x4 ac[4][2];
#pragma unroll
      for (int i = 0; i < 4; ++i) { ac[i][0] = zero4; ac[i][1] = zero4; }
      LAS char* const ab = pin(sm + tq * 4096 + l * 16);
      LAS char* const ab3 = pin(ab + AS3);
      LAS char* cb[2][2];
#pragma unroll
      for (int i = 0; i < 2; ++i) { cb[0][i] = pin(sm + 49152 + i * 49152 + cq * 8192 + rb0); cb[1][i] = pin(sm + 49152 + i * 49152 + cq * 8192 + (rb0 ^ 64)); }
      struct FC { bf16x8 c[4], b0, b1; };
      FC G0, G1;
      auto stepC = [&](auto mf, FC& R, int ks, int s, const FC& M) AINL {
        LAS char* const pa = ((s & 3) == 3 ? ab3 : ab + (s & 3) * 16384) + ks * 1024;
        LAS char* const pc = cb[ks][(s % 5) / 3] + ((s % 5) % 3) * 16384;
        R.b0 = lds128(pa); R.c[0] = lds128(pc); SB();
        if constexpr (mf.v) ac[0][0] = mfma(M.c[0], M.b0, ac[0][0]);
        SB(); R.b1 = lds128(pa + 2048); R.c[1] = lds128(pc + 2048); SB();
        if constexpr (mf.v) ac[0][1] = mfma(M.c[0], M.b1, ac[0][1]);
        SB(); R.c[2] = lds128(pc + 4096); R.c[3] = lds128(pc + 6144); SB();
        if constexpr (mf.v) {
#pragma unroll
          for (int ct = 1; ct < 4; ++ct) { ac[ct][0] = mfma(M.c[ct], M.b0, ac[ct][0]); ac[ct][1] = mfma(M.c[ct], M.b1, ac[ct][1]); }
        }
        SB();
      };
      sfor<0, 20>([&](auto ic) AINL {
        constexpr int s = ic.v;
        if constexpr (s == 4) { bar(); STAMP(6); }
        if constexpr (s >= 4) bar();
        if constexpr (s == 4) STAMP(17);
        stepC(IC<(s > 0)>{}, G0, 0, s, G1);
        stepC(IC<1>{}, G1, 1, s, G0);
      });
#pragma unroll
      for (int ct = 0; ct < 4; ++ct) { ac[ct][0] = mfma(G1.c[ct], G1.b0, ac[ct][0]); ac[ct][1] = mfma(G1.c[ct], G1.b1, ac[ct][1]); }
      STAMP(7);
#pragma unroll
      for (int cb_ = 0; cb_ < 2; ++cb_)
#pragma unroll
        for (int tt = 0; tt < 2; ++tt)
          st128_sc1(pack8(ac[2 * cb_][tt], ac[2 * cb_ + 1][tt]), WS_SS + ((m * 10 + 2 * a + cq) * 8 + tq + 4 * cb_) * 2048 + tt * 1024 + l * 16, rws);
      waitbar<0>();
      if (w == 0) signal(l < 2 ? WS_IB + (m * 10 + 2 * a + l) * IB_B + 512 : OOB);
      STAMP(8);
    }
  } else if (w < 8) {
    row_stats();
    auto add8 = [&](float (&c)[8], u32x4 t) AINL {
      c[0] += bflo(t.x); c[1] += bfhi(t.x); c[2] += bflo(t.y); c[3] += bfhi(t.y); c[4] += bflo(t.z); c[5] += bfhi(t.z); c[6] += bflo(t.w); c[7] += bfhi(t.w);
    };
    STAMP(1);
    if (m < 12) {
      const int c = m;
      const rsrc_t rw3 = mk_rsrc(w3, 640 * 3072);
      float cs[8] = {0.f, 0.f, 0.f, 0.f, 0.f, 0.f, 0.f, 0.f};
      const u32 vo = (tid >> 4) * 3072 + c * 256 + (tid & 15) * 16;
      u32x4 t[20];
#pragma unroll
      for (int i = 0; i < 20; ++i) t[i] = bld128(vo, rw3, i * 32 * 3072);
      SB();
#pragma unroll
      for (int i = 0; i < 20; ++i) add8(cs, t[i]);
#pragma unroll
      for (int e = 0; e < 8; ++e) cs[e] = sum32(sum16(cs[e]));
      if (l < 16) {
        *(LAS f32x4*)(sm + w * 512 + l * 32) = (f32x4){cs[0], cs[1], cs[2], cs[3]};
        *(LAS f32x4*)(sm + w * 512 + l * 32 + 16) = (f32x4){cs[4], cs[5], cs[6], cs[7]};
      }
      bar();
      if (w < 2) {
        float s = 0.f;
#pragma unroll
        for (int i = 0; i < 8; ++i) s += *(LAS float*)(sm + i * 512 + tid * 4);
        st32_sc1(asu(s), WS_CS3 + (c * 128 + tid) * 4, rws);
      }
      waitbar<0>();
      if (w == 0) signal(l < 16 ? WS_CF + c * 128 + l * 8 : OOB);
    }
    {
      const rsrc_t rw4 = mk_rsrc(w4, 1536 * 1280);
      u32 d = 0;
      if (m < 12) { touch(d, (m * 640 + tid) * 128, rw4); touch(d, tid < 128 ? (m * 640 + 512 + tid) * 128 : OOB, rw4); }
      else {
#pragma unroll
        for (int i = 0; i < 4; ++i) touch(d, i < 3 || tid < 384 ? (7680 + (m - 12) * 1920 + i * 512 + tid) * 128 : OOB, rw4);
      }
      asm volatile("s_waitcnt vmcnt(0)" : "+v"(d)::"memory");
    }
    STAMP(2);
    if (m == 0) {
#pragma unroll
      for (int i = 0; i < 4; ++i) (void)poll_granule(WS_XS + (i * 512 + tid) * 16, true, rws, ta, tb);
      bar();
      if (tid == 0) st64_sc1((u32x2){ta, tb}, WS_EPOCH, rws);
    }
  }
#endif
}

extern "C" void kernel_launch(void* const* d_in, const int* in_sizes, int n_in, void* d_out, int out_size,
                              void* d_ws, size_t ws_size, hipStream_t stream) {
  (void)in_sizes; (void)n_in; (void)out_size; (void)ws_size;
  s5_mixer_kernel<<<256, 768, 0, stream>>>(d_in[0], d_in[1], d_in[2], d_in[3], d_in[4], d_in[5], d_in[6], d_in[7],
                                           d_in[8], d_in[10], d_in[11], d_in[12], d_in[13], d_out, d_ws);
}
```

```cpp
#include <hip/hip_runtime.h>
#include <hip/hip_bf16.h>
#include <stdint.h>

#define DI __device__ __forceinline__
#define LAS __attribute__((address_space(3)))
#define AINL __attribute__((always_inline))
typedef unsigned u32;
typedef unsigned long long u64;
typedef u32 u32x4 __attribute__((ext_vector_type(4)));
typedef u32 u32x2 __attribute__((ext_vector_type(2)));
typedef float f32x4 __attribute__((ext_vector_type(4)));
typedef float f32x2 __attribute__((ext_vector_type(2)));
typedef __bf16 bf16x8 __attribute__((ext_vector_type(8)));
typedef __bf16 bf16x2 __attribute__((ext_vector_type(2)));
typedef short s16x4 __attribute__((ext_vector_type(4)));
typedef short s16x8 __attribute__((ext_vector_type(8)));

#define XMAP 0
#define ABL 0
#define PSLEEP 2
#define WARM 1
#define IBW 1
#define RSS 2
#define SDELAY 32
#define ROTM 0
#define STAMPS 0

constexpr u32 WS_EPOCH = 0;
constexpr u32 WS_CF = 0x1000;
constexpr u32 WS_IB = 0x340000;
constexpr u32 IB_B = 640, IB_A = 160 * 640;
constexpr u32 WS_XS = 0x20000;
constexpr u32 WS_CS3 = 0x2A000;
constexpr u32 WS_RS = 0x58000;
constexpr u32 WS_EG = 0x30000;
constexpr u32 WS_ST1 = 0x60000;
constexpr u32 WS_SS = 0xC0000;
constexpr u32 WS_H1 = 0x400000;
constexpr u32 WS_X1 = 0x680000;
constexpr u32 WS_HH = 0x900000;
constexpr u32 WS_H2 = 0xE00000;
constexpr u32 WS_STAMP = 0x1400000;
constexpr u32 WS_BYTES = 0x1500000;
constexpr u32 OOB = 0x7FFFFFF0u;
constexpr float LN_EPS = 1e-5f;
constexpr int SPIN_CAP = 1 << 17;

template <int I> struct IC { static constexpr int v = I; };
template <int B, int E, class F> DI void sfor(F&& f) {
  if constexpr (B < E) { f(IC<B>{}); sfor<B + 1, E>(f); }
}

#if defined(__HIP_DEVICE_COMPILE__) && __HIP_DEVICE_COMPILE__
typedef __amdgpu_buffer_rsrc_t rsrc_t;
DI rsrc_t mk_rsrc(const void* p, u32 bytes) { return __builtin_amdgcn_make_buffer_rsrc((void*)p, 0, bytes, 0x00020000); }

template <int SC1> DI void dma1(u32 lds, u32 voff, rsrc_t r, u32 soff) {
  if (ABL == 2) return;
  if constexpr (SC1)
    asm volatile("s_mov_b32 m0, %0\n\ts_nop 0\n\tbuffer_load_dwordx4 %1, %2, %3 offen sc1 lds" ::"s"(lds), "v"(voff), "s"(r), "s"(soff) : "memory");
  else
    asm volatile("s_mov_b32 m0, %0\n\ts_nop 0\n\tbuffer_load_dwordx4 %1, %2, %3 offen lds" ::"s"(lds), "v"(voff), "s"(r), "s"(soff) : "memory");
}
template <int SC1> DI void dma2(u32 lds, u32 v0, u32 v1, rsrc_t r, u32 soff) {
  if (ABL == 2) return;
  if constexpr (SC1)
    asm volatile("s_mov_b32 m0, %0\n\ts_nop 0\n\tbuffer_load_dwordx4 %1, %3, %4 offen sc1 lds\n\tbuffer_load_dwordx4 %2, %3, %4 offen offset:1024 sc1 lds"
                 ::"s"(lds), "v"(v0), "v"(v1), "s"(r), "s"(soff) : "memory");
  else
    asm volatile("s_mov_b32 m0, %0\n\ts_nop 0\n\tbuffer_load_dwordx4 %1, %3, %4 offen lds\n\tbuffer_load_dwordx4 %2, %3, %4 offen offset:1024 lds"
                 ::"s"(lds), "v"(v0), "v"(v1), "s"(r), "s"(soff) : "memory");
}
template <int SC1> DI void dma4(u32 lds, u32 v, rsrc_t r, u32 soff) {
  if (ABL == 2) return;
  if constexpr (SC1)
    asm volatile("s_mov_b32 m0, %0\n\ts_nop 0\n\tbuffer_load_dwordx4 %1, %2, %3 offen sc1 lds\n\tbuffer_load_dwordx4 %1, %2, %3 offen offset:1024 sc1 lds\n\t"
                 "buffer_load_dwordx4 %1, %2, %3 offen offset:2048 sc1 lds\n\tbuffer_load_dwordx4 %1, %2, %3 offen offset:3072 sc1 lds"
                 ::"s"(lds), "v"(v), "s"(r), "s"(soff) : "memory");
  else
    asm volatile("s_mov_b32 m0, %0\n\ts_nop 0\n\tbuffer_load_dwordx4 %1, %2, %3 offen lds\n\tbuffer_load_dwordx4 %1, %2, %3 offen offset:1024 lds\n\t"
                 "buffer_load_dwordx4 %1, %2, %3 offen offset:2048 lds\n\tbuffer_load_dwordx4 %1, %2, %3 offen offset:3072 lds"
                 ::"s"(lds), "v"(v), "s"(r), "s"(soff) : "memory");
}
constexpr int VC(int n) { return ABL == 2 ? 0 : n; }
template <int N> DI void waitv() { asm volatile("s_waitcnt vmcnt(%0)" ::"i"(VC(N)) : "memory"); }
DI void bar() { asm volatile("s_waitcnt lgkmcnt(0)\n\ts_barrier" ::: "memory"); }
template <int N> DI void waitbar() { asm volatile("s_waitcnt vmcnt(%0) lgkmcnt(0)\n\ts_barrier" ::"i"(VC(N)) : "memory"); }

DI void touch(u32& d, u32 voff, rsrc_t r) { asm volatile("buffer_load_dword %0, %1, %2, 0 offen" : "+v"(d) : "v"(voff), "s"(r) : "memory"); }

DI u32x4 bld128(u32 voff, rsrc_t r, u32 soff) { return __builtin_amdgcn_raw_buffer_load_b128(r, voff, soff, 0); }
DI u32x2 bld64(u32 voff, rsrc_t r, u32 soff) { return __builtin_amdgcn_raw_buffer_load_b64(r, voff, soff, 0); }
DI u32x2 bld64_sc1(u32 voff, rsrc_t r, u32 soff) { return __builtin_amdgcn_raw_buffer_load_b64(r, voff, soff, 16); }
DI u32 bld32(u32 voff, rsrc_t r, u32 soff) { return __builtin_amdgcn_raw_buffer_load_b32(r, voff, soff, 0); }
DI u32 bld16(u32 voff, rsrc_t r) { return __builtin_amdgcn_raw_buffer_load_b16(r, voff, 0, 0); }
DI void st128_sc1(u32x4 v, u32 voff, rsrc_t r) { __builtin_amdgcn_raw_buffer_store_b128(v, r, voff, 0, 16); }
DI void st64_sc1(u32x2 v, u32 voff, rsrc_t r) { __builtin_amdgcn_raw_buffer_store_b64(v, r, voff, 0, 16); }
DI void st32_sc1(u32 v, u32 voff, rsrc_t r) { __builtin_amdgcn_raw_buffer_store_b32(v, r, voff, 0, 16); }

DI void poll_flags(u32 voff, bool active, rsrc_t ws, u32 ta, u32 tb) {
  _Pragma("nounroll") for (int it = 0; it < SPIN_CAP; ++it) {
    u32x2 v;
    asm volatile("buffer_load_dwordx2 %0, %1, %2, 0 offen sc1\n\ts_waitcnt vmcnt(0)" : "=&v"(v) : "v"(voff), "s"(ws) : "memory");
    const bool ok = !active || (v.x == ta && v.y == tb);
    if (__all(ok)) break;
    __builtin_amdgcn_s_sleep(1);
  }
}
DI u32x4 poll_granule(u32 voff, bool active, rsrc_t ws, u32 ta, u32 tb) {
  u32x4 v;
  _Pragma("nounroll") for (int it = 0; it < SPIN_CAP; ++it) {
    asm volatile("buffer_load_dwordx4 %0, %1, %2, 0 offen sc1\n\ts_waitcnt vmcnt(0)" : "=&v"(v) : "v"(voff), "s"(ws) : "memory");
    const bool ok = !active || (v.y == ta && v.w == tb);
    if (__all(ok)) break;
    __builtin_amdgcn_s_sleep(1);
  }
  return v;
}

DI float asf(u32 x) { return __uint_as_float(x); }
DI u32 asu(float x) { return __float_as_uint(x); }
DI float bflo(u32 x) { return asf(x << 16); }
DI float bfhi(u32 x) { return asf(x & 0xffff0000u); }
DI u32 pk2(float a, float b) { f32x2 p = {a, b}; bf16x2 q = __builtin_convertvector(p, bf16x2); return __builtin_bit_cast(u32, q); }
DI float sum16(float x) { const u32 u = asu(x); auto r = __builtin_amdgcn_permlane16_swap(u, u, false, false); const u32 a = r[0], b = r[1]; return asf(a) + asf(b); }
DI float sum32(float x) { const u32 u = asu(x); auto r = __builtin_amdgcn_permlane32_swap(u, u, false, false); const u32 a = r[0], b = r[1]; return asf(a) + asf(b); }
template <int C> DI float dppadd(float x) { return x + asf(__builtin_amdgcn_update_dpp(0u, asu(x), C, 0xf, 0xf, true)); }
DI float wavesum(float x) { x = dppadd<0xB1>(x); x = dppadd<0x4E>(x); x = dppadd<0x141>(x); x = dppadd<0x140>(x); return sum32(sum16(x)); }
DI void gather4(float x, float (&e)[4]) {
  const u32 u = asu(x);
  auto p = __builtin_amdgcn_permlane16_swap(u, u, false, false);
  const u32 a = p[0], b = p[1];
  auto pa = __builtin_amdgcn_permlane32_swap(a, a, false, false);
  auto pb = __builtin_amdgcn_permlane32_swap(b, b, false, false);
  const u32 e0 = pa[0], e2 = pa[1], e1 = pb[0], e3 = pb[1];
  e[0] = asf(e0); e[1] = asf(e1); e[2] = asf(e2); e[3] = asf(e3);
}
typedef f32x4 GeluK;
DI GeluK gelu_k(float r, float ri) { return (f32x4){-2.302208198f * r, -0.1029432397f * r * r * r, ri, 0.f}; }
DI u32 gelu2(f32x2 t, GeluK k) {
  const f32x2 u = t * __builtin_elementwise_fma(t * t, (f32x2){k.y, k.y}, (f32x2){k.x, k.x});
  const f32x2 e = {__builtin_amdgcn_exp2f(u.x), __builtin_amdgcn_exp2f(u.y)};
  const f32x2 d = __builtin_elementwise_fma(e, (f32x2){k.z, k.z}, (f32x2){k.z, k.z});
  const f32x2 o = t * (f32x2){__builtin_amdgcn_rcpf(d.x), __builtin_amdgcn_rcpf(d.y)};
  return __builtin_bit_cast(u32, __builtin_convertvector(o, bf16x2));
}
DI u32x2 gelu4(f32x4 acc, f32x4 cs, float mu, GeluK k) {
  const f32x2 nm = {-mu, -mu};
  return (u32x2){gelu2(__builtin_elementwise_fma(nm, cs.xy, acc.xy), k), gelu2(__builtin_elementwise_fma(nm, cs.zw, acc.zw), k)};
}
DI float silu_f(float z) { return z * __builtin_amdgcn_rcpf(1.0f + __builtin_amdgcn_exp2f(z * -1.4426950409f)); }
DI f32x4 mfma(bf16x8 a, bf16x8 b, f32x4 c) { if (ABL == 1 || ABL == 4) { asm volatile("" : "+v"(c) : "v"(a), "v"(b)); return c; } return __builtin_amdgcn_mfma_f32_16x16x32_bf16(a, b, c, 0, 0, 0); }
DI bf16x8 lds128(LAS char* p) { if (ABL == 1 || ABL == 3) { bf16x8 r; asm volatile("" : "=v"(r)); return r; } return *(LAS bf16x8*)p; }
DI bf16x8 ldstr(LAS char* p0, LAS char* p1) {
  if (ABL == 1 || ABL == 3) { bf16x8 r; asm volatile("" : "=v"(r)); return r; }
  const s16x4 a = __builtin_amdgcn_ds_read_tr16_b64_v4i16((LAS s16x4*)p0);
  const s16x4 b = __builtin_amdgcn_ds_read_tr16_b64_v4i16((LAS s16x4*)p1);
  const s16x8 w = __builtin_shufflevector(a, b, 0, 1, 2, 3, 4, 5, 6, 7);
  return __builtin_bit_cast(bf16x8, w);
}
DI u32x4 pack8(f32x4 a, f32x4 b) { u32x4 r; r.x = pk2(a.x, a.y); r.y = pk2(a.z, a.w); r.z = pk2(b.x, b.y); r.w = pk2(b.z, b.w); return r; }
DI u32x2 pack4(f32x4 a) { u32x2 r; r.x = pk2(a.x, a.y); r.y = pk2(a.z, a.w); return r; }
DI int ssel_lt(int a, int b, int x, int y) { int r; asm("s_cmp_lt_i32 %1, %2\n\ts_cselect_b32 %0, %3, %4" : "=s"(r) : "s"(a), "s"(b), "s"(x), "s"(y) : "scc"); return r; }
DI int wrapk(int k, int n) { return k >= n ? k - n : k; }
#define SB() __builtin_amdgcn_sched_barrier(0)
DI f32x4 keep(f32x4 v) { asm volatile("" : "+v"(v)); return v; }
DI LAS char* pin(LAS char* p) { asm volatile("" : "+v"(p)); return p; }
#endif

#define STAMP(i) do { } while (0)
#define CSTAMP(i) do { } while (0)

__global__ __launch_bounds__(768, 3) void s5_mixer_kernel(
    const void* __restrict__ x, const void* __restrict__ lam_re, const void* __restrict__ lam_im,
    const void* __restrict__ b_re, const void* __restrict__ b_im, const void* __restrict__ c_re,
    const void* __restrict__ c_im, const void* __restrict__ log_dt, const void* __restrict__ dskip,
    const void* __restrict__ w1, const void* __restrict__ w2, const void* __restrict__ w3,
    const void* __restrict__ w4, void* __restrict__ out, void* __restrict__ ws) {
#if defined(__HIP_DEVICE_COMPILE__) && __HIP_DEVICE_COMPILE__
  __shared__ __attribute__((aligned(16))) char smem_[163840];
  LAS char* const sm = (LAS char*)smem_;
  const u32 L0 = (u32)(size_t)sm;
  const int tid = threadIdx.x, l = tid & 63;
  const int w = __builtin_amdgcn_readfirstlane(tid >> 6);
  const int bid = blockIdx.x, xcd = bid & 7, rr = bid >> 3;
  const int m = xcd * 2 + (rr < 20 ? rr / 10 : (rr - 20) / 6), role = rr < 20 ? rr % 10 : 10 + (rr - 20) % 6;
  const int g = l >> 4, li = l & 15, r8 = l >> 3, tq = w & 3, cq = w >> 2, L = w - 8;
  const rsrc_t rws = mk_rsrc(ws, WS_BYTES);
  const rsrc_t rx = mk_rsrc(x, 2048 * 1280);
  STAMP(0); CSTAMP(30);
  const int fm = ((5 * (m & 3) + 1) >> 1) + (m >> 2);
  const int rot1 = (2 * (role % 5) + (role >= 5) + fm) % 10, rotA1 = (2 * (role % 5) + 1 + fm) % 10;

  u32 ta, tb;
  auto row_stats = [&]() AINL {
    const int stok = m * 128 + role * 8 + w;
    const u32x2 ep = bld64_sc1(0, rws, 0);
    const u32x4 ra = bld128(l * 16, rx, stok * 1280);
    const u32 rb_ = bld32(1024 + l * 4, rx, stok * 1280);
    const u64 e = (((u64)__builtin_amdgcn_readfirstlane(ep.y) << 32) | (u32)__builtin_amdgcn_readfirstlane(ep.x)) + 0x9E3779B97F4A7C15ull;
    ta = (u32)e; tb = (u32)(e >> 32);
    float v[10] = {bflo(ra.x), bfhi(ra.x), bflo(ra.y), bfhi(ra.y), bflo(ra.z), bfhi(ra.z), bflo(ra.w), bfhi(ra.w), bflo(rb_), bfhi(rb_)};
    float s = 0.f;
#pragma unroll
    for (int i = 0; i < 10; ++i) s += v[i];
    const float mu = wavesum(s) * (1.0f / 640.0f);
    float q = 0.f;
#pragma unroll
    for (int i = 0; i < 10; ++i) { const float d = v[i] - mu; q = __builtin_fmaf(d, d, q); }
    const float rs = 1.0f / sqrtf(wavesum(q) * (1.0f / 640.0f) + LN_EPS);
    u32x4 gr = {asu(mu), ta, asu(rs), tb};
    st128_sc1(gr, l == 0 ? WS_XS + stok * 16 : OOB, rws);
  };
  auto put_tag = [&](u32 off) AINL { if (tid == 0) *(LAS u32x2*)(sm + off) = (u32x2){ta, tb}; };
  auto get_tag = [&](u32 off) AINL {
    const u32x2 t = *(volatile LAS u32x2*)(sm + off);
    ta = __builtin_amdgcn_readfirstlane(t.x); tb = __builtin_amdgcn_readfirstlane(t.y);
  };
  auto signal = [&](u32 off) AINL { st64_sc1((u32x2){ta, tb}, off, rws); };
  auto await = [&](auto nv, u32 sig, u32 src, bool act, auto&& pre) AINL {
    waitbar<nv.v>();
    if (L == 0) { signal(sig); poll_flags(src, act, rws, ta, tb); }
    else pre();
    bar();
  };
  const u32 rb0 = li * 128 + ((g ^ (li >> 1)) << 4);
  const f32x4 zero4 = {0.f, 0.f, 0.f, 0.f};

  if (role < 10) {
    const int j = role;
    constexpr u32 ACT = 0, W = 65536;
    const int b0 = j < 9 ? 5 * j : 44, nb = j < 8 ? 5 : 4, kt0 = (b0 + 1) >> 1;
    if (w >= 8) {
      const rsrc_t rw1 = mk_rsrc(w1, 640 * 1280), rw2 = mk_rsrc(w2, 640 * 1280);
      const rsrc_t rw3 = mk_rsrc(w3, 640 * 3072), rw4 = mk_rsrc(w4, 1536 * 1280);
      const u32 vx0 = (m * 128 + L * 32 + r8) * 1280 + (((l & 7) ^ g) << 4);
      const u32 vx1 = (m * 128 + L * 32 + 8 + r8) * 1280 + (((l & 7) ^ (4 + g)) << 4) - 1024;
      const u32 wch = j * 128 + (((l & 7) ^ (2 * g)) << 4);
      const u32 vq0 = ((L >> 1) * 32 + (r8 >> 2) * 8 + (L & 1) * 4 + (r8 & 3)) * 1280 + wch, vq1 = vq0 + 16 * 1280 - 1024;
      const u32 ldsA = L0 + ACT + L * 4096, ldsW = L0 + W + L * 2048;
      auto issue_1 = [&](int s) AINL {
        const int kt = wrapk(s + rot1, 10);
        dma2<0>(ldsA + (s & 3) * 16384, vx0, vx1, rx, kt * 128);
        dma2<0>(ldsW + 65536 + (s & 3) * 8192, vq0, vq1, rw1, kt * 81920);
        dma2<0>(ldsA + (s & 3) * 16384 + 2048, vx0, vx1, rx, kt * 128 + 20480);
      };
      issue_1(0); issue_1(1); issue_1(2);
      const u32 vw0 = (L * 16 + r8) * 1280 + wch, vw1 = vw0 + 10240 - 1024;
      const u32 va = L * 4096 + l * 16, vl = L * 1024 + l * 16;
      auto k3 = [&](int s) AINL { return s == 0 ? j : ssel_lt(s - 1, j, s - 1, s); };
      auto k4 = [&](int s) AINL { return s < 2 ? kt0 + s : ssel_lt(s - 2, kt0, s - 2, s); };
      u32 v3[5];
#pragma unroll
      for (int i = 0; i < 5; ++i) {
        const int c = 64 * i + l, r = c / 20, pos = c - 20 * r;
        v3[i] = (L * 16 + r) * 3072 + b0 * 64 + ((pos ^ (2 * ((r >> 2) & 1))) << 4) - 1024 * (i & 1);
      }
      auto issue_w2 = [&](int s) AINL { dma2<0>(ldsW + 16384 + s * 8192, vw0, vw1, rw2, s * 81920); };
      auto issue_w3 = [&](int s) AINL {
        const u32 d = L0 + W + (s & 3) * 20480 + L * 5120, so = k3(s) * 196608;
        dma2<0>(d, v3[0], v3[1], rw3, so); dma2<0>(d + 2048, v3[2], v3[3], rw3, so); dma1<0>(d + 4096, v3[4], rw3, so);
      };
      const u32 vz0 = r8 * 1280 + wch, vz1 = vz0 + 10240 - 1024;
      auto whole_w = [&](u32 d, rsrc_t r, u32 so) AINL {
#pragma unroll
        for (int i = 0; i < 4; ++i) dma2<0>(d + i * 2048, vz0, vz1, r, so + i * 20480);
      };
      auto issue_w4 = [&](int s) AINL { dma2<0>(ldsW + ((s + 5) % 12) * 8192, vw0, vw1, rw4, k4(s) * 81920); };
      auto issue_a = [&](int s, u32 base) AINL { dma4<1>(ldsA + (s & 3) * 16384, va, rws, base); };
      auto issue_h1 = [&](int s) AINL {
#pragma unroll
        for (int ks = 0; ks < 2; ++ks) {
          const int b = 2 * s + ks, grp = b / 5;
          const int t = m * 128 + l - ((grp & 1) ? -(1 + (grp >> 1)) : (1 + (grp >> 1)));
          dma2<1>(L0 + ACT + (s & 3) * 16384 + ks * 8192 + L * 2048, t >= 0 ? t * 16 + 32 : OOB, t < 2048 - 64 ? t * 16 + 32 : OOB, rws, WS_H1 + (b * 4 + L) * 32768 - 32);
        }
      };
      const bf16x8 ones = __builtin_bit_cast(bf16x8, (u32x4){0x3F803F80u, 0x3F803F80u, 0x3F803F80u, 0x3F803F80u});
      LAS char* const wl = pin(sm + W + 65536 + (4 * g + (li >> 2)) * 128 + ((L ^ ((2 * g + (li >> 3)) & 3)) << 5) + 8 * (l & 3));
      f32x4 cs = zero4;
      sfor<0, 10>([&](auto ic) AINL {
        constexpr int s = ic.v;
        constexpr int gs[13] = {6, 6, 6, 6, 6, 6, 6, 6, 6, 6, 3, 2, 0};
        waitbar<(s == 9 ? 4 : gs[s + 1] + gs[s + 2])>();
        if constexpr (s + 3 < 10) issue_1(s + 3);
        if constexpr (s == 0) get_tag(W);
        const bf16x8 c0 = ldstr(wl + (s & 3) * 8192, wl + (s & 3) * 8192 + 2048), c1 = ldstr(wl + (s & 3) * 8192 + 4096, wl + (s & 3) * 8192 + 6144);
        cs = keep(mfma(c1, ones, mfma(c0, ones, cs)));
        if constexpr (s == 7) { dma1<1>(L0 + ACT + 2 * 16384 + L * 1024, vl, rws, WS_XS + m * 2048); issue_w2(0); }
        if constexpr (s == 8) issue_w2(1);
      });
      if (li == 0) *(LAS f32x4*)(sm + ACT + 2 * 16384 + 4096 + L * 64 + g * 16) = cs;
      bar();
      issue_w2(2); issue_w2(3); issue_w2(4); issue_w2(5); issue_w2(6);
      const u32 ib = WS_IB + (m * 10 + j) * IB_B, ibt = WS_IB + (m * 10 + l) * IB_B;
      const int j5 = j % 5;
      await(IC<63>{},
            l < 10 ? ibt + j * 8 : l < 20 ? (j5 < 3 && m < 15 ? ibt + (10 + j5 + 3 * (j / 5)) * 8 : OOB)
                   : (l < 30 && j5 >= 2 && m > 0 ? ibt - 30 * IB_B + (14 + j5 + 3 * (j / 5)) * 8 : OOB),
            ib + l * 8, l < 10 || (l < 16 && m > 0) || (l >= 16 && l < 22 && m < 15),
            [&]() AINL { whole_w(L0 + W + 16384 + (6 + L) * 8192, rw2, (6 + L) * 81920); });
      issue_h1(0); issue_h1(1); issue_h1(2);
      sfor<0, 10>([&](auto ic) AINL {
        constexpr int s = ic.v;
        constexpr int gs[13] = {4, 4, 4, 4, 4, 4, 4, 4, 4, 4, 5, 5, 5};
        waitbar<gs[s + 1] + gs[s + 2]>();
        if constexpr (s + 3 < 10) issue_h1(s + 3);
        else issue_w3(s - 7);
      });
      issue_w3(3);
      await(IC<15>{}, l < 10 ? ibt + 256 + j * 8 : OOB, l < 10 ? ib + 256 + l * 8 : WS_CF + ((b0 >> 2) + l - 10) * 128, l < (j < 9 ? 12 : 11), [&]() AINL {});
      sfor<1, 4>([&](auto ic) AINL { constexpr int s = ic.v; issue_a(s, WS_X1 + (m * 10 + k3(s)) * 16384); });
      sfor<1, 10>([&](auto ic) AINL {
        constexpr int s = ic.v;
        constexpr int gs[13] = {0, 4, 4, 4, 9, 9, 9, 9, 9, 9, 4, 2, 0};
        waitbar<gs[s + 1] + gs[s + 2]>();
        if constexpr (s + 3 < 10) {
          issue_a(s + 3, WS_X1 + (m * 10 + k3(s + 3)) * 16384);
          issue_w3(s + 3);
        } else if constexpr (s == 7) {
          issue_a(2, WS_ST1 + m * 20480);
        } else if constexpr (s == 8) {
          dma1<1>(L0 + ACT + 3 * 16384 + L * 1024, vl, rws, WS_ST1 + m * 20480 + 16384);
          dma1<1>(L0 + ACT + 3 * 16384 + 4096 + L * 1024, vl, rws, WS_CS3 + b0 * 128);
        }
      });
      waitbar<0>();
      sfor<0, 12>([&](auto ic) AINL { issue_w4(ic.v); });
      await(IC<20>{}, l < 10 ? ibt + 384 + j * 8 : OOB, ib + 384 + l * 8, l < 10, [&]() AINL {});
      sfor<2, 5>([&](auto ic) AINL { constexpr int s = ic.v; issue_a(s, WS_H2 + (m * 24 + k4(s)) * 16384); });
      u32x2 fs;
      const u32 fso = ib + 512;
      sfor<2, 24>([&](auto ic) AINL {
        constexpr int s = ic.v;
        constexpr int gs[27] = {0, 0, 4, 4, 4, 6, 6, 6, 6, 6, 6, 6, 6, 6, 6, 6, 6, 4, 4, 4, 5, 4, 4, 4, 4, 0, 0};
        if constexpr (s == 20) { asm volatile("s_waitcnt vmcnt(%1) lgkmcnt(0)\n\ts_barrier" : "+v"(fs) : "i"(VC(gs[s + 1] + gs[s + 2])) : "memory"); }
        else waitbar<gs[s + 1] + gs[s + 2]>();
        if constexpr (s == 17) asm volatile("buffer_load_dwordx2 %0, %1, %2, 0 offen sc1" : "=v"(fs) : "v"(fso), "s"(rws) : "memory");
        if constexpr (s + 3 < 24) issue_a(s + 3, WS_H2 + (m * 24 + k4(s + 3)) * 16384);
        if constexpr (s <= 13) issue_w4(s + 10);
        if constexpr (s == 21) {
          if (!__all(fs.x == ta && fs.y == tb)) poll_flags(fso, true, rws, ta, tb);
          issue_a(0, WS_SS + (m * 10 + j) * 16384);
        }
      });
      waitbar<0>();
    } else {
      row_stats();
      put_tag(W);
      const int q4 = li >> 2, p4 = l & 3;
      LAS char* const wb0 = pin(sm + W + (4 * g + q4) * 128 + ((((2 * cq) ^ ((2 * g + (q4 >> 1)) & 3))) << 5) + 8 * p4);
      LAS char* const wb1 = pin(sm + W + (4 * g + q4) * 128 + ((((2 * cq + 1) ^ ((2 * g + (q4 >> 1)) & 3))) << 5) + 8 * p4);
      LAS char* const wh0 = pin(wb0 + 65536);
      LAS char* const wh1 = pin(wb1 + 65536);
      LAS char* const ab = pin(sm + ACT + tq * 4096 + l * 16);
      struct F64 { bf16x8 a0, a1, b0, b1; };
      F64 F0, F1;
      f32x4 acc[2][2], cs[2];
      u32x2 xs[2][2];
      auto step64 = [&](auto mf, auto tts, F64& R, LAS char* pb, u32 wo, const F64& M) AINL {
        LAS char* const p0 = wo >= 65536 ? wh0 + (wo - 65536) : wb0 + wo;
        LAS char* const p1 = wo >= 65536 ? wh1 + (wo - 65536) : wb1 + wo;
        auto mm = [&](int i) AINL { if constexpr (mf.v) acc[i & 1][i >> 1] = mfma((i & 1) ? M.a1 : M.a0, (i >> 1) ? M.b1 : M.b0, acc[i & 1][i >> 1]); SB(); };
        mm(0); R.a0 = ldstr(p0, p0 + 2048); SB();
        mm(1); R.b0 = lds128(pb); R.b1 = lds128(pb + tts.v); SB();
        mm(2); R.a1 = ldstr(p1, p1 + 2048); SB();
        mm(3);
      };
      auto mm = [&](const F64& M) AINL {
        acc[0][0] = mfma(M.a0, M.b0, acc[0][0]); acc[1][0] = mfma(M.a1, M.b0, acc[1][0]);
        acc[0][1] = mfma(M.a0, M.b1, acc[0][1]); acc[1][1] = mfma(M.a1, M.b1, acc[1][1]);
      };
      float mu[2], rs[2];
      {
        LAS char* const xb0 = pin(sm + ACT + tq * 4096 + rb0);
        LAS char* const xb1 = pin(sm + ACT + tq * 4096 + (rb0 ^ 64));
#pragma unroll
        for (int a = 0; a < 2; ++a) { acc[a][0] = zero4; acc[a][1] = zero4; }
        const int s_own = wrapk(j - rot1 + 10, 10);
        LAS char* const xo0 = pin(sm + ACT + tq * 4096 + li * 128 + (((4 * cq + (g >> 1)) ^ (li >> 1)) << 4) + 8 * (g & 1));
        LAS char* const xo1 = pin(sm + ACT + tq * 4096 + li * 128 + (((4 * cq + 2 + (g >> 1)) ^ (li >> 1)) << 4) + 8 * (g & 1));
        u32 wdum = 0;
        if (WARM && w == 7) touch(wdum, l < 40 ? ((m * 10 + j) * 40 + l) * 128 : OOB, mk_rsrc(w2, 640 * 1280));
        if (IBW && w == 6) touch(wdum, l < 5 ? WS_IB + (m * 10 + j) * IB_B + l * 128 : OOB, rws);
        STAMP(1);
        sfor<0, 10>([&](auto ic) AINL {
          constexpr int s = ic.v;
          constexpr u32 so = (s & 3) * 16384, wo = 65536 + (s & 3) * 8192;
          bar();
          if constexpr (s == 0) STAMP(16);
          if constexpr (s == 3) STAMP(20);
          if constexpr (s == 6) STAMP(21);
          step64(IC<(s > 0)>{}, IC<2048>{}, F0, xb0 + so, wo, F1);
          step64(IC<1>{}, IC<2048>{}, F1, xb1 + so, wo + 4096, F0);
          if (s == s_own) {
#pragma unroll
            for (int tt = 0; tt < 2; ++tt)
#pragma unroll
              for (int ct = 0; ct < 2; ++ct) xs[ct][tt] = *(LAS u32x2*)((ct ? xo1 : xo0) + so + tt * 2048);
          }
        });
        mm(F1);
        bar();
        STAMP(2);
        u32x4 g0 = *(LAS u32x4*)(sm + ACT + 2 * 16384 + (tq * 32 + li) * 16);
        u32x4 g1 = *(LAS u32x4*)(sm + ACT + 2 * 16384 + (tq * 32 + 16 + li) * 16);
        cs[0] = *(LAS f32x4*)(sm + ACT + 2 * 16384 + 4096 + cq * 128 + g * 16);
        cs[1] = *(LAS f32x4*)(sm + ACT + 2 * 16384 + 4096 + cq * 128 + 64 + g * 16);
        if (!__all(g0.y == ta && g0.w == tb && g1.y == ta && g1.w == tb)) {
          g0 = poll_granule(WS_XS + (m * 128 + tq * 32 + li) * 16, true, rws, ta, tb);
          g1 = poll_granule(WS_XS + (m * 128 + tq * 32 + 16 + li) * 16, true, rws, ta, tb);
        }
        mu[0] = asf(g0.x); rs[0] = asf(g0.z); mu[1] = asf(g1.x); rs[1] = asf(g1.z);
#pragma unroll
        for (int tt = 0; tt < 2; ++tt) {
          const float q2 = 1.0f - LN_EPS * rs[tt] * rs[tt] + LN_EPS;
          const float r2 = rs[tt] * __builtin_amdgcn_rsqf(q2);
          const GeluK k = gelu_k(r2, __builtin_amdgcn_rcpf(r2));
          const u32x2 h0 = gelu4(acc[0][tt], cs[0], mu[tt], k), h1 = gelu4(acc[1][tt], cs[1], mu[tt], k);
          st128_sc1((u32x4){h0.x, h0.y, h1.x, h1.y}, WS_H1 + ((2 * j + cq) * 4 + g) * 32768 + (m * 128 + tq * 32 + tt * 16 + li) * 16, rws);
          SB();
        }
        STAMP(3);
        asm volatile("s_waitcnt vmcnt(0) lgkmcnt(0)\n\ts_barrier" : "+v"(wdum)::"memory");
      }
      STAMP(4);
      bar();
      STAMP(5);
      LAS char* const abt = pin(sm + ACT + g * 2048 + (tq * 32 + li) * 16);
#pragma unroll
      for (int a = 0; a < 2; ++a) { acc[a][0] = zero4; acc[a][1] = zero4; }
      sfor<0, 10>([&](auto ic) AINL {
        constexpr int s = ic.v;
        bar();
        if constexpr (s == 0) STAMP(17);
        if constexpr (s == 3) STAMP(27);
        if constexpr (s == 6) STAMP(28);
        step64(IC<(s > 0)>{}, IC<256>{}, F0, abt + (s & 3) * 16384, 16384 + s * 8192, F1);
        step64(IC<1>{}, IC<256>{}, F1, abt + (s & 3) * 16384 + 8192, 16384 + s * 8192 + 4096, F0);
      });
      mm(F1);
      STAMP(6);
      u32x4 x1p[2];
      {
        float sm1[2], sq1[2];
#pragma unroll
        for (int tt = 0; tt < 2; ++tt) {
          float s1 = 0.f, s2 = 0.f;
          f32x4 x1[2];
#pragma unroll
          for (int ct = 0; ct < 2; ++ct) {
            const float xv[4] = {bflo(xs[ct][tt].x), bfhi(xs[ct][tt].x), bflo(xs[ct][tt].y), bfhi(xs[ct][tt].y)};
#pragma unroll
            for (int e = 0; e < 4; ++e) {
              const float v = __builtin_fmaf(xv[e] - mu[tt], rs[tt], acc[ct][tt][e]);
              x1[ct][e] = v; s1 += v; s2 = __builtin_fmaf(v, v, s2);
            }
          }
          x1p[tt] = pack8(x1[0], x1[1]);
          *(LAS u32x4*)(sm + ACT + (2 * tq + tt) * 2048 + cq * 1024 + l * 16) = x1p[tt];
          st128_sc1(x1p[tt], WS_X1 + (m * 10 + j) * 16384 + (2 * tq + tt) * 2048 + cq * 1024 + l * 16, rws);
          sm1[tt] = sum32(sum16(s1)); sq1[tt] = sum32(sum16(s2));
        }
        const u32x2 st = {asu(g & 1 ? sm1[1] : sm1[0]), asu(g & 1 ? sq1[1] : sq1[0])};
        st64_sc1(st, l < 32 ? WS_ST1 + m * 20480 + (2 * j + cq) * 1024 + (tq * 32 + l) * 8 : OOB, rws);
      }
      STAMP(7);
      asm volatile("" : "+v"(xs[0][0]), "+v"(xs[0][1]), "+v"(xs[1][0]), "+v"(xs[1][1]), "+v"(x1p[0]), "+v"(x1p[1]));
      waitbar<0>();
      STAMP(8);
      {
        const int sg = (g & 1) * 32;
        LAS char* const bA = pin(sm + W + (4 * g + q4) * 320 + 8 * p4 + 160 * cq + (cq ? -sg : sg));
        LAS char* const bB = pin(sm + W + (4 * g + q4) * 320 + 8 * p4 + 160 * cq + (cq ? sg : -sg));
        LAS char* const bA2 = pin(bA + 40960);
        LAS char* const bB2 = pin(bB + 40960);
        struct F3 { bf16x8 a[5], b0, b1; };
        F3 G0, G1;
        f32x4 a3[5][2];
        auto step3 = [&](auto mf, F3& R, u32 so, u32 wo, const F3& M) AINL {
          LAS char* const pA = wo >= 40960 ? bA2 + (wo - 40960) : bA + wo;
          LAS char* const pB = wo >= 40960 ? bB2 + (wo - 40960) : bB + wo;
          auto mm = [&](int i) AINL { if constexpr (mf.v) a3[i >> 1][i & 1] = mfma(M.a[i >> 1], (i & 1) ? M.b1 : M.b0, a3[i >> 1][i & 1]); SB(); };
          auto ra = [&](int i) AINL { LAS char* const pw = ((i & 1) ? pB : pA) + 32 * i; R.a[i] = ldstr(pw, pw + 5120); SB(); };
          mm(0); ra(0); mm(1); R.b0 = lds128(ab + so); SB(); mm(2); R.b1 = lds128(ab + so + 2048); SB();
          mm(3); ra(1); mm(4); ra(2); mm(5); ra(3); mm(6); ra(4); mm(7); mm(8); mm(9);
        };
#pragma unroll
        for (int i = 0; i < 5; ++i) { a3[i][0] = zero4; a3[i][1] = zero4; }
        sfor<0, 10>([&](auto ic) AINL {
          constexpr int s = ic.v;
          constexpr u32 so = (s & 3) * 16384, wo = (s & 3) * 20480;
          if constexpr (s == 1) { bar(); STAMP(9); }
          if constexpr (s >= 1) bar();
          if constexpr (s == 1) STAMP(18);
          if constexpr (s == 3) STAMP(25);
          step3(IC<(s > 0)>{}, G0, so, wo, G1);
          step3(IC<1>{}, G1, so + 1024, wo + 10240, G0);
          if constexpr (s == 9) *(volatile LAS u32*)(sm + ACT + 3 * 16384 + 13312 + w * 4) = 0;
        });
#pragma unroll
        for (int i = 0; i < 5; ++i) { a3[i][0] = mfma(G1.a[i], G1.b0, a3[i][0]); a3[i][1] = mfma(G1.a[i], G1.b1, a3[i][1]); }
        bar();
        STAMP(10);
        float m1[2]; GeluK k1[2];
#pragma unroll
        for (int tt = 0; tt < 2; ++tt) {
          float s1 = 0.f, s2 = 0.f;
#pragma unroll
          for (int i = 0; i < 5; ++i) {
            const f32x2 v = *(LAS f32x2*)(sm + ACT + 2 * 16384 + (g + 4 * i) * 1024 + (tq * 32 + tt * 16 + li) * 8);
            s1 += v.x; s2 += v.y;
          }
          s1 = sum32(sum16(s1)) * (1.0f / 640.0f); s2 = sum32(sum16(s2)) * (1.0f / 640.0f);
          const float vq = fmaxf(__builtin_fmaf(-s1, s1, s2), 0.f) + LN_EPS, r1 = __builtin_amdgcn_rsqf(vq);
          m1[tt] = s1; k1[tt] = gelu_k(r1, vq * r1);
        }
        f32x4 c3[5];
#pragma unroll
        for (int i = 0; i < 5; ++i) c3[i] = *(LAS f32x4*)(sm + ACT + 3 * 16384 + 4096 + (80 * cq + 16 * i + 4 * g) * 4);
        auto act4 = [&](int i, int tt) AINL { return gelu4(a3[i][tt], c3[i], m1[tt], k1[tt]); };
        auto put = [&](u32x2 lo, u32x2 hi, int b, int tt) AINL {
          const u32x4 v = {lo.x, lo.y, hi.x, hi.y};
          const u32 ob = b0 + b - 2 * kt0;
          st128_sc1(v, b < nb ? WS_H2 + (m * 24 + ((b0 + b) >> 1)) * 16384 + (2 * tq + tt) * 2048 + ((b0 + b) & 1) * 1024 + l * 16 : OOB, rws);
          *(LAS u32x4*)(sm + ACT + (ob < 4 ? (ob >> 1) * 16384 + (ob & 1) * 1024 + (2 * tq + tt) * 2048 : 3 * 16384 + 12288) + l * 16) = v;
          SB();
        };
        LAS char* const xch = sm + ACT + 3 * 16384 + 8192 + tq * 1024 + l * 8;
        volatile LAS u32* const myf = (volatile LAS u32*)(sm + ACT + 3 * 16384 + 13312 + w * 4);
        auto partner = [&]() AINL {
          _Pragma("nounroll") for (int it = 0; it < SPIN_CAP; ++it)
            if (__builtin_amdgcn_readfirstlane(*(volatile LAS u32*)(sm + ACT + 3 * 16384 + 13312 + (w ^ 4) * 4))) break;
          asm volatile("" ::: "memory");
        };
        if (cq == 0) {
          *(volatile LAS u32x2*)(xch + 512) = act4(4, 1);
          *myf = 1;
          put(act4(0, 0), act4(1, 0), 0, 0); put(act4(2, 0), act4(3, 0), 1, 0);
          partner();
          put(act4(4, 0), *(LAS u32x2*)xch, 2, 0);
          put(act4(0, 1), act4(1, 1), 0, 1); put(act4(2, 1), act4(3, 1), 1, 1);
        } else {
          *(volatile LAS u32x2*)xch = act4(0, 0);
          *myf = 1;
          put(act4(1, 0), act4(2, 0), 3, 0); put(act4(3, 0), act4(4, 0), 4, 0);
          partner();
          put(*(LAS u32x2*)(xch + 512), act4(0, 1), 2, 1);
          put(act4(1, 1), act4(2, 1), 3, 1); put(act4(3, 1), act4(4, 1), 4, 1);
        }
      }
      STAMP(11);
      waitbar<0>();
      STAMP(12);
      const rsrc_t rdk = mk_rsrc(dskip, 1280);
      const u32x2 dk0 = bld64((cq * 32 + 4 * g) * 2, rdk, j * 128), dk1 = bld64((cq * 32 + 16 + 4 * g) * 2, rdk, j * 128);
#pragma unroll
      for (int a = 0; a < 2; ++a) { acc[a][0] = zero4; acc[a][1] = zero4; }
      sfor<0, 24>([&](auto ic) AINL {
        constexpr int s = ic.v;
        if constexpr (s == 2) { bar(); STAMP(13); }
        if constexpr (s >= 2) bar();
        if constexpr (s == 2) STAMP(19);
        if constexpr (s == 6) STAMP(22);
        if constexpr (s == 12) STAMP(23);
        if constexpr (s == 18) STAMP(24);
        step64(IC<(s > 0)>{}, IC<2048>{}, F0, ab + (s & 3) * 16384, ((s + 5) % 12) * 8192, F1);
        step64(IC<1>{}, IC<2048>{}, F1, ab + (s & 3) * 16384 + 1024, ((s + 5) % 12) * 8192 + 4096, F0);
      });
      mm(F1);
      bar();
      STAMP(14);
      const rsrc_t rout = mk_rsrc(out, 2048 * 2560);
#pragma unroll
      for (int tt = 0; tt < 2; ++tt) {
        const u32x4 sv = *(LAS u32x4*)(sm + ACT + w * 2048 + tt * 1024 + l * 16);
#pragma unroll
        for (int ct = 0; ct < 2; ++ct) {
          const float xv[4] = {bflo(xs[ct][tt].x), bfhi(xs[ct][tt].x), bflo(xs[ct][tt].y), bfhi(xs[ct][tt].y)};
          const u32 s0 = ct ? sv.z : sv.x, s1 = ct ? sv.w : sv.y;
          const float sy[4] = {bflo(s0), bfhi(s0), bflo(s1), bfhi(s1)};
          const u32 y0 = ct ? x1p[tt].z : x1p[tt].x, y1 = ct ? x1p[tt].w : x1p[tt].y;
          const float x1[4] = {bflo(y0), bfhi(y0), bflo(y1), bfhi(y1)};
          const u32x2 dk = ct ? dk1 : dk0;
          const float dv[4] = {bflo(dk.x), bfhi(dk.x), bflo(dk.y), bfhi(dk.y)};
          u32x4 o;
#pragma unroll
          for (int e = 0; e < 4; ++e) {
            const float sfull = __builtin_fmaf((xv[e] - mu[tt]) * rs[tt], dv[e], sy[e]);
            o[e] = asu(__builtin_fmaf(sfull, silu_f(x1[e] + acc[ct][tt][e]), xv[e]));
          }
          __builtin_amdgcn_raw_buffer_store_b128(o, rout, (m * 128 + tq * 32 + tt * 16 + li) * 2560 + (j * 64 + cq * 32 + ct * 16 + 4 * g) * 4, 0, 0);
        }
      }
      STAMP(15); CSTAMP(31);
    }
  } else if (role < 15) {
    const int a = role - 10;
    constexpr u32 MISC = 147456, CST = 157696, TAG = 159744, AS3 = 131072;
    if (w >= 8) {
      const rsrc_t rbr = mk_rsrc(b_re, 640 * 1280), rbi = mk_rsrc(b_im, 640 * 1280);
      const rsrc_t rcr = mk_rsrc(c_re, 640 * 1280), rci = mk_rsrc(c_im, 640 * 1280);
      const u32 vx0 = (m * 128 + 32 * (r8 >> 2) + 8 * L + (r8 & 3)) * 1280 + (((l & 7) ^ g) << 4);
      const u32 vx1 = (m * 128 + 32 * (2 + (r8 >> 2)) + 8 * L + (r8 & 3)) * 1280 + (((l & 7) ^ (4 + g)) << 4) - 1024;
      const u32 vb0 = (a * 128 + L * 32 + r8) * 1280 + (((l & 7) ^ g) << 4);
      const u32 vb1 = (a * 128 + L * 32 + 8 + r8) * 1280 + (((l & 7) ^ (4 + g)) << 4) - 1024;
      const u32 ldsw = L0 + L * 4096;
      auto issue_nk = [&](u32 d, rsrc_t r, u32 so) AINL { dma2<0>(d, vb0, vb1, r, so); dma2<0>(d + 2048, vb0, vb1, r, so + 20480); };
      auto issue1 = [&](int s) AINL {
        const int kt = wrapk(s + rotA1, 10);
        const u32 d = ldsw + (s % 3) * 49152;
        dma2<0>(d, vx0, vx1, rx, kt * 128); dma2<0>(d + 2048, vx0, vx1, rx, kt * 128 + 5120);
        issue_nk(d + 16384, rbr, kt * 128); issue_nk(d + 32768, rbi, kt * 128);
      };
      auto ktc = [&](int s) AINL { return s < 4 ? (s >> 1) * 10 + 2 * a + (s & 1) : ((s - 4) >> 3) * 10 + ssel_lt((s - 4) & 7, 2 * a, (s - 4) & 7, ((s - 4) & 7) + 2); };
      auto issue_c = [&](int s) AINL {
        const int kt = ktc(s);
        issue_nk(ldsw + 49152 + (s % 5) * 16384, kt < 10 ? rcr : rci, wrapk(kt, 10) * 128);
      };
      const u32 va = L * 4096 + l * 16;
      auto issue_h = [&](int s) AINL { dma4<1>(ldsw + ((s & 3) == 3 ? AS3 : (s & 3) * 16384), va, rws, WS_HH + (m * 20 + ktc(s)) * 16384); };
      issue1(0); issue1(1);
      sfor<0, 10>([&](auto ic) AINL {
        constexpr int s = ic.v;
        waitbar<(s == 9 ? 13 : 12)>();
        if constexpr (s + 2 < 10) issue1(s + 2);
        else if constexpr (s == 8) { dma1<1>(L0 + MISC + L * 1024, (L & 1) * 1024 + l * 16, rws, L < 2 ? WS_XS + m * 2048 : WS_RS + a * 2048); issue_c(0); issue_c(1); issue_c(2); }
        else { issue_c(3); issue_c(4); }
        if constexpr (s == 0) get_tag(TAG);
      });
      waitbar<20>();
      bar();
      bar();
      await(IC<0>{}, l < 5 ? WS_IB + IB_A + (m * 5 + l) * 128 + a * 8 : OOB, WS_IB + IB_A + (m * 5 + a) * 128 + l * 8, l < 5, [&]() AINL {});
      issue_h(4); issue_h(5); issue_c(5); issue_h(6); issue_c(6);
      sfor<4, 20>([&](auto ic) AINL {
        constexpr int s = ic.v;
        constexpr int gs[23] = {0, 0, 0, 0, 4, 8, 8, 8, 8, 8, 8, 8, 8, 8, 8, 8, 8, 8, 8, 8, 0, 0, 0};
        waitbar<gs[s + 1] + gs[s + 2]>();
        if constexpr (s + 3 < 20) { issue_h(s + 3); issue_c(s + 3); }
      });
    } else {
      const int p = a * 128 + w * 16 + li;
      const u32 plr = bld16(p * 2, mk_rsrc(lam_re, 1280)), pli = bld16(p * 2, mk_rsrc(lam_im, 1280)), pdt = bld16(p * 2, mk_rsrc(log_dt, 1280));
      row_stats();
      put_tag(TAG);
      float lbr, lbi, kr, ki;
      {
        const float Lr = bflo(plr), Li = bflo(pli);
        const float dt = bflo(pk2(expf(bflo(pdt)), 0.f));
        const float ea = expf(dt * Lr);
        float sn, cn; sincosf(dt * Li, &sn, &cn);
        lbr = ea * cn; lbi = ea * sn;
        const float inv = 2.0f / (Lr * Lr + Li * Li);
        kr = ((lbr - 1.0f) * Lr + lbi * Li) * inv;
        ki = (lbi * Lr - (lbr - 1.0f) * Li) * inv;
        asm volatile("" : "+v"(lbr), "+v"(lbi), "+v"(kr), "+v"(ki));
      }
      f32x4 are[8], aim[8];
      u32 wdum = 0;
      if (WARM && w >= 4) touch(wdum, l < 40 ? ((m * 5 + a) * 80 + (w & 1) * 40 + l) * 128 : OOB, mk_rsrc(w >= 6 ? c_im : c_re, 640 * 1280));
#pragma unroll
      for (int i = 0; i < 8; ++i) { are[i] = zero4; aim[i] = zero4; }
      {
        struct FW { bf16x8 br, bi; };
        struct FX { bf16x8 x[4]; };
        FW W0, W1; FX Xa, Xb;
        const int pc = a * 128 + m * 8 + w;
        u32x4 t0, t1; u32 u0, u1;
        auto half = [&](auto mf, auto rw, auto hh, FW& RW, LAS char* pw, FX& RX, LAS char* px, const FW& MW, const FX& MX) AINL {
          constexpr int h = hh.v;
          auto mm = [&](int i) AINL {
            if constexpr (mf.v) { if (i & 1) aim[4 * h + (i >> 1)] = mfma(MX.x[i >> 1], MW.bi, aim[4 * h + (i >> 1)]); else are[4 * h + (i >> 1)] = mfma(MX.x[i >> 1], MW.br, are[4 * h + (i >> 1)]); }
            SB();
          };
          auto rx_ = [&](int i) AINL { RX.x[i] = lds128(px + i * 2048); SB(); };
          if constexpr (rw.v) {
            mm(0); RW.br = lds128(pw); SB(); mm(1); rx_(0); mm(2); RW.bi = lds128(pw + 16384); SB();
            mm(3); rx_(1); mm(4); rx_(2); mm(5); rx_(3); mm(6); mm(7);
          } else {
            mm(0); rx_(0); mm(1); rx_(1); mm(2); rx_(2); mm(3); rx_(3); mm(4); mm(5); mm(6); mm(7);
          }
        };
        STAMP(1);
        sfor<0, 10>([&](auto ic) AINL {
          constexpr int s = ic.v;
          LAS char* const x0 = pin(sm + rb0 + (s % 3) * 49152);
          LAS char* const x1 = pin(sm + (rb0 ^ 64) + (s % 3) * 49152);
          LAS char* const w0 = pin(x0 + w * 2048 + 16384);
          LAS char* const w1_ = pin(x1 + w * 2048 + 16384);
          bar();
          if constexpr (s == 0) STAMP(16);
          half(IC<(s > 0)>{}, IC<1>{}, IC<1>{}, W0, w0, Xa, x0, W1, Xb);
          half(IC<1>{}, IC<0>{}, IC<0>{}, W0, w0, Xb, x0 + 8192, W0, Xa);
          half(IC<1>{}, IC<1>{}, IC<1>{}, W1, w1_, Xa, x1, W0, Xb);
          half(IC<1>{}, IC<0>{}, IC<0>{}, W1, w1_, Xb, x1 + 8192, W1, Xa);
          if constexpr (s == RSS) {
            const rsrc_t rbr = mk_rsrc(b_re, 640 * 1280), rbi = mk_rsrc(b_im, 640 * 1280);
            t0 = bld128(l * 16, rbr, pc * 1280); t1 = bld128(l * 16, rbi, pc * 1280);
            u0 = bld32(1024 + l * 4, rbr, pc * 1280); u1 = bld32(1024 + l * 4, rbi, pc * 1280);
          }
          if constexpr (s == RSS + 2) {
            auto sum10 = [&](u32x4 t, u32 u) AINL {
              return wavesum(((bflo(t.x) + bfhi(t.x)) + (bflo(t.y) + bfhi(t.y))) + ((bflo(t.z) + bfhi(t.z)) + (bflo(t.w) + bfhi(t.w))) + (bflo(u) + bfhi(u)));
            };
            st128_sc1((u32x4){asu(sum10(t0, u0)), ta, asu(sum10(t1, u1)), tb}, l == 0 ? WS_RS + pc * 16 : OOB, rws);
          }
        });
#pragma unroll
        for (int i = 0; i < 4; ++i) { are[4 + i] = mfma(Xb.x[i], W1.br, are[4 + i]); aim[4 + i] = mfma(Xb.x[i], W1.bi, aim[4 + i]); }
      }
      bar();
      STAMP(2);
      if (w < 2) {
        u32x4 g0 = *(LAS u32x4*)(sm + MISC + tid * 16);
        if (!__all(g0.y == ta && g0.w == tb)) g0 = poll_granule(WS_XS + (m * 128 + tid) * 16, true, rws, ta, tb);
        const float rs = asf(g0.z);
        *(LAS float*)(sm + CST + (tid >> 5) * 144 + (tid & 31) * 4) = rs;
        *(LAS float*)(sm + CST + 576 + (tid >> 5) * 144 + (tid & 31) * 4) = -rs * asf(g0.x);
      }
      u32x4 rg = *(LAS u32x4*)(sm + MISC + 2048 + (w * 16 + li) * 16);
      if (!__all(rg.y == ta && rg.w == tb)) rg = poll_granule(WS_RS + p * 16, true, rws, ta, tb);
      const float sre = asf(rg.x), sim = asf(rg.z);
      asm volatile("s_waitcnt vmcnt(0) lgkmcnt(0)\n\ts_barrier" : "+v"(wdum)::"memory");
      STAMP(21);
      LAS char* const cst = pin(sm + CST + g * 144);
#pragma unroll
      for (int mt = 0; mt < 8; ++mt) {
        const f32x4 rv = *(LAS f32x4*)(cst + mt * 16), nv = *(LAS f32x4*)(cst + 576 + mt * 16);
        are[mt] = rv * are[mt] + nv * sre;
        aim[mt] = rv * aim[mt] + nv * sim;
      }
      float hr = 0.f, hi = 0.f;
#pragma unroll
      for (int mt = 0; mt < 8; ++mt)
#pragma unroll
        for (int e = 0; e < 4; ++e) {
          const float nr = __builtin_fmaf(-lbi, hi, __builtin_fmaf(lbr, hr, are[mt][e]));
          const float ni = __builtin_fmaf(lbi, hr, __builtin_fmaf(lbr, hi, aim[mt][e]));
          hr = nr; hi = ni;
        }
      SB(); STAMP(23); SB();
      float er[4], ei[4];
      gather4(hr, er); gather4(hi, ei);
      float p32r = lbr, p32i = lbi;
#pragma unroll
      for (int i = 0; i < 5; ++i) { const float t = p32r * p32r - p32i * p32i; p32i = 2.0f * p32r * p32i; p32r = t; }
      float cr[5], ci[5];
      cr[0] = 0.f; ci[0] = 0.f;
#pragma unroll
      for (int q = 0; q < 4; ++q) {
        cr[q + 1] = __builtin_fmaf(-p32i, ci[q], __builtin_fmaf(p32r, cr[q], er[q]));
        ci[q + 1] = __builtin_fmaf(p32i, cr[q], __builtin_fmaf(p32r, ci[q], ei[q]));
      }
      st128_sc1((u32x4){asu(cr[4]), ta, asu(ci[4]), tb}, l < 16 ? WS_EG + (m * 640 + p) * 16 : OOB, rws);
      SB();
      STAMP(3);
      const float p64r = p32r * p32r - p32i * p32i, p64i = 2.0f * p32r * p32i;
      const float Pr = p64r * p64r - p64i * p64i, Pi = 2.0f * p64r * p64i;
#pragma unroll
      for (int mt = 0; mt < 8; ++mt) {
#pragma unroll
        for (int e = 0; e < 4; ++e) {
          const float ur = are[mt][e], ui = aim[mt][e];
          are[mt][e] = __builtin_fmaf(kr, ur, -ki * ui);
          aim[mt][e] = __builtin_fmaf(-kr, ui, -ki * ur);
        }
        are[mt] = keep(are[mt]); aim[mt] = keep(aim[mt]);
      }
      SB(); STAMP(24); SB();
      float Hr = 0.f, Hi = 0.f;
      if (m > 0) {
        u32x4 e0, e1, e2, e3;
        const int m0 = m - 1 - 4 * g;
        const u32 o0 = WS_EG + (m0 * 640 + p) * 16;
        const bool v0 = m0 >= 0, v1 = m0 >= 1, v2 = m0 >= 2, v3 = m0 >= 3;
        _Pragma("nounroll") for (int it = 0; it < SPIN_CAP; ++it) {
          asm volatile("buffer_load_dwordx4 %0, %4, %8, 0 offen sc1\n\tbuffer_load_dwordx4 %1, %5, %8, 0 offen sc1\n\t"
                       "buffer_load_dwordx4 %2, %6, %8, 0 offen sc1\n\tbuffer_load_dwordx4 %3, %7, %8, 0 offen sc1\n\ts_waitcnt vmcnt(0)"
                       : "=&v"(e0), "=&v"(e1), "=&v"(e2), "=&v"(e3)
                       : "v"(v0 ? o0 : OOB), "v"(v1 ? o0 - 10240 : OOB), "v"(v2 ? o0 - 20480 : OOB), "v"(v3 ? o0 - 30720 : OOB), "s"(rws) : "memory");
          const bool ok = (!v0 || (e0.y == ta && e0.w == tb)) && (!v1 || (e1.y == ta && e1.w == tb)) &&
                          (!v2 || (e2.y == ta && e2.w == tb)) && (!v3 || (e3.y == ta && e3.w == tb));
          if (__all(ok)) break;
          __builtin_amdgcn_s_sleep(1);
        }
        float qr = asf(e3.x), qi = asf(e3.z), t;
        t = __builtin_fmaf(-Pi, qi, __builtin_fmaf(Pr, qr, asf(e2.x))); qi = __builtin_fmaf(Pi, qr, __builtin_fmaf(Pr, qi, asf(e2.z))); qr = t;
        t = __builtin_fmaf(-Pi, qi, __builtin_fmaf(Pr, qr, asf(e1.x))); qi = __builtin_fmaf(Pi, qr, __builtin_fmaf(Pr, qi, asf(e1.z))); qr = t;
        t = __builtin_fmaf(-Pi, qi, __builtin_fmaf(Pr, qr, asf(e0.x))); qi = __builtin_fmaf(Pi, qr, __builtin_fmaf(Pr, qi, asf(e0.z))); qr = t;
        float gr[4], gi[4];
        gather4(qr, gr); gather4(qi, gi);
        const float P2r = Pr * Pr - Pi * Pi, P2i = 2.0f * Pr * Pi;
        const float P4r = P2r * P2r - P2i * P2i, P4i = 2.0f * P2r * P2i;
        Hr = gr[3]; Hi = gi[3];
#pragma unroll
        for (int q = 2; q >= 0; --q) {
          t = __builtin_fmaf(-P4i, Hi, __builtin_fmaf(P4r, Hr, gr[q])); Hi = __builtin_fmaf(P4i, Hr, __builtin_fmaf(P4r, Hi, gi[q])); Hr = t;
        }
      }
      STAMP(4);
      {
        const float p96r = p64r * p32r - p64i * p32i, p96i = p64r * p32i + p64i * p32r;
        const float fr = g == 0 ? 1.f : g == 1 ? p32r : g == 2 ? p64r : p96r;
        const float fi = g == 0 ? 0.f : g == 1 ? p32i : g == 2 ? p64i : p96i;
        const float c0r = g == 0 ? cr[0] : g == 1 ? cr[1] : g == 2 ? cr[2] : cr[3];
        const float c0i = g == 0 ? ci[0] : g == 1 ? ci[1] : g == 2 ? ci[2] : ci[3];
        const float sr = __builtin_fmaf(-fi, Hi, __builtin_fmaf(fr, Hr, c0r)), si = __builtin_fmaf(fi, Hr, __builtin_fmaf(fr, Hi, c0i));
        hr = __builtin_fmaf(kr, sr, -ki * si);
        hi = __builtin_fmaf(-kr, si, -ki * sr);
      }
      {
        LAS char* const tb_ = sm + (w < 6 ? w * 8192 : AS3 + (w - 6) * 8192);
        u32x4 hv[2][2][2];
        const int swc = ((li & 3) << 1) | ((li >> 2) & 1) | ((li >> 3) << 3);
        const int q4 = li >> 2, p4 = l & 3;
        const int c0 = 8 * (g & 1) + q4, c1 = c0 + 4;
        const int sw0 = ((c0 & 3) << 1) | ((c0 >> 3) << 3), sw1 = sw0 | 1;
        const u32 ob = WS_HH + (m * 20 + 2 * a + (w >> 2)) * 16384 + ((w >> 1) & 1) * 1024 + ((2 * (w & 1) + (g & 1)) * 16 + li) * 16;
#pragma unroll
        for (int hf = 0; hf < 2; ++hf) {
#pragma unroll
          for (int mt = 4 * hf; mt < 4 * hf + 4; ++mt)
#pragma unroll
            for (int e = 0; e < 4; ++e) {
              const float nr = __builtin_fmaf(lbi, hi, __builtin_fmaf(lbr, hr, are[mt][e]));
              const float ni = __builtin_fmaf(-lbi, hr, __builtin_fmaf(lbr, hi, aim[mt][e]));
              hr = nr; hi = ni;
              are[mt][e] = hr; aim[mt][e] = hi;
            }
#pragma unroll
          for (int i = 2 * hf; i < 2 * hf + 2; ++i) {
            *(LAS u32x4*)(tb_ + li * 256 + (((4 * g + i) ^ swc) << 4)) = pack8(are[2 * i], are[2 * i + 1]);
            *(LAS u32x4*)(tb_ + 4096 + li * 256 + (((4 * g + i) ^ swc) << 4)) = pack8(aim[2 * i], aim[2 * i + 1]);
          }
          asm volatile("s_waitcnt lgkmcnt(0)" ::: "memory");
#pragma unroll
          for (int part = 0; part < 2; ++part)
#pragma unroll
            for (int u = 0; u < 2; ++u) {
              const int tt = 4 * u + 2 * (g >> 1) + hf, ch = 2 * tt + (p4 >> 1);
              const bf16x8 v = ldstr(tb_ + part * 4096 + c0 * 256 + ((ch ^ sw0) << 4) + 8 * (p4 & 1),
                                     tb_ + part * 4096 + c1 * 256 + ((ch ^ sw1) << 4) + 8 * (p4 & 1));
              hv[hf][part][u] = __builtin_bit_cast(u32x4, v);
              st128_sc1(hv[hf][part][u], ob + part * 163840 + tt * 2048, rws);
            }
          SB();
          STAMP(25 + hf);
        }
        bar();
        LAS char* const hd = sm + (w >> 2) * 16384 + (g >> 1) * 4096 + ((w >> 1) & 1) * 1024 + ((2 * (w & 1) + (g & 1)) * 16 + li) * 16;
        LAS char* const hd1 = hd + ((w >> 2) ? AS3 - 16384 : 32768);
#pragma unroll
        for (int hf = 0; hf < 2; ++hf)
#pragma unroll
          for (int u = 0; u < 2; ++u) {
            *(LAS u32x4*)(hd + u * 8192 + hf * 2048) = hv[hf][0][u];
            *(LAS u32x4*)(hd1 + u * 8192 + hf * 2048) = hv[hf][1][u];
          }
      }
      waitbar<0>();
      STAMP(5);
      f32x4 ac[4][2];
#pragma unroll
      for (int i = 0; i < 4; ++i) { ac[i][0] = zero4; ac[i][1] = zero4; }
      LAS char* const ab = pin(sm + tq * 4096 + l * 16);
      LAS char* const ab3 = pin(ab + AS3);
      LAS char* cb[2][2];
#pragma unroll
      for (int i = 0; i < 2; ++i) { cb[0][i] = pin(sm + 49152 + i * 49152 + cq * 8192 + rb0); cb[1][i] = pin(sm + 49152 + i * 49152 + cq * 8192 + (rb0 ^ 64)); }
      struct FC { bf16x8 c[4], b0, b1; };
      FC G0, G1;
      auto stepC = [&](auto mf, FC& R, int ks, int s, const FC& M) AINL {
        LAS char* const pa = ((s & 3) == 3 ? ab3 : ab + (s & 3) * 16384) + ks * 1024;
        LAS char* const pc = cb[ks][(s % 5) / 3] + ((s % 5) % 3) * 16384;
        auto mm = [&](int i) AINL { if constexpr (mf.v) ac[i >> 1][i & 1] = mfma(M.c[i >> 1], (i & 1) ? M.b1 : M.b0, ac[i >> 1][i & 1]); SB(); };
        auto rc = [&](int i) AINL { R.c[i] = lds128(pc + i * 2048); SB(); };
        mm(0); R.b0 = lds128(pa); SB(); mm(1); rc(0); mm(2); R.b1 = lds128(pa + 2048); SB(); mm(3); rc(1); mm(4); rc(2); mm(5); rc(3); mm(6); mm(7);
      };
      sfor<0, 20>([&](auto ic) AINL {
        constexpr int s = ic.v;
        if constexpr (s == 4) { bar(); STAMP(6); }
        if constexpr (s >= 4) bar();
        if constexpr (s == 4) STAMP(17);
        stepC(IC<(s > 0)>{}, G0, 0, s, G1);
        stepC(IC<1>{}, G1, 1, s, G0);
      });
#pragma unroll
      for (int ct = 0; ct < 4; ++ct) { ac[ct][0] = mfma(G1.c[ct], G1.b0, ac[ct][0]); ac[ct][1] = mfma(G1.c[ct], G1.b1, ac[ct][1]); }
      STAMP(7);
#pragma unroll
      for (int cb_ = 0; cb_ < 2; ++cb_)
#pragma unroll
        for (int tt = 0; tt < 2; ++tt)
          st128_sc1(pack8(ac[2 * cb_][tt], ac[2 * cb_ + 1][tt]), WS_SS + ((m * 10 + 2 * a + cq) * 8 + tq + 4 * cb_) * 2048 + tt * 1024 + l * 16, rws);
      waitbar<0>();
      if (w == 0) signal(l < 2 ? WS_IB + (m * 10 + 2 * a + l) * IB_B + 512 : OOB);
      STAMP(8);
    }
  } else if (w < 8) {
    row_stats();
    auto add8 = [&](float (&c)[8], u32x4 t) AINL {
      c[0] += bflo(t.x); c[1] += bfhi(t.x); c[2] += bflo(t.y); c[3] += bfhi(t.y); c[4] += bflo(t.z); c[5] += bfhi(t.z); c[6] += bflo(t.w); c[7] += bfhi(t.w);
    };
    STAMP(1);
    if (SDELAY) __builtin_amdgcn_s_sleep(SDELAY);
    if (m < 12) {
      const int c = m;
      const rsrc_t rw3 = mk_rsrc(w3, 640 * 3072);
      float cs[8] = {0.f, 0.f, 0.f, 0.f, 0.f, 0.f, 0.f, 0.f};
      const u32 vo = (tid >> 4) * 3072 + c * 256 + (tid & 15) * 16;
      u32x4 t[20];
#pragma unroll
      for (int i = 0; i < 20; ++i) t[i] = bld128(vo, rw3, i * 32 * 3072);
      SB();
#pragma unroll
      for (int i = 0; i < 20; ++i) add8(cs, t[i]);
#pragma unroll
      for (int e = 0; e < 8; ++e) cs[e] = sum32(sum16(cs[e]));
      if (l < 16) {
        *(LAS f32x4*)(sm + w * 512 + l * 32) = (f32x4){cs[0], cs[1], cs[2], cs[3]};
        *(LAS f32x4*)(sm + w * 512 + l * 32 + 16) = (f32x4){cs[4], cs[5], cs[6], cs[7]};
      }
      bar();
      if (w < 2) {
        float s = 0.f;
#pragma unroll
        for (int i = 0; i < 8; ++i) s += *(LAS float*)(sm + i * 512 + tid * 4);
        st32_sc1(asu(s), WS_CS3 + (c * 128 + tid) * 4, rws);
      }
      waitbar<0>();
      if (w == 0) signal(l < 16 ? WS_CF + c * 128 + l * 8 : OOB);
    }
    {
      const rsrc_t rw4 = mk_rsrc(w4, 1536 * 1280);
      u32 d = 0;
      if (m < 12) { touch(d, (m * 640 + tid) * 128, rw4); touch(d, tid < 128 ? (m * 640 + 512 + tid) * 128 : OOB, rw4); }
      else {
#pragma unroll
        for (int i = 0; i < 4; ++i) touch(d, i < 3 || tid < 384 ? (7680 + (m - 12) * 1920 + i * 512 + tid) * 128 : OOB, rw4);
      }
      asm volatile("s_waitcnt vmcnt(0)" : "+v"(d)::"memory");
    }
    STAMP(2);
    if (m == 0) {
#pragma unroll
      for (int i = 0; i < 4; ++i) (void)poll_granule(WS_XS + (i * 512 + tid) * 16, true, rws, ta, tb);
      bar();
      if (tid == 0) st64_sc1((u32x2){ta, tb}, WS_EPOCH, rws);
    }
  }
#endif
}

extern "C" void kernel_launch(void* const* d_in, const int* in_sizes, int n_in, void* d_out, int out_size,
                              void* d_ws, size_t ws_size, hipStream_t stream) {
  (void)in_sizes; (void)n_in; (void)out_size; (void)ws_size;
  s5_mixer_kernel<<<256, 768, 0, stream>>>(d_in[0], d_in[1], d_in[2], d_in[3], d_in[4], d_in[5], d_in[6], d_in[7],
                                           d_in[8], d_in[10], d_in[11], d_in[12], d_in[13], d_out, d_ws);
}
```

```cpp
#include <hip/hip_runtime.h>
#include <hip/hip_bf16.h>
#include <stdint.h>

#define DI __device__ __forceinline__
#define LAS __attribute__((address_space(3)))
#define AINL __attribute__((always_inline))
typedef unsigned u32;
typedef unsigned long long u64;
typedef u32 u32x4 __attribute__((ext_vector_type(4)));
typedef u32 u32x2 __attribute__((ext_vector_type(2)));
typedef float f32x4 __attribute__((ext_vector_type(4)));
typedef float f32x2 __attribute__((ext_vector_type(2)));
typedef __bf16 bf16x8 __attribute__((ext_vector_type(8)));
typedef __bf16 bf16x2 __attribute__((ext_vector_type(2)));
typedef short s16x4 __attribute__((ext_vector_type(4)));
typedef short s16x8 __attribute__((ext_vector_type(8)));

#define XMAP 1
#define ABL 0
#define PSLEEP 2
#define WARM 1
#define IBW 1
#define RSS 2
#define SDELAY 32
#define ROTM 0
#define STAMPS 0

constexpr u32 WS_EPOCH = 0;
constexpr u32 WS_CF = 0x1000;
constexpr u32 WS_IB = 0x340000;
constexpr u32 IB_B = 640, IB_A = 160 * 640;
constexpr u32 WS_XS = 0x20000;
constexpr u32 WS_CS3 = 0x2A000;
constexpr u32 WS_RS = 0x58000;
constexpr u32 WS_EG = 0x30000;
constexpr u32 WS_ST1 = 0x60000;
constexpr u32 WS_SS = 0xC0000;
constexpr u32 WS_H1 = 0x400000;
constexpr u32 WS_X1 = 0x680000;
constexpr u32 WS_HH = 0x900000;
constexpr u32 WS_H2 = 0xE00000;
constexpr u32 WS_STAMP = 0x1400000;
constexpr u32 WS_BYTES = 0x1500000;
constexpr u32 OOB = 0x7FFFFFF0u;
constexpr float LN_EPS = 1e-5f;
constexpr int SPIN_CAP = 1 << 17;

template <int I> struct IC { static constexpr int v = I; };
template <int B, int E, class F> DI void sfor(F&& f) {
  if constexpr (B < E) { f(IC<B>{}); sfor<B + 1, E>(f); }
}

#if defined(__HIP_DEVICE_COMPILE__) && __HIP_DEVICE_COMPILE__
typedef __amdgpu_buffer_rsrc_t rsrc_t;
DI rsrc_t mk_rsrc(const void* p, u32 bytes) { return __builtin_amdgcn_make_buffer_rsrc((void*)p, 0, bytes, 0x00020000); }

template <int SC1> DI void dma1(u32 lds, u32 voff, rsrc_t r, u32 soff) {
  if (ABL == 2) return;
  if constexpr (SC1)
    asm volatile("s_mov_b32 m0, %0\n\ts_nop 0\n\tbuffer_load_dwordx4 %1, %2, %3 offen sc1 lds" ::"s"(lds), "v"(voff), "s"(r), "s"(soff) : "memory");
  else
    asm volatile("s_mov_b32 m0, %0\n\ts_nop 0\n\tbuffer_load_dwordx4 %1, %2, %3 offen lds" ::"s"(lds), "v"(voff), "s"(r), "s"(soff) : "memory");
}
template <int SC1> DI void dma2(u32 lds, u32 v0, u32 v1, rsrc_t r, u32 soff) {
  if (ABL == 2) return;
  if constexpr (SC1)
    asm volatile("s_mov_b32 m0, %0\n\ts_nop 0\n\tbuffer_load_dwordx4 %1, %3, %4 offen sc1 lds\n\tbuffer_load_dwordx4 %2, %3, %4 offen offset:1024 sc1 lds"
                 ::"s"(lds), "v"(v0), "v"(v1), "s"(r), "s"(soff) : "memory");
  else
    asm volatile("s_mov_b32 m0, %0\n\ts_nop 0\n\tbuffer_load_dwordx4 %1, %3, %4 offen lds\n\tbuffer_load_dwordx4 %2, %3, %4 offen offset:1024 lds"
                 ::"s"(lds), "v"(v0), "v"(v1), "s"(r), "s"(soff) : "memory");
}
template <int SC1> DI void dma4(u32 lds, u32 v, rsrc_t r, u32 soff) {
  if (ABL == 2) return;
  if constexpr (SC1)
    asm volatile("s_mov_b32 m0, %0\n\ts_nop 0\n\tbuffer_load_dwordx4 %1, %2, %3 offen sc1 lds\n\tbuffer_load_dwordx4 %1, %2, %3 offen offset:1024 sc1 lds\n\t"
                 "buffer_load_dwordx4 %1, %2, %3 offen offset:2048 sc1 lds\n\tbuffer_load_dwordx4 %1, %2, %3 offen offset:3072 sc1 lds"
                 ::"s"(lds), "v"(v), "s"(r), "s"(soff) : "memory");
  else
    asm volatile("s_mov_b32 m0, %0\n\ts_nop 0\n\tbuffer_load_dwordx4 %1, %2, %3 offen lds\n\tbuffer_load_dwordx4 %1, %2, %3 offen offset:1024 lds\n\t"
                 "buffer_load_dwordx4 %1, %2, %3 offen offset:2048 lds\n\tbuffer_load_dwordx4 %1, %2, %3 offen offset:3072 lds"
                 ::"s"(lds), "v"(v), "s"(r), "s"(soff) : "memory");
}
constexpr int VC(int n) { return ABL == 2 ? 0 : n; }
template <int N> DI void waitv() { asm volatile("s_waitcnt vmcnt(%0)" ::"i"(VC(N)) : "memory"); }
DI void bar() { asm volatile("s_waitcnt lgkmcnt(0)\n\ts_barrier" ::: "memory"); }
template <int N> DI void waitbar() { asm volatile("s_waitcnt vmcnt(%0) lgkmcnt(0)\n\ts_barrier" ::"i"(VC(N)) : "memory"); }

DI void touch(u32& d, u32 voff, rsrc_t r) { asm volatile("buffer_load_dword %0, %1, %2, 0 offen" : "+v"(d) : "v"(voff), "s"(r) : "memory"); }

DI u32x4 bld128(u32 voff, rsrc_t r, u32 soff) { return __builtin_amdgcn_raw_buffer_load_b128(r, voff, soff, 0); }
DI u32x2 bld64(u32 voff, rsrc_t r, u32 soff) { return __builtin_amdgcn_raw_buffer_load_b64(r, voff, soff, 0); }
DI u32x2 bld64_sc1(u32 voff, rsrc_t r, u32 soff) { return __builtin_amdgcn_raw_buffer_load_b64(r, voff, soff, 16); }
DI u32 bld32(u32 voff, rsrc_t r, u32 soff) { return __builtin_amdgcn_raw_buffer_load_b32(r, voff, soff, 0); }
DI u32 bld16(u32 voff, rsrc_t r) { return __builtin_amdgcn_raw_buffer_load_b16(r, voff, 0, 0); }
DI void st128_sc1(u32x4 v, u32 voff, rsrc_t r) { __builtin_amdgcn_raw_buffer_store_b128(v, r, voff, 0, 16); }
DI void st64_sc1(u32x2 v, u32 voff, rsrc_t r) { __builtin_amdgcn_raw_buffer_store_b64(v, r, voff, 0, 16); }
DI void st32_sc1(u32 v, u32 voff, rsrc_t r) { __builtin_amdgcn_raw_buffer_store_b32(v, r, voff, 0, 16); }

DI void poll_flags(u32 voff, bool active, rsrc_t ws, u32 ta, u32 tb) {
  _Pragma("nounroll") for (int it = 0; it < SPIN_CAP; ++it) {
    u32x2 v;
    asm volatile("buffer_load_dwordx2 %0, %1, %2, 0 offen sc1\n\ts_waitcnt vmcnt(0)" : "=&v"(v) : "v"(voff), "s"(ws) : "memory");
    const bool ok = !active || (v.x == ta && v.y == tb);
    if (__all(ok)) break;
    __builtin_amdgcn_s_sleep(1);
  }
}
DI u32x4 poll_granule(u32 voff, bool active, rsrc_t ws, u32 ta, u32 tb) {
  u32x4 v;
  _Pragma("nounroll") for (int it = 0; it < SPIN_CAP; ++it) {
    asm volatile("buffer_load_dwordx4 %0, %1, %2, 0 offen sc1\n\ts_waitcnt vmcnt(0)" : "=&v"(v) : "v"(voff), "s"(ws) : "memory");
    const bool ok = !active || (v.y == ta && v.w == tb);
    if (__all(ok)) break;
    __builtin_amdgcn_s_sleep(1);
  }
  return v;
}

DI float asf(u32 x) { return __uint_as_float(x); }
DI u32 asu(float x) { return __float_as_uint(x); }
DI float bflo(u32 x) { return asf(x << 16); }
DI float bfhi(u32 x) { return asf(x & 0xffff0000u); }
DI u32 pk2(float a, float b) { f32x2 p = {a, b}; bf16x2 q = __builtin_convertvector(p, bf16x2); return __builtin_bit_cast(u32, q); }
DI float sum16(float x) { const u32 u = asu(x); auto r = __builtin_amdgcn_permlane16_swap(u, u, false, false); const u32 a = r[0], b = r[1]; return asf(a) + asf(b); }
DI float sum32(float x) { const u32 u = asu(x); auto r = __builtin_amdgcn_permlane32_swap(u, u, false, false); const u32 a = r[0], b = r[1]; return asf(a) + asf(b); }
template <int C> DI float dppadd(float x) { return x + asf(__builtin_amdgcn_update_dpp(0u, asu(x), C, 0xf, 0xf, true)); }
DI float wavesum(float x) { x = dppadd<0xB1>(x); x = dppadd<0x4E>(x); x = dppadd<0x141>(x); x = dppadd<0x140>(x); return sum32(sum16(x)); }
DI void gather4(float x, float (&e)[4]) {
  const u32 u = asu(x);
  auto p = __builtin_amdgcn_permlane16_swap(u, u, false, false);
  const u32 a = p[0], b = p[1];
  auto pa = __builtin_amdgcn_permlane32_swap(a, a, false, false);
  auto pb = __builtin_amdgcn_permlane32_swap(b, b, false, false);
  const u32 e0 = pa[0], e2 = pa[1], e1 = pb[0], e3 = pb[1];
  e[0] = asf(e0); e[1] = asf(e1); e[2] = asf(e2); e[3] = asf(e3);
}
typedef f32x4 GeluK;
DI GeluK gelu_k(float r, float ri) { return (f32x4){-2.302208198f * r, -0.1029432397f * r * r * r, ri, 0.f}; }
DI u32 gelu2(f32x2 t, GeluK k) {
  const f32x2 u = t * __builtin_elementwise_fma(t * t, (f32x2){k.y, k.y}, (f32x2){k.x, k.x});
  const f32x2 e = {__builtin_amdgcn_exp2f(u.x), __builtin_amdgcn_exp2f(u.y)};
  const f32x2 d = __builtin_elementwise_fma(e, (f32x2){k.z, k.z}, (f32x2){k.z, k.z});
  const f32x2 o = t * (f32x2){__builtin_amdgcn_rcpf(d.x), __builtin_amdgcn_rcpf(d.y)};
  return __builtin_bit_cast(u32, __builtin_convertvector(o, bf16x2));
}
DI u32x2 gelu4(f32x4 acc, f32x4 cs, float mu, GeluK k) {
  const f32x2 nm = {-mu, -mu};
  return (u32x2){gelu2(__builtin_elementwise_fma(nm, cs.xy, acc.xy), k), gelu2(__builtin_elementwise_fma(nm, cs.zw, acc.zw), k)};
}
DI float silu_f(float z) { return z * __builtin_amdgcn_rcpf(1.0f + __builtin_amdgcn_exp2f(z * -1.4426950409f)); }
DI f32x4 mfma(bf16x8 a, bf16x8 b, f32x4 c) { if (ABL == 1 || ABL == 4) { asm volatile("" : "+v"(c) : "v"(a), "v"(b)); return c; } return __builtin_amdgcn_mfma_f32_16x16x32_bf16(a, b, c, 0, 0, 0); }
DI bf16x8 lds128(LAS char* p) { if (ABL == 1 || ABL == 3) { bf16x8 r; asm volatile("" : "=v"(r)); return r; } return *(LAS bf16x8*)p; }
DI bf16x8 ldstr(LAS char* p0, LAS char* p1) {
  if (ABL == 1 || ABL == 3) { bf16x8 r; asm volatile("" : "=v"(r)); return r; }
  const s16x4 a = __builtin_amdgcn_ds_read_tr16_b64_v4i16((LAS s16x4*)p0);
  const s16x4 b = __builtin_amdgcn_ds_read_tr16_b64_v4i16((LAS s16x4*)p1);
  const s16x8 w = __builtin_shufflevector(a, b, 0, 1, 2, 3, 4, 5, 6, 7);
  return __builtin_bit_cast(bf16x8, w);
}
DI u32x4 pack8(f32x4 a, f32x4 b) { u32x4 r; r.x = pk2(a.x, a.y); r.y = pk2(a.z, a.w); r.z = pk2(b.x, b.y); r.w = pk2(b.z, b.w); return r; }
DI u32x2 pack4(f32x4 a) { u32x2 r; r.x = pk2(a.x, a.y); r.y = pk2(a.z, a.w); return r; }
DI int ssel_lt(int a, int b, int x, int y) { int r; asm("s_cmp_lt_i32 %1, %2\n\ts_cselect_b32 %0, %3, %4" : "=s"(r) : "s"(a), "s"(b), "s"(x), "s"(y) : "scc"); return r; }
DI int wrapk(int k, int n) { return k >= n ? k - n : k; }
#define SB() __builtin_amdgcn_sched_barrier(0)
DI f32x4 keep(f32x4 v) { asm volatile("" : "+v"(v)); return v; }
DI LAS char* pin(LAS char* p) { asm volatile("" : "+v"(p)); return p; }
#endif

#define STAMP(i) do { } while (0)
#define CSTAMP(i) do { } while (0)

__global__ __launch_bounds__(768, 3) void s5_mixer_kernel(
    const void* __restrict__ x, const void* __restrict__ lam_re, const void* __restrict__ lam_im,
    const void* __restrict__ b_re, const void* __restrict__ b_im, const void* __restrict__ c_re,
    const void* __restrict__ c_im, const void* __restrict__ log_dt, const void* __restrict__ dskip,
    const void* __restrict__ w1, const void* __restrict__ w2, const void* __restrict__ w3,
    const void* __restrict__ w4, void* __restrict__ out, void* __restrict__ ws) {
#if defined(__HIP_DEVICE_COMPILE__) && __HIP_DEVICE_COMPILE__
  __shared__ __attribute__((aligned(16))) char smem_[163840];
  LAS char* const sm = (LAS char*)smem_;
  const u32 L0 = (u32)(size_t)sm;
  const int tid = threadIdx.x, l = tid & 63;
  const int w = __builtin_amdgcn_readfirstlane(tid >> 6);
  const int bid = blockIdx.x, xcd = bid & 7, rr = bid >> 3;
  const int m = (xcd >> 1) * 4 + (rr < 20 ? rr / 5 : (rr - 20) / 3);
  const int role = rr < 20 ? rr % 5 + 5 * (xcd & 1) : 10 + (rr - 20) % 3 + 3 * (xcd & 1);
  const int g = l >> 4, li = l & 15, r8 = l >> 3, tq = w & 3, cq = w >> 2, L = w - 8;
  const rsrc_t rws = mk_rsrc(ws, WS_BYTES);
  const rsrc_t rx = mk_rsrc(x, 2048 * 1280);
  STAMP(0); CSTAMP(30);
  const int fm = ((5 * (m & 3) + 1) >> 1) + (m >> 2);
  const int rot1 = (2 * (role % 5) + (role >= 5) + fm) % 10, rotA1 = (2 * (role % 5) + 1 + fm) % 10;

  u32 ta, tb;
  auto row_stats = [&]() AINL {
    const int stok = m * 128 + role * 8 + w;
    const u32x2 ep = bld64_sc1(0, rws, 0);
    const u32x4 ra = bld128(l * 16, rx, stok * 1280);
    const u32 rb_ = bld32(1024 + l * 4, rx, stok * 1280);
    const u64 e = (((u64)__builtin_amdgcn_readfirstlane(ep.y) << 32) | (u32)__builtin_amdgcn_readfirstlane(ep.x)) + 0x9E3779B97F4A7C15ull;
    ta = (u32)e; tb = (u32)(e >> 32);
    float v[10] = {bflo(ra.x), bfhi(ra.x), bflo(ra.y), bfhi(ra.y), bflo(ra.z), bfhi(ra.z), bflo(ra.w), bfhi(ra.w), bflo(rb_), bfhi(rb_)};
    float s = 0.f;
#pragma unroll
    for (int i = 0; i < 10; ++i) s += v[i];
    const float mu = wavesum(s) * (1.0f / 640.0f);
    float q = 0.f;
#pragma unroll
    for (int i = 0; i < 10; ++i) { const float d = v[i] - mu; q = __builtin_fmaf(d, d, q); }
    const float rs = 1.0f / sqrtf(wavesum(q) * (1.0f / 640.0f) + LN_EPS);
    u32x4 gr = {asu(mu), ta, asu(rs), tb};
    st128_sc1(gr, l == 0 ? WS_XS + stok * 16 : OOB, rws);
  };
  auto put_tag = [&](u32 off) AINL { if (tid == 0) *(LAS u32x2*)(sm + off) = (u32x2){ta, tb}; };
  auto get_tag = [&](u32 off) AINL {
    const u32x2 t = *(volatile LAS u32x2*)(sm + off);
    ta = __builtin_amdgcn_readfirstlane(t.x); tb = __builtin_amdgcn_readfirstlane(t.y);
  };
  auto signal = [&](u32 off) AINL { st64_sc1((u32x2){ta, tb}, off, rws); };
  auto await = [&](auto nv, u32 sig, u32 src, bool act, auto&& pre) AINL {
    waitbar<nv.v>();
    if (L == 0) { signal(sig); poll_flags(src, act, rws, ta, tb); }
    else pre();
    bar();
  };
  const u32 rb0 = li * 128 + ((g ^ (li >> 1)) << 4);
  const f32x4 zero4 = {0.f, 0.f, 0.f, 0.f};

  if (role < 10) {
    const int j = role;
    constexpr u32 ACT = 0, W = 65536;
    const int b0 = j < 9 ? 5 * j : 44, nb = j < 8 ? 5 : 4, kt0 = (b0 + 1) >> 1;
    if (w >= 8) {
      const rsrc_t rw1 = mk_rsrc(w1, 640 * 1280), rw2 = mk_rsrc(w2, 640 * 1280);
      const rsrc_t rw3 = mk_rsrc(w3, 640 * 3072), rw4 = mk_rsrc(w4, 1536 * 1280);
      const u32 vx0 = (m * 128 + L * 32 + r8) * 1280 + (((l & 7) ^ g) << 4);
      const u32 vx1 = (m * 128 + L * 32 + 8 + r8) * 1280 + (((l & 7) ^ (4 + g)) << 4) - 1024;
      const u32 wch = j * 128 + (((l & 7) ^ (2 * g)) << 4);
      const u32 vq0 = ((L >> 1) * 32 + (r8 >> 2) * 8 + (L & 1) * 4 + (r8 & 3)) * 1280 + wch, vq1 = vq0 + 16 * 1280 - 1024;
      const u32 ldsA = L0 + ACT + L * 4096, ldsW = L0 + W + L * 2048;
      auto issue_1 = [&](int s) AINL {
        const int kt = wrapk(s + rot1, 10);
        dma2<0>(ldsA + (s & 3) * 16384, vx0, vx1, rx, kt * 128);
        dma2<0>(ldsW + 65536 + (s & 3) * 8192, vq0, vq1, rw1, kt * 81920);
        dma2<0>(ldsA + (s & 3) * 16384 + 2048, vx0, vx1, rx, kt * 128 + 20480);
      };
      issue_1(0); issue_1(1); issue_1(2);
      const u32 vw0 = (L * 16 + r8) * 1280 + wch, vw1 = vw0 + 10240 - 1024;
      const u32 va = L * 4096 + l * 16, vl = L * 1024 + l * 16;
      auto k3 = [&](int s) AINL { return s == 0 ? j : ssel_lt(s - 1, j, s - 1, s); };
      auto k4 = [&](int s) AINL { return s < 2 ? kt0 + s : ssel_lt(s - 2, kt0, s - 2, s); };
      u32 v3[5];
#pragma unroll
      for (int i = 0; i < 5; ++i) {
        const int c = 64 * i + l, r = c / 20, pos = c - 20 * r;
        v3[i] = (L * 16 + r) * 3072 + b0 * 64 + ((pos ^ (2 * ((r >> 2) & 1))) << 4) - 1024 * (i & 1);
      }
      auto issue_w2 = [&](int s) AINL { dma2<0>(ldsW + 16384 + s * 8192, vw0, vw1, rw2, s * 81920); };
      auto issue_w3 = [&](int s) AINL {
        const u32 d = L0 + W + (s & 3) * 20480 + L * 5120, so = k3(s) * 196608;
        dma2<0>(d, v3[0], v3[1], rw3, so); dma2<0>(d + 2048, v3[2], v3[3], rw3, so); dma1<0>(d + 4096, v3[4], rw3, so);
      };
      const u32 vz0 = r8 * 1280 + wch, vz1 = vz0 + 10240 - 1024;
      auto whole_w = [&](u32 d, rsrc_t r, u32 so) AINL {
#pragma unroll
        for (int i = 0; i < 4; ++i) dma2<0>(d + i * 2048, vz0, vz1, r, so + i * 20480);
      };
      auto issue_w4 = [&](int s) AINL { dma2<0>(ldsW + ((s + 5) % 12) * 8192, vw0, vw1, rw4, k4(s) * 81920); };
      auto issue_a = [&](int s, u32 base) AINL { dma4<1>(ldsA + (s & 3) * 16384, va, rws, base); };
      auto issue_h1 = [&](int s) AINL {
#pragma unroll
        for (int ks = 0; ks < 2; ++ks) {
          const int b = 2 * s + ks, grp = b / 5;
          const int t = m * 128 + l - ((grp & 1) ? -(1 + (grp >> 1)) : (1 + (grp >> 1)));
          dma2<1>(L0 + ACT + (s & 3) * 16384 + ks * 8192 + L * 2048, t >= 0 ? t * 16 + 32 : OOB, t < 2048 - 64 ? t * 16 + 32 : OOB, rws, WS_H1 + (b * 4 + L) * 32768 - 32);
        }
      };
      const bf16x8 ones = __builtin_bit_cast(bf16x8, (u32x4){0x3F803F80u, 0x3F803F80u, 0x3F803F80u, 0x3F803F80u});
      LAS char* const wl = pin(sm + W + 65536 + (4 * g + (li >> 2)) * 128 + ((L ^ ((2 * g + (li >> 3)) & 3)) << 5) + 8 * (l & 3));
      f32x4 cs = zero4;
      sfor<0, 10>([&](auto ic) AINL {
        constexpr int s = ic.v;
        constexpr int gs[13] = {6, 6, 6, 6, 6, 6, 6, 6, 6, 6, 3, 2, 0};
        waitbar<(s == 9 ? 4 : gs[s + 1] + gs[s + 2])>();
        if constexpr (s + 3 < 10) issue_1(s + 3);
        if constexpr (s == 0) get_tag(W);
        const bf16x8 c0 = ldstr(wl + (s & 3) * 8192, wl + (s & 3) * 8192 + 2048), c1 = ldstr(wl + (s & 3) * 8192 + 4096, wl + (s & 3) * 8192 + 6144);
        cs = keep(mfma(c1, ones, mfma(c0, ones, cs)));
        if constexpr (s == 7) { dma1<1>(L0 + ACT + 2 * 16384 + L * 1024, vl, rws, WS_XS + m * 2048); issue_w2(0); }
        if constexpr (s == 8) issue_w2(1);
      });
      if (li == 0) *(LAS f32x4*)(sm + ACT + 2 * 16384 + 4096 + L * 64 + g * 16) = cs;
      bar();
      issue_w2(2); issue_w2(3); issue_w2(4); issue_w2(5); issue_w2(6);
      const u32 ib = WS_IB + (m * 10 + j) * IB_B, ibt = WS_IB + (m * 10 + l) * IB_B;
      const int j5 = j % 5;
      await(IC<63>{},
            l < 10 ? ibt + j * 8 : l < 20 ? (j5 < 3 && m < 15 ? ibt + (10 + j5 + 3 * (j / 5)) * 8 : OOB)
                   : (l < 30 && j5 >= 2 && m > 0 ? ibt - 30 * IB_B + (14 + j5 + 3 * (j / 5)) * 8 : OOB),
            ib + l * 8, l < 10 || (l < 16 && m > 0) || (l >= 16 && l < 22 && m < 15),
            [&]() AINL { whole_w(L0 + W + 16384 + (6 + L) * 8192, rw2, (6 + L) * 81920); });
      issue_h1(0); issue_h1(1); issue_h1(2);
      sfor<0, 10>([&](auto ic) AINL {
        constexpr int s = ic.v;
        constexpr int gs[13] = {4, 4, 4, 4, 4, 4, 4, 4, 4, 4, 5, 5, 5};
        waitbar<gs[s + 1] + gs[s + 2]>();
        if constexpr (s + 3 < 10) issue_h1(s + 3);
        else issue_w3(s - 7);
      });
      issue_w3(3);
      await(IC<15>{}, l < 10 ? ibt + 256 + j * 8 : OOB, l < 10 ? ib + 256 + l * 8 : WS_CF + ((b0 >> 2) + l - 10) * 128, l < (j < 9 ? 12 : 11), [&]() AINL {});
      sfor<1, 4>([&](auto ic) AINL { constexpr int s = ic.v; issue_a(s, WS_X1 + (m * 10 + k3(s)) * 16384); });
      sfor<1, 10>([&](auto ic) AINL {
        constexpr int s = ic.v;
        constexpr int gs[13] = {0, 4, 4, 4, 9, 9, 9, 9, 9, 9, 4, 2, 0};
        waitbar<gs[s + 1] + gs[s + 2]>();
        if constexpr (s + 3 < 10) {
          issue_a(s + 3, WS_X1 + (m * 10 + k3(s + 3)) * 16384);
          issue_w3(s + 3);
        } else if constexpr (s == 7) {
          issue_a(2, WS_ST1 + m * 20480);
        } else if constexpr (s == 8) {
          dma1<1>(L0 + ACT + 3 * 16384 + L * 1024, vl, rws, WS_ST1 + m * 20480 + 16384);
          dma1<1>(L0 + ACT + 3 * 16384 + 4096 + L * 1024, vl, rws, WS_CS3 + b0 * 128);
        }
      });
      waitbar<0>();
      sfor<0, 12>([&](auto ic) AINL { issue_w4(ic.v); });
      await(IC<20>{}, l < 10 ? ibt + 384 + j * 8 : OOB, ib + 384 + l * 8, l < 10, [&]() AINL {});
      sfor<2, 5>([&](auto ic) AINL { constexpr int s = ic.v; issue_a(s, WS_H2 + (m * 24 + k4(s)) * 16384); });
      u32x2 fs;
      const u32 fso = ib + 512;
      sfor<2, 24>([&](auto ic) AINL {
        constexpr int s = ic.v;
        constexpr int gs[27] = {0, 0, 4, 4, 4, 6, 6, 6, 6, 6, 6, 6, 6, 6, 6, 6, 6, 4, 4, 4, 5, 4, 4, 4, 4, 0, 0};
        if constexpr (s == 20) { asm volatile("s_waitcnt vmcnt(%1) lgkmcnt(0)\n\ts_barrier" : "+v"(fs) : "i"(VC(gs[s + 1] + gs[s + 2])) : "memory"); }
        else waitbar<gs[s + 1] + gs[s + 2]>();
        if constexpr (s == 17) asm volatile("buffer_load_dwordx2 %0, %1, %2, 0 offen sc1" : "=v"(fs) : "v"(fso), "s"(rws) : "memory");
        if constexpr (s + 3 < 24) issue_a(s + 3, WS_H2 + (m * 24 + k4(s + 3)) * 16384);
        if constexpr (s <= 13) issue_w4(s + 10);
        if constexpr (s == 21) {
          if (!__all(fs.x == ta && fs.y == tb)) poll_flags(fso, true, rws, ta, tb);
          issue_a(0, WS_SS + (m * 10 + j) * 16384);
        }
      });
      waitbar<0>();
    } else {
      row_stats();
      put_tag(W);
      const int q4 = li >> 2, p4 = l & 3;
      LAS char* const wb0 = pin(sm + W + (4 * g + q4) * 128 + ((((2 * cq) ^ ((2 * g + (q4 >> 1)) & 3))) << 5) + 8 * p4);
      LAS char* const wb1 = pin(sm + W + (4 * g + q4) * 128 + ((((2 * cq + 1) ^ ((2 * g + (q4 >> 1)) & 3))) << 5) + 8 * p4);
      LAS char* const wh0 = pin(wb0 + 65536);
      LAS char* const wh1 = pin(wb1 + 65536);
      LAS char* const ab = pin(sm + ACT + tq * 4096 + l * 16);
      struct F64 { bf16x8 a0, a1, b0, b1; };
      F64 F0, F1;
      f32x4 acc[2][2], cs[2];
      u32x2 xs[2][2];
      auto step64 = [&](auto mf, auto tts, F64& R, LAS char* pb, u32 wo, const F64& M) AINL {
        LAS char* const p0 = wo >= 65536 ? wh0 + (wo - 65536) : wb0 + wo;
        LAS char* const p1 = wo >= 65536 ? wh1 + (wo - 65536) : wb1 + wo;
        auto mm = [&](int i) AINL { if constexpr (mf.v) acc[i & 1][i >> 1] = mfma((i & 1) ? M.a1 : M.a0, (i >> 1) ? M.b1 : M.b0, acc[i & 1][i >> 1]); SB(); };
        mm(0); R.a0 = ldstr(p0, p0 + 2048); SB();
        mm(1); R.b0 = lds128(pb); R.b1 = lds128(pb + tts.v); SB();
        mm(2); R.a1 = ldstr(p1, p1 + 2048); SB();
        mm(3);
      };
      auto mm = [&](const F64& M) AINL {
        acc[0][0] = mfma(M.a0, M.b0, acc[0][0]); acc[1][0] = mfma(M.a1, M.b0, acc[1][0]);
        acc[0][1] = mfma(M.a0, M.b1, acc[0][1]); acc[1][1] = mfma(M.a1, M.b1, acc[1][1]);
      };
      float mu[2], rs[2];
      {
        LAS char* const xb0 = pin(sm + ACT + tq * 4096 + rb0);
        LAS char* const xb1 = pin(sm + ACT + tq * 4096 + (rb0 ^ 64));
#pragma unroll
        for (int a = 0; a < 2; ++a) { acc[a][0] = zero4; acc[a][1] = zero4; }
        const int s_own = wrapk(j - rot1 + 10, 10);
        LAS char* const xo0 = pin(sm + ACT + tq * 4096 + li * 128 + (((4 * cq + (g >> 1)) ^ (li >> 1)) << 4) + 8 * (g & 1));
        LAS char* const xo1 = pin(sm + ACT + tq * 4096 + li * 128 + (((4 * cq + 2 + (g >> 1)) ^ (li >> 1)) << 4) + 8 * (g & 1));
        u32 wdum = 0;
        if (WARM && w == 7) touch(wdum, l < 40 ? ((m * 10 + j) * 40 + l) * 128 : OOB, mk_rsrc(w2, 640 * 1280));
        if (IBW && w == 6) touch(wdum, l < 5 ? WS_IB + (m * 10 + j) * IB_B + l * 128 : OOB, rws);
        STAMP(1);
        sfor<0, 10>([&](auto ic) AINL {
          constexpr int s = ic.v;
          constexpr u32 so = (s & 3) * 16384, wo = 65536 + (s & 3) * 8192;
          bar();
          if constexpr (s == 0) STAMP(16);
          if constexpr (s == 3) STAMP(20);
          if constexpr (s == 6) STAMP(21);
          step64(IC<(s > 0)>{}, IC<2048>{}, F0, xb0 + so, wo, F1);
          step64(IC<1>{}, IC<2048>{}, F1, xb1 + so, wo + 4096, F0);
          if (s == s_own) {
#pragma unroll
            for (int tt = 0; tt < 2; ++tt)
#pragma unroll
              for (int ct = 0; ct < 2; ++ct) xs[ct][tt] = *(LAS u32x2*)((ct ? xo1 : xo0) + so + tt * 2048);
          }
        });
        mm(F1);
        bar();
        STAMP(2);
        u32x4 g0 = *(LAS u32x4*)(sm + ACT + 2 * 16384 + (tq * 32 + li) * 16);
        u32x4 g1 = *(LAS u32x4*)(sm + ACT + 2 * 16384 + (tq * 32 + 16 + li) * 16);
        cs[0] = *(LAS f32x4*)(sm + ACT + 2 * 16384 + 4096 + cq * 128 + g * 16);
        cs[1] = *(LAS f32x4*)(sm + ACT + 2 * 16384 + 4096 + cq * 128 + 64 + g * 16);
        if (!__all(g0.y == ta && g0.w == tb && g1.y == ta && g1.w == tb)) {
          g0 = poll_granule(WS_XS + (m * 128 + tq * 32 + li) * 16, true, rws, ta, tb);
          g1 = poll_granule(WS_XS + (m * 128 + tq * 32 + 16 + li) * 16, true, rws, ta, tb);
        }
        mu[0] = asf(g0.x); rs[0] = asf(g0.z); mu[1] = asf(g1.x); rs[1] = asf(g1.z);
#pragma unroll
        for (int tt = 0; tt < 2; ++tt) {
          const float q2 = 1.0f - LN_EPS * rs[tt] * rs[tt] + LN_EPS;
          const float r2 = rs[tt] * __builtin_amdgcn_rsqf(q2);
          const GeluK k = gelu_k(r2, __builtin_amdgcn_rcpf(r2));
          const u32x2 h0 = gelu4(acc[0][tt], cs[0], mu[tt], k), h1 = gelu4(acc[1][tt], cs[1], mu[tt], k);
          st128_sc1((u32x4){h0.x, h0.y, h1.x, h1.y}, WS_H1 + ((2 * j + cq) * 4 + g) * 32768 + (m * 128 + tq * 32 + tt * 16 + li) * 16, rws);
          SB();
        }
        STAMP(3);
        asm volatile("s_waitcnt vmcnt(0) lgkmcnt(0)\n\ts_barrier" : "+v"(wdum)::"memory");
      }
      STAMP(4);
      bar();
      STAMP(5);
      LAS char* const abt = pin(sm + ACT + g * 2048 + (tq * 32 + li) * 16);
#pragma unroll
      for (int a = 0; a < 2; ++a) { acc[a][0] = zero4; acc[a][1] = zero4; }
      sfor<0, 10>([&](auto ic) AINL {
        constexpr int s = ic.v;
        bar();
        if constexpr (s == 0) STAMP(17);
        if constexpr (s == 3) STAMP(27);
        if constexpr (s == 6) STAMP(28);
        step64(IC<(s > 0)>{}, IC<256>{}, F0, abt + (s & 3) * 16384, 16384 + s * 8192, F1);
        step64(IC<1>{}, IC<256>{}, F1, abt + (s & 3) * 16384 + 8192, 16384 + s * 8192 + 4096, F0);
      });
      mm(F1);
      STAMP(6);
      u32x4 x1p[2];
      {
        float sm1[2], sq1[2];
#pragma unroll
        for (int tt = 0; tt < 2; ++tt) {
          float s1 = 0.f, s2 = 0.f;
          f32x4 x1[2];
#pragma unroll
          for (int ct = 0; ct < 2; ++ct) {
            const float xv[4] = {bflo(xs[ct][tt].x), bfhi(xs[ct][tt].x), bflo(xs[ct][tt].y), bfhi(xs[ct][tt].y)};
#pragma unroll
            for (int e = 0; e < 4; ++e) {
              const float v = __builtin_fmaf(xv[e] - mu[tt], rs[tt], acc[ct][tt][e]);
              x1[ct][e] = v; s1 += v; s2 = __builtin_fmaf(v, v, s2);
            }
          }
          x1p[tt] = pack8(x1[0], x1[1]);
          *(LAS u32x4*)(sm + ACT + (2 * tq + tt) * 2048 + cq * 1024 + l * 16) = x1p[tt];
          st128_sc1(x1p[tt], WS_X1 + (m * 10 + j) * 16384 + (2 * tq + tt) * 2048 + cq * 1024 + l * 16, rws);
          sm1[tt] = sum32(sum16(s1)); sq1[tt] = sum32(sum16(s2));
        }
        const u32x2 st = {asu(g & 1 ? sm1[1] : sm1[0]), asu(g & 1 ? sq1[1] : sq1[0])};
        st64_sc1(st, l < 32 ? WS_ST1 + m * 20480 + (2 * j + cq) * 1024 + (tq * 32 + l) * 8 : OOB, rws);
      }
      STAMP(7);
      asm volatile("" : "+v"(xs[0][0]), "+v"(xs[0][1]), "+v"(xs[1][0]), "+v"(xs[1][1]), "+v"(x1p[0]), "+v"(x1p[1]));
      waitbar<0>();
      STAMP(8);
      {
        const int sg = (g & 1) * 32;
        LAS char* const bA = pin(sm + W + (4 * g + q4) * 320 + 8 * p4 + 160 * cq + (cq ? -sg : sg));
        LAS char* const bB = pin(sm + W + (4 * g + q4) * 320 + 8 * p4 + 160 * cq + (cq ? sg : -sg));
        LAS char* const bA2 = pin(bA + 40960);
        LAS char* const bB2 = pin(bB + 40960);
        struct F3 { bf16x8 a[5], b0, b1; };
        F3 G0, G1;
        f32x4 a3[5][2];
        auto step3 = [&](auto mf, F3& R, u32 so, u32 wo, const F3& M) AINL {
          LAS char* const pA = wo >= 40960 ? bA2 + (wo - 40960) : bA + wo;
          LAS char* const pB = wo >= 40960 ? bB2 + (wo - 40960) : bB + wo;
          auto mm = [&](int i) AINL { if constexpr (mf.v) a3[i >> 1][i & 1] = mfma(M.a[i >> 1], (i & 1) ? M.b1 : M.b0, a3[i >> 1][i & 1]); SB(); };
          auto ra = [&](int i) AINL { LAS char* const pw = ((i & 1) ? pB : pA) + 32 * i; R.a[i] = ldstr(pw, pw + 5120); SB(); };
          mm(0); ra(0); mm(1); R.b0 = lds128(ab + so); SB(); mm(2); R.b1 = lds128(ab + so + 2048); SB();
          mm(3); ra(1); mm(4); ra(2); mm(5); ra(3); mm(6); ra(4); mm(7); mm(8); mm(9);
        };
#pragma unroll
        for (int i = 0; i < 5; ++i) { a3[i][0] = zero4; a3[i][1] = zero4; }
        sfor<0, 10>([&](auto ic) AINL {
          constexpr int s = ic.v;
          constexpr u32 so = (s & 3) * 16384, wo = (s & 3) * 20480;
          if constexpr (s == 1) { bar(); STAMP(9); }
          if constexpr (s >= 1) bar();
          if constexpr (s == 1) STAMP(18);
          if constexpr (s == 3) STAMP(25);
          step3(IC<(s > 0)>{}, G0, so, wo, G1);
          step3(IC<1>{}, G1, so + 1024, wo + 10240, G0);
          if constexpr (s == 9) *(volatile LAS u32*)(sm + ACT + 3 * 16384 + 13312 + w * 4) = 0;
        });
#pragma unroll
        for (int i = 0; i < 5; ++i) { a3[i][0] = mfma(G1.a[i], G1.b0, a3[i][0]); a3[i][1] = mfma(G1.a[i], G1.b1, a3[i][1]); }
        bar();
        STAMP(10);
        float m1[2]; GeluK k1[2];
#pragma unroll
        for (int tt = 0; tt < 2; ++tt) {
          float s1 = 0.f, s2 = 0.f;
#pragma unroll
          for (int i = 0; i < 5; ++i) {
            const f32x2 v = *(LAS f32x2*)(sm + ACT + 2 * 16384 + (g + 4 * i) * 1024 + (tq * 32 + tt * 16 + li) * 8);
            s1 += v.x; s2 += v.y;
          }
          s1 = sum32(sum16(s1)) * (1.0f / 640.0f); s2 = sum32(sum16(s2)) * (1.0f / 640.0f);
          const float vq = fmaxf(__builtin_fmaf(-s1, s1, s2), 0.f) + LN_EPS, r1 = __builtin_amdgcn_rsqf(vq);
          m1[tt] = s1; k1[tt] = gelu_k(r1, vq * r1);
        }
        f32x4 c3[5];
#pragma unroll
        for (int i = 0; i < 5; ++i) c3[i] = *(LAS f32x4*)(sm + ACT + 3 * 16384 + 4096 + (80 * cq + 16 * i + 4 * g) * 4);
        auto act4 = [&](int i, int tt) AINL { return gelu4(a3[i][tt], c3[i], m1[tt], k1[tt]); };
        auto put = [&](u32x2 lo, u32x2 hi, int b, int tt) AINL {
          const u32x4 v = {lo.x, lo.y, hi.x, hi.y};
          const u32 ob = b0 + b - 2 * kt0;
          st128_sc1(v, b < nb ? WS_H2 + (m * 24 + ((b0 + b) >> 1)) * 16384 + (2 * tq + tt) * 2048 + ((b0 + b) & 1) * 1024 + l * 16 : OOB, rws);
          *(LAS u32x4*)(sm + ACT + (ob < 4 ? (ob >> 1) * 16384 + (ob & 1) * 1024 + (2 * tq + tt) * 2048 : 3 * 16384 + 12288) + l * 16) = v;
          SB();
        };
        LAS char* const xch = sm + ACT + 3 * 16384 + 8192 + tq * 1024 + l * 8;
        volatile LAS u32* const myf = (volatile LAS u32*)(sm + ACT + 3 * 16384 + 13312 + w * 4);
        auto partner = [&]() AINL {
          _Pragma("nounroll") for (int it = 0; it < SPIN_CAP; ++it)
            if (__builtin_amdgcn_readfirstlane(*(volatile LAS u32*)(sm + ACT + 3 * 16384 + 13312 + (w ^ 4) * 4))) break;
          asm volatile("" ::: "memory");
        };
        if (cq == 0) {
          *(volatile LAS u32x2*)(xch + 512) = act4(4, 1);
          *myf = 1;
          put(act4(0, 0), act4(1, 0), 0, 0); put(act4(2, 0), act4(3, 0), 1, 0);
          partner();
          put(act4(4, 0), *(LAS u32x2*)xch, 2, 0);
          put(act4(0, 1), act4(1, 1), 0, 1); put(act4(2, 1), act4(3, 1), 1, 1);
        } else {
          *(volatile LAS u32x2*)xch = act4(0, 0);
          *myf = 1;
          put(act4(1, 0), act4(2, 0), 3, 0); put(act4(3, 0), act4(4, 0), 4, 0);
          partner();
          put(*(LAS u32x2*)(xch + 512), act4(0, 1), 2, 1);
          put(act4(1, 1), act4(2, 1), 3, 1); put(act4(3, 1), act4(4, 1), 4, 1);
        }
      }
      STAMP(11);
      waitbar<0>();
      STAMP(12);
      const rsrc_t rdk = mk_rsrc(dskip, 1280);
      const u32x2 dk0 = bld64((cq * 32 + 4 * g) * 2, rdk, j * 128), dk1 = bld64((cq * 32 + 16 + 4 * g) * 2, rdk, j * 128);
#pragma unroll
      for (int a = 0; a < 2; ++a) { acc[a][0] = zero4; acc[a][1] = zero4; }
      sfor<0, 24>([&](auto ic) AINL {
        constexpr int s = ic.v;
        if constexpr (s == 2) { bar(); STAMP(13); }
        if constexpr (s >= 2) bar();
        if constexpr (s == 2) STAMP(19);
        if constexpr (s == 6) STAMP(22);
        if constexpr (s == 12) STAMP(23);
        if constexpr (s == 18) STAMP(24);
        step64(IC<(s > 0)>{}, IC<2048>{}, F0, ab + (s & 3) * 16384, ((s + 5) % 12) * 8192, F1);
        step64(IC<1>{}, IC<2048>{}, F1, ab + (s & 3) * 16384 + 1024, ((s + 5) % 12) * 8192 + 4096, F0);
      });
      mm(F1);
      bar();
      STAMP(14);
      const rsrc_t rout = mk_rsrc(out, 2048 * 2560);
#pragma unroll
      for (int tt = 0; tt < 2; ++tt) {
        const u32x4 sv = *(LAS u32x4*)(sm + ACT + w * 2048 + tt * 1024 + l * 16);
#pragma unroll
        for (int ct = 0; ct < 2; ++ct) {
          const float xv[4] = {bflo(xs[ct][tt].x), bfhi(xs[ct][tt].x), bflo(xs[ct][tt].y), bfhi(xs[ct][tt].y)};
          const u32 s0 = ct ? sv.z : sv.x, s1 = ct ? sv.w : sv.y;
          const float sy[4] = {bflo(s0), bfhi(s0), bflo(s1), bfhi(s1)};
          const u32 y0 = ct ? x1p[tt].z : x1p[tt].x, y1 = ct ? x1p[tt].w : x1p[tt].y;
          const float x1[4] = {bflo(y0), bfhi(y0), bflo(y1), bfhi(y1)};
          const u32x2 dk = ct ? dk1 : dk0;
          const float dv[4] = {bflo(dk.x), bfhi(dk.x), bflo(dk.y), bfhi(dk.y)};
          u32x4 o;
#pragma unroll
          for (int e = 0; e < 4; ++e) {
            const float sfull = __builtin_fmaf((xv[e] - mu[tt]) * rs[tt], dv[e], sy[e]);
            o[e] = asu(__builtin_fmaf(sfull, silu_f(x1[e] + acc[ct][tt][e]), xv[e]));
          }
          __builtin_amdgcn_raw_buffer_store_b128(o, rout, (m * 128 + tq * 32 + tt * 16 + li) * 2560 + (j * 64 + cq * 32 + ct * 16 + 4 * g) * 4, 0, 0);
        }
      }
      STAMP(15); CSTAMP(31);
    }
  } else if (role < 15) {
    const int a = role - 10;
    constexpr u32 MISC = 147456, CST = 157696, TAG = 159744, AS3 = 131072;
    if (w >= 8) {
      const rsrc_t rbr = mk_rsrc(b_re, 640 * 1280), rbi = mk_rsrc(b_im, 640 * 1280);
      const rsrc_t rcr = mk_rsrc(c_re, 640 * 1280), rci = mk_rsrc(c_im, 640 * 1280);
      const u32 vx0 = (m * 128 + 32 * (r8 >> 2) + 8 * L + (r8 & 3)) * 1280 + (((l & 7) ^ g) << 4);
      const u32 vx1 = (m * 128 + 32 * (2 + (r8 >> 2)) + 8 * L + (r8 & 3)) * 1280 + (((l & 7) ^ (4 + g)) << 4) - 1024;
      const u32 vb0 = (a * 128 + L * 32 + r8) * 1280 + (((l & 7) ^ g) << 4);
      const u32 vb1 = (a * 128 + L * 32 + 8 + r8) * 1280 + (((l & 7) ^ (4 + g)) << 4) - 1024;
      const u32 ldsw = L0 + L * 4096;
      auto issue_nk = [&](u32 d, rsrc_t r, u32 so) AINL { dma2<0>(d, vb0, vb1, r, so); dma2<0>(d + 2048, vb0, vb1, r, so + 20480); };
      auto issue1 = [&](int s) AINL {
        const int kt = wrapk(s + rotA1, 10);
        const u32 d = ldsw + (s % 3) * 49152;
        dma2<0>(d, vx0, vx1, rx, kt * 128); dma2<0>(d + 2048, vx0, vx1, rx, kt * 128 + 5120);
        issue_nk(d + 16384, rbr, kt * 128); issue_nk(d + 32768, rbi, kt * 128);
      };
      auto ktc = [&](int s) AINL { return s < 4 ? (s >> 1) * 10 + 2 * a + (s & 1) : ((s - 4) >> 3) * 10 + ssel_lt((s - 4) & 7, 2 * a, (s - 4) & 7, ((s - 4) & 7) + 2); };
      auto issue_c = [&](int s) AINL {
        const int kt = ktc(s);
        issue_nk(ldsw + 49152 + (s % 5) * 16384, kt < 10 ? rcr : rci, wrapk(kt, 10) * 128);
      };
      const u32 va = L * 4096 + l * 16;
      auto issue_h = [&](int s) AINL { dma4<1>(ldsw + ((s & 3) == 3 ? AS3 : (s & 3) * 16384), va, rws, WS_HH + (m * 20 + ktc(s)) * 16384); };
      issue1(0); issue1(1);
      sfor<0, 10>([&](auto ic) AINL {
        constexpr int s = ic.v;
        waitbar<(s == 9 ? 13 : 12)>();
        if constexpr (s + 2 < 10) issue1(s + 2);
        else if constexpr (s == 8) { dma1<1>(L0 + MISC + L * 1024, (L & 1) * 1024 + l * 16, rws, L < 2 ? WS_XS + m * 2048 : WS_RS + a * 2048); issue_c(0); issue_c(1); issue_c(2); }
        else { issue_c(3); issue_c(4); }
        if constexpr (s == 0) get_tag(TAG);
      });
      waitbar<20>();
      bar();
      bar();
      await(IC<0>{}, l < 5 ? WS_IB + IB_A + (m * 5 + l) * 128 + a * 8 : OOB, WS_IB + IB_A + (m * 5 + a) * 128 + l * 8, l < 5, [&]() AINL {});
      issue_h(4); issue_h(5); issue_c(5); issue_h(6); issue_c(6);
      sfor<4, 20>([&](auto ic) AINL {
        constexpr int s = ic.v;
        constexpr int gs[23] = {0, 0, 0, 0, 4, 8, 8, 8, 8, 8, 8, 8, 8, 8, 8, 8, 8, 8, 8, 8, 0, 0, 0};
        waitbar<gs[s + 1] + gs[s + 2]>();
        if constexpr (s + 3 < 20) { issue_h(s + 3); issue_c(s + 3); }
      });
    } else {
      const int p = a * 128 + w * 16 + li;
      const u32 plr = bld16(p * 2, mk_rsrc(lam_re, 1280)), pli = bld16(p * 2, mk_rsrc(lam_im, 1280)), pdt = bld16(p * 2, mk_rsrc(log_dt, 1280));
      row_stats();
      put_tag(TAG);
      float lbr, lbi, kr, ki;
      {
        const float Lr = bflo(plr), Li = bflo(pli);
        const float dt = bflo(pk2(expf(bflo(pdt)), 0.f));
        const float ea = expf(dt * Lr);
        float sn, cn; sincosf(dt * Li, &sn, &cn);
        lbr = ea * cn; lbi = ea * sn;
        const float inv = 2.0f / (Lr * Lr + Li * Li);
        kr = ((lbr - 1.0f) * Lr + lbi * Li) * inv;
        ki = (lbi * Lr - (lbr - 1.0f) * Li) * inv;
        asm volatile("" : "+v"(lbr), "+v"(lbi), "+v"(kr), "+v"(ki));
      }
      f32x4 are[8], aim[8];
      u32 wdum = 0;
      if (WARM && w >= 4) touch(wdum, l < 40 ? ((m * 5 + a) * 80 + (w & 1) * 40 + l) * 128 : OOB, mk_rsrc(w >= 6 ? c_im : c_re, 640 * 1280));
#pragma unroll
      for (int i = 0; i < 8; ++i) { are[i] = zero4; aim[i] = zero4; }
      {
        struct FW { bf16x8 br, bi; };
        struct FX { bf16x8 x[4]; };
        FW W0, W1; FX Xa, Xb;
        const int pc = a * 128 + m * 8 + w;
        u32x4 t0, t1; u32 u0, u1;
        auto half = [&](auto mf, auto rw, auto hh, FW& RW, LAS char* pw, FX& RX, LAS char* px, const FW& MW, const FX& MX) AINL {
          constexpr int h = hh.v;
          auto mm = [&](int i) AINL {
            if constexpr (mf.v) { if (i & 1) aim[4 * h + (i >> 1)] = mfma(MX.x[i >> 1], MW.bi, aim[4 * h + (i >> 1)]); else are[4 * h + (i >> 1)] = mfma(MX.x[i >> 1], MW.br, are[4 * h + (i >> 1)]); }
            SB();
          };
          auto rx_ = [&](int i) AINL { RX.x[i] = lds128(px + i * 2048); SB(); };
          if constexpr (rw.v) {
            mm(0); RW.br = lds128(pw); SB(); mm(1); rx_(0); mm(2); RW.bi = lds128(pw + 16384); SB();
            mm(3); rx_(1); mm(4); rx_(2); mm(5); rx_(3); mm(6); mm(7);
          } else {
            mm(0); rx_(0); mm(1); rx_(1); mm(2); rx_(2); mm(3); rx_(3); mm(4); mm(5); mm(6); mm(7);
          }
        };
        STAMP(1);
        sfor<0, 10>([&](auto ic) AINL {
          constexpr int s = ic.v;
          LAS char* const x0 = pin(sm + rb0 + (s % 3) * 49152);
          LAS char* const x1 = pin(sm + (rb0 ^ 64) + (s % 3) * 49152);
          LAS char* const w0 = pin(x0 + w * 2048 + 16384);
          LAS char* const w1_ = pin(x1 + w * 2048 + 16384);
          bar();
          if constexpr (s == 0) STAMP(16);
          half(IC<(s > 0)>{}, IC<1>{}, IC<1>{}, W0, w0, Xa, x0, W1, Xb);
          half(IC<1>{}, IC<0>{}, IC<0>{}, W0, w0, Xb, x0 + 8192, W0, Xa);
          half(IC<1>{}, IC<1>{}, IC<1>{}, W1, w1_, Xa, x1, W0, Xb);
          half(IC<1>{}, IC<0>{}, IC<0>{}, W1, w1_, Xb, x1 + 8192, W1, Xa);
          if constexpr (s == RSS) {
            const rsrc_t rbr = mk_rsrc(b_re, 640 * 1280), rbi = mk_rsrc(b_im, 640 * 1280);
            t0 = bld128(l * 16, rbr, pc * 1280); t1 = bld128(l * 16, rbi, pc * 1280);
            u0 = bld32(1024 + l * 4, rbr, pc * 1280); u1 = bld32(1024 + l * 4, rbi, pc * 1280);
          }
          if constexpr (s == RSS + 2) {
            auto sum10 = [&](u32x4 t, u32 u) AINL {
              return wavesum(((bflo(t.x) + bfhi(t.x)) + (bflo(t.y) + bfhi(t.y))) + ((bflo(t.z) + bfhi(t.z)) + (bflo(t.w) + bfhi(t.w))) + (bflo(u) + bfhi(u)));
            };
            st128_sc1((u32x4){asu(sum10(t0, u0)), ta, asu(sum10(t1, u1)), tb}, l == 0 ? WS_RS + pc * 16 : OOB, rws);
          }
        });
#pragma unroll
        for (int i = 0; i < 4; ++i) { are[4 + i] = mfma(Xb.x[i], W1.br, are[4 + i]); aim[4 + i] = mfma(Xb.x[i], W1.bi, aim[4 + i]); }
      }
      bar();
      STAMP(2);
      if (w < 2) {
        u32x4 g0 = *(LAS u32x4*)(sm + MISC + tid * 16);
        if (!__all(g0.y == ta && g0.w == tb)) g0 = poll_granule(WS_XS + (m * 128 + tid) * 16, true, rws, ta, tb);
        const float rs = asf(g0.z);
        *(LAS float*)(sm + CST + (tid >> 5) * 144 + (tid & 31) * 4) = rs;
        *(LAS float*)(sm + CST + 576 + (tid >> 5) * 144 + (tid & 31) * 4) = -rs * asf(g0.x);
      }
      u32x4 rg = *(LAS u32x4*)(sm + MISC + 2048 + (w * 16 + li) * 16);
      if (!__all(rg.y == ta && rg.w == tb)) rg = poll_granule(WS_RS + p * 16, true, rws, ta, tb);
      const float sre = asf(rg.x), sim = asf(rg.z);
      asm volatile("s_waitcnt vmcnt(0) lgkmcnt(0)\n\ts_barrier" : "+v"(wdum)::"memory");
      STAMP(21);
      LAS char* const cst = pin(sm + CST + g * 144);
#pragma unroll
      for (int mt = 0; mt < 8; ++mt) {
        const f32x4 rv = *(LAS f32x4*)(cst + mt * 16), nv = *(LAS f32x4*)(cst + 576 + mt * 16);
        are[mt] = rv * are[mt] + nv * sre;
        aim[mt] = rv * aim[mt] + nv * sim;
      }
      float hr = 0.f, hi = 0.f;
#pragma unroll
      for (int mt = 0; mt < 8; ++mt)
#pragma unroll
        for (int e = 0; e < 4; ++e) {
          const float nr = __builtin_fmaf(-lbi, hi, __builtin_fmaf(lbr, hr, are[mt][e]));
          const float ni = __builtin_fmaf(lbi, hr, __builtin_fmaf(lbr, hi, aim[mt][e]));
          hr = nr; hi = ni;
        }
      SB(); STAMP(23); SB();
      float er[4], ei[4];
      gather4(hr, er); gather4(hi, ei);
      float p32r = lbr, p32i = lbi;
#pragma unroll
      for (int i = 0; i < 5; ++i) { const float t = p32r * p32r - p32i * p32i; p32i = 2.0f * p32r * p32i; p32r = t; }
      float cr[5], ci[5];
      cr[0] = 0.f; ci[0] = 0.f;
#pragma unroll
      for (int q = 0; q < 4; ++q) {
        cr[q + 1] = __builtin_fmaf(-p32i, ci[q], __builtin_fmaf(p32r, cr[q], er[q]));
        ci[q + 1] = __builtin_fmaf(p32i, cr[q], __builtin_fmaf(p32r, ci[q], ei[q]));
      }
      st128_sc1((u32x4){asu(cr[4]), ta, asu(ci[4]), tb}, l < 16 ? WS_EG + (m * 640 + p) * 16 : OOB, rws);
      SB();
      STAMP(3);
      const float p64r = p32r * p32r - p32i * p32i, p64i = 2.0f * p32r * p32i;
      const float Pr = p64r * p64r - p64i * p64i, Pi = 2.0f * p64r * p64i;
#pragma unroll
      for (int mt = 0; mt < 8; ++mt) {
#pragma unroll
        for (int e = 0; e < 4; ++e) {
          const float ur = are[mt][e], ui = aim[mt][e];
          are[mt][e] = __builtin_fmaf(kr, ur, -ki * ui);
          aim[mt][e] = __builtin_fmaf(-kr, ui, -ki * ur);
        }
        are[mt] = keep(are[mt]); aim[mt] = keep(aim[mt]);
      }
      SB(); STAMP(24); SB();
      float Hr = 0.f, Hi = 0.f;
      if (m > 0) {
        u32x4 e0, e1, e2, e3;
        const int m0 = m - 1 - 4 * g;
        const u32 o0 = WS_EG + (m0 * 640 + p) * 16;
        const bool v0 = m0 >= 0, v1 = m0 >= 1, v2 = m0 >= 2, v3 = m0 >= 3;
        _Pragma("nounroll") for (int it = 0; it < SPIN_CAP; ++it) {
          asm volatile("buffer_load_dwordx4 %0, %4, %8, 0 offen sc1\n\tbuffer_load_dwordx4 %1, %5, %8, 0 offen sc1\n\t"
                       "buffer_load_dwordx4 %2, %6, %8, 0 offen sc1\n\tbuffer_load_dwordx4 %3, %7, %8, 0 offen sc1\n\ts_waitcnt vmcnt(0)"
                       : "=&v"(e0), "=&v"(e1), "=&v"(e2), "=&v"(e3)
                       : "v"(v0 ? o0 : OOB), "v"(v1 ? o0 - 10240 : OOB), "v"(v2 ? o0 - 20480 : OOB), "v"(v3 ? o0 - 30720 : OOB), "s"(rws) : "memory");
          const bool ok = (!v0 || (e0.y == ta && e0.w == tb)) && (!v1 || (e1.y == ta && e1.w == tb)) &&
                          (!v2 || (e2.y == ta && e2.w == tb)) && (!v3 || (e3.y == ta && e3.w == tb));
          if (__all(ok)) break;
          __builtin_amdgcn_s_sleep(1);
        }
        float qr = asf(e3.x), qi = asf(e3.z), t;
        t = __builtin_fmaf(-Pi, qi, __builtin_fmaf(Pr, qr, asf(e2.x))); qi = __builtin_fmaf(Pi, qr, __builtin_fmaf(Pr, qi, asf(e2.z))); qr = t;
        t = __builtin_fmaf(-Pi, qi, __builtin_fmaf(Pr, qr, asf(e1.x))); qi = __builtin_fmaf(Pi, qr, __builtin_fmaf(Pr, qi, asf(e1.z))); qr = t;
        t = __builtin_fmaf(-Pi, qi, __builtin_fmaf(Pr, qr, asf(e0.x))); qi = __builtin_fmaf(Pi, qr, __builtin_fmaf(Pr, qi, asf(e0.z))); qr = t;
        float gr[4], gi[4];
        gather4(qr, gr); gather4(qi, gi);
        const float P2r = Pr * Pr - Pi * Pi, P2i = 2.0f * Pr * Pi;
        const float P4r = P2r * P2r - P2i * P2i, P4i = 2.0f * P2r * P2i;
        Hr = gr[3]; Hi = gi[3];
#pragma unroll
        for (int q = 2; q >= 0; --q) {
          t = __builtin_fmaf(-P4i, Hi, __builtin_fmaf(P4r, Hr, gr[q])); Hi = __builtin_fmaf(P4i, Hr, __builtin_fmaf(P4r, Hi, gi[q])); Hr = t;
        }
      }
      STAMP(4);
      {
        const float p96r = p64r * p32r - p64i * p32i, p96i = p64r * p32i + p64i * p32r;
        const float fr = g == 0 ? 1.f : g == 1 ? p32r : g == 2 ? p64r : p96r;
        const float fi = g == 0 ? 0.f : g == 1 ? p32i : g == 2 ? p64i : p96i;
        const float c0r = g == 0 ? cr[0] : g == 1 ? cr[1] : g == 2 ? cr[2] : cr[3];
        const float c0i = g == 0 ? ci[0] : g == 1 ? ci[1] : g == 2 ? ci[2] : ci[3];
        const float sr = __builtin_fmaf(-fi, Hi, __builtin_fmaf(fr, Hr, c0r)), si = __builtin_fmaf(fi, Hr, __builtin_fmaf(fr, Hi, c0i));
        hr = __builtin_fmaf(kr, sr, -ki * si);
        hi = __builtin_fmaf(-kr, si, -ki * sr);
      }
      {
        LAS char* const tb_ = sm + (w < 6 ? w * 8192 : AS3 + (w - 6) * 8192);
        u32x4 hv[2][2][2];
        const int swc = ((li & 3) << 1) | ((li >> 2) & 1) | ((li >> 3) << 3);
        const int q4 = li >> 2, p4 = l & 3;
        const int c0 = 8 * (g & 1) + q4, c1 = c0 + 4;
        const int sw0 = ((c0 & 3) << 1) | ((c0 >> 3) << 3), sw1 = sw0 | 1;
        const u32 ob = WS_HH + (m * 20 + 2 * a + (w >> 2)) * 16384 + ((w >> 1) & 1) * 1024 + ((2 * (w & 1) + (g & 1)) * 16 + li) * 16;
#pragma unroll
        for (int hf = 0; hf < 2; ++hf) {
#pragma unroll
          for (int mt = 4 * hf; mt < 4 * hf + 4; ++mt)
#pragma unroll
            for (int e = 0; e < 4; ++e) {
              const float nr = __builtin_fmaf(lbi, hi, __builtin_fmaf(lbr, hr, are[mt][e]));
              const float ni = __builtin_fmaf(-lbi, hr, __builtin_fmaf(lbr, hi, aim[mt][e]));
              hr = nr; hi = ni;
              are[mt][e] = hr; aim[mt][e] = hi;
            }
#pragma unroll
          for (int i = 2 * hf; i < 2 * hf + 2; ++i) {
            *(LAS u32x4*)(tb_ + li * 256 + (((4 * g + i) ^ swc) << 4)) = pack8(are[2 * i], are[2 * i + 1]);
            *(LAS u32x4*)(tb_ + 4096 + li * 256 + (((4 * g + i) ^ swc) << 4)) = pack8(aim[2 * i], aim[2 * i + 1]);
          }
          asm volatile("s_waitcnt lgkmcnt(0)" ::: "memory");
#pragma unroll
          for (int part = 0; part < 2; ++part)
#pragma unroll
            for (int u = 0; u < 2; ++u) {
              const int tt = 4 * u + 2 * (g >> 1) + hf, ch = 2 * tt + (p4 >> 1);
              const bf16x8 v = ldstr(tb_ + part * 4096 + c0 * 256 + ((ch ^ sw0) << 4) + 8 * (p4 & 1),
                                     tb_ + part * 4096 + c1 * 256 + ((ch ^ sw1) << 4) + 8 * (p4 & 1));
              hv[hf][part][u] = __builtin_bit_cast(u32x4, v);
              st128_sc1(hv[hf][part][u], ob + part * 163840 + tt * 2048, rws);
            }
          SB();
          STAMP(25 + hf);
        }
        bar();
        LAS char* const hd = sm + (w >> 2) * 16384 + (g >> 1) * 4096 + ((w >> 1) & 1) * 1024 + ((2 * (w & 1) + (g & 1)) * 16 + li) * 16;
        LAS char* const hd1 = hd + ((w >> 2) ? AS3 - 16384 : 32768);
#pragma unroll
        for (int hf = 0; hf < 2; ++hf)
#pragma unroll
          for (int u = 0; u < 2; ++u) {
            *(LAS u32x4*)(hd + u * 8192 + hf * 2048) = hv[hf][0][u];
            *(LAS u32x4*)(hd1 + u * 8192 + hf * 2048) = hv[hf][1][u];
          }
      }
      waitbar<0>();
      STAMP(5);
      f32x4 ac[4][2];
#pragma unroll
      for (int i = 0; i < 4; ++i) { ac[i][0] = zero4; ac[i][1] = zero4; }
      LAS char* const ab = pin(sm + tq * 4096 + l * 16);
      LAS char* const ab3 = pin(ab + AS3);
      LAS char* cb[2][2];
#pragma unroll
      for (int i = 0; i < 2; ++i) { cb[0][i] = pin(sm + 49152 + i * 49152 + cq * 8192 + rb0); cb[1][i] = pin(sm + 49152 + i * 49152 + cq * 8192 + (rb0 ^ 64)); }
      struct FC { bf16x8 c[4], b0, b1; };
      FC G0, G1;
      auto stepC = [&](auto mf, FC& R, int ks, int s, const FC& M) AINL {
        LAS char* const pa = ((s & 3) == 3 ? ab3 : ab + (s & 3) * 16384) + ks * 1024;
        LAS char* const pc = cb[ks][(s % 5) / 3] + ((s % 5) % 3) * 16384;
        auto mm = [&](int i) AINL { if constexpr (mf.v) ac[i >> 1][i & 1] = mfma(M.c[i >> 1], (i & 1) ? M.b1 : M.b0, ac[i >> 1][i & 1]); SB(); };
        auto rc = [&](int i) AINL { R.c[i] = lds128(pc + i * 2048); SB(); };
        mm(0); R.b0 = lds128(pa); SB(); mm(1); rc(0); mm(2); R.b1 = lds128(pa + 2048); SB(); mm(3); rc(1); mm(4); rc(2); mm(5); rc(3); mm(6); mm(7);
      };
      sfor<0, 20>([&](auto ic) AINL {
        constexpr int s = ic.v;
        if constexpr (s == 4) { bar(); STAMP(6); }
        if constexpr (s >= 4) bar();
        if constexpr (s == 4) STAMP(17);
        stepC(IC<(s > 0)>{}, G0, 0, s, G1);
        stepC(IC<1>{}, G1, 1, s, G0);
      });
#pragma unroll
      for (int ct = 0; ct < 4; ++ct) { ac[ct][0] = mfma(G1.c[ct], G1.b0, ac[ct][0]); ac[ct][1] = mfma(G1.c[ct], G1.b1, ac[ct][1]); }
      STAMP(7);
#pragma unroll
      for (int cb_ = 0; cb_ < 2; ++cb_)
#pragma unroll
        for (int tt = 0; tt < 2; ++tt)
          st128_sc1(pack8(ac[2 * cb_][tt], ac[2 * cb_ + 1][tt]), WS_SS + ((m * 10 + 2 * a + cq) * 8 + tq + 4 * cb_) * 2048 + tt * 1024 + l * 16, rws);
      waitbar<0>();
      if (w == 0) signal(l < 2 ? WS_IB + (m * 10 + 2 * a + l) * IB_B + 512 : OOB);
      STAMP(8);
    }
  } else if (w < 8) {
    row_stats();
    auto add8 = [&](float (&c)[8], u32x4 t) AINL {
      c[0] += bflo(t.x); c[1] += bfhi(t.x); c[2] += bflo(t.y); c[3] += bfhi(t.y); c[4] += bflo(t.z); c[5] += bfhi(t.z); c[6] += bflo(t.w); c[7] += bfhi(t.w);
    };
    STAMP(1);
    if (SDELAY) __builtin_amdgcn_s_sleep(SDELAY);
    if (m < 12) {
      const int c = m;
      const rsrc_t rw3 = mk_rsrc(w3, 640 * 3072);
      float cs[8] = {0.f, 0.f, 0.f, 0.f, 0.f, 0.f, 0.f, 0.f};
      const u32 vo = (tid >> 4) * 3072 + c * 256 + (tid & 15) * 16;
      u32x4 t[20];
#pragma unroll
      for (int i = 0; i < 20; ++i) t[i] = bld128(vo, rw3, i * 32 * 3072);
      SB();
#pragma unroll
      for (int i = 0; i < 20; ++i) add8(cs, t[i]);
#pragma unroll
      for (int e = 0; e < 8; ++e) cs[e] = sum32(sum16(cs[e]));
      if (l < 16) {
        *(LAS f32x4*)(sm + w * 512 + l * 32) = (f32x4){cs[0], cs[1], cs[2], cs[3]};
        *(LAS f32x4*)(sm + w * 512 + l * 32 + 16) = (f32x4){cs[4], cs[5], cs[6], cs[7]};
      }
      bar();
      if (w < 2) {
        float s = 0.f;
#pragma unroll
        for (int i = 0; i < 8; ++i) s += *(LAS float*)(sm + i * 512 + tid * 4);
        st32_sc1(asu(s), WS_CS3 + (c * 128 + tid) * 4, rws);
      }
      waitbar<0>();
      if (w == 0) signal(l < 16 ? WS_CF + c * 128 + l * 8 : OOB);
    }
    {
      const rsrc_t rw4 = mk_rsrc(w4, 1536 * 1280);
      u32 d = 0;
      if (m < 12) { touch(d, (m * 640 + tid) * 128, rw4); touch(d, tid < 128 ? (m * 640 + 512 + tid) * 128 : OOB, rw4); }
      else {
#pragma unroll
        for (int i = 0; i < 4; ++i) touch(d, i < 3 || tid < 384 ? (7680 + (m - 12) * 1920 + i * 512 + tid) * 128 : OOB, rw4);
      }
      asm volatile("s_waitcnt vmcnt(0)" : "+v"(d)::"memory");
    }
    STAMP(2);
    if (m == 0) {
#pragma unroll
      for (int i = 0; i < 4; ++i) (void)poll_granule(WS_XS + (i * 512 + tid) * 16, true, rws, ta, tb);
      bar();
      if (tid == 0) st64_sc1((u32x2){ta, tb}, WS_EPOCH, rws);
    }
  }
#endif
}

extern "C" void kernel_launch(void* const* d_in, const int* in_sizes, int n_in, void* d_out, int out_size,
                              void* d_ws, size_t ws_size, hipStream_t stream) {
  (void)in_sizes; (void)n_in; (void)out_size; (void)ws_size;
  s5_mixer_kernel<<<256, 768, 0, stream>>>(d_in[0], d_in[1], d_in[2], d_in[3], d_in[4], d_in[5], d_in[6], d_in[7],
                                           d_in[8], d_in[10], d_in[11], d_in[12], d_in[13], d_out, d_ws);
}
```

```cpp
#include <hip/hip_runtime.h>
#include <hip/hip_bf16.h>
#include <stdint.h>

#define DI __device__ __forceinline__
#define LAS __attribute__((address_space(3)))
#define AINL __attribute__((always_inline))
typedef unsigned u32;
typedef unsigned long long u64;
typedef u32 u32x4 __attribute__((ext_vector_type(4)));
typedef u32 u32x2 __attribute__((ext_vector_type(2)));
typedef float f32x4 __attribute__((ext_vector_type(4)));
typedef float f32x2 __attribute__((ext_vector_type(2)));
typedef __bf16 bf16x8 __attribute__((ext_vector_type(8)));
typedef __bf16 bf16x2 __attribute__((ext_vector_type(2)));
typedef short s16x4 __attribute__((ext_vector_type(4)));
typedef short s16x8 __attribute__((ext_vector_type(8)));

constexpr u32 WS_EPOCH = 0;
constexpr u32 WS_CF = 0x1000;
constexpr u32 WS_IB = 0x340000;
constexpr u32 IB_B = 640, IB_A = 160 * 640;
constexpr u32 WS_XS = 0x20000;
constexpr u32 WS_CS3 = 0x2A000;
constexpr u32 WS_RS = 0x58000;
constexpr u32 WS_EG = 0x30000;
constexpr u32 WS_ST1 = 0x60000;
constexpr u32 WS_SS = 0xC0000;
constexpr u32 WS_H1 = 0x400000;
constexpr u32 WS_X1 = 0x680000;
constexpr u32 WS_HH = 0x900000;
constexpr u32 WS_H2 = 0xE00000;
constexpr u32 WS_BYTES = 0x1400000;
constexpr u32 OOB = 0x7FFFFFF0u;
constexpr float LN_EPS = 1e-5f;
constexpr int SPIN_CAP = 1 << 17;

template <int I> struct IC { static constexpr int v = I; };
template <int B, int E, class F> DI void sfor(F&& f) {
  if constexpr (B < E) { f(IC<B>{}); sfor<B + 1, E>(f); }
}

#if defined(__HIP_DEVICE_COMPILE__) && __HIP_DEVICE_COMPILE__
typedef __amdgpu_buffer_rsrc_t rsrc_t;
DI rsrc_t mk_rsrc(const void* p, u32 bytes) { return __builtin_amdgcn_make_buffer_rsrc((void*)p, 0, bytes, 0x00020000); }

template <int SC1> DI void dma1(u32 lds, u32 voff, rsrc_t r, u32 soff) {
  if constexpr (SC1)
    asm volatile("s_mov_b32 m0, %0\n\ts_nop 0\n\tbuffer_load_dwordx4 %1, %2, %3 offen sc1 lds" ::"s"(lds), "v"(voff), "s"(r), "s"(soff) : "memory");
  else
    asm volatile("s_mov_b32 m0, %0\n\ts_nop 0\n\tbuffer_load_dwordx4 %1, %2, %3 offen lds" ::"s"(lds), "v"(voff), "s"(r), "s"(soff) : "memory");
}
template <int SC1> DI void dma2(u32 lds, u32 v0, u32 v1, rsrc_t r, u32 soff) {
  if constexpr (SC1)
    asm volatile("s_mov_b32 m0, %0\n\ts_nop 0\n\tbuffer_load_dwordx4 %1, %3, %4 offen sc1 lds\n\tbuffer_load_dwordx4 %2, %3, %4 offen offset:1024 sc1 lds"
                 ::"s"(lds), "v"(v0), "v"(v1), "s"(r), "s"(soff) : "memory");
  else
    asm volatile("s_mov_b32 m0, %0\n\ts_nop 0\n\tbuffer_load_dwordx4 %1, %3, %4 offen lds\n\tbuffer_load_dwordx4 %2, %3, %4 offen offset:1024 lds"
                 ::"s"(lds), "v"(v0), "v"(v1), "s"(r), "s"(soff) : "memory");
}
template <int SC1> DI void dma4(u32 lds, u32 v, rsrc_t r, u32 soff) {
  if constexpr (SC1)
    asm volatile("s_mov_b32 m0, %0\n\ts_nop 0\n\tbuffer_load_dwordx4 %1, %2, %3 offen sc1 lds\n\tbuffer_load_dwordx4 %1, %2, %3 offen offset:1024 sc1 lds\n\t"
                 "buffer_load_dwordx4 %1, %2, %3 offen offset:2048 sc1 lds\n\tbuffer_load_dwordx4 %1, %2, %3 offen offset:3072 sc1 lds"
                 ::"s"(lds), "v"(v), "s"(r), "s"(soff) : "memory");
  else
    asm volatile("s_mov_b32 m0, %0\n\ts_nop 0\n\tbuffer_load_dwordx4 %1, %2, %3 offen lds\n\tbuffer_load_dwordx4 %1, %2, %3 offen offset:1024 lds\n\t"
                 "buffer_load_dwordx4 %1, %2, %3 offen offset:2048 lds\n\tbuffer_load_dwordx4 %1, %2, %3 offen offset:3072 lds"
                 ::"s"(lds), "v"(v), "s"(r), "s"(soff) : "memory");
}
DI void bar() { asm volatile("s_waitcnt lgkmcnt(0)\n\ts_barrier" ::: "memory"); }
template <int N> DI void waitbar() { asm volatile("s_waitcnt vmcnt(%0) lgkmcnt(0)\n\ts_barrier" ::"i"(N) : "memory"); }

DI void touch(u32& d, u32 voff, rsrc_t r) { asm volatile("buffer_load_dword %0, %1, %2, 0 offen" : "+v"(d) : "v"(voff), "s"(r) : "memory"); }

DI u32x4 bld128(u32 voff, rsrc_t r, u32 soff) { return __builtin_amdgcn_raw_buffer_load_b128(r, voff, soff, 0); }
DI u32x2 bld64(u32 voff, rsrc_t r, u32 soff) { return __builtin_amdgcn_raw_buffer_load_b64(r, voff, soff, 0); }
DI u32x2 bld64_sc1(u32 voff, rsrc_t r, u32 soff) { return __builtin_amdgcn_raw_buffer_load_b64(r, voff, soff, 16); }
DI u32 bld32(u32 voff, rsrc_t r, u32 soff) { return __builtin_amdgcn_raw_buffer_load_b32(r, voff, soff, 0); }
DI u32 bld16(u32 voff, rsrc_t r) { return __builtin_amdgcn_raw_buffer_load_b16(r, voff, 0, 0); }
DI void st128_sc1(u32x4 v, u32 voff, rsrc_t r) { __builtin_amdgcn_raw_buffer_store_b128(v, r, voff, 0, 16); }
DI void st64_sc1(u32x2 v, u32 voff, rsrc_t r) { __builtin_amdgcn_raw_buffer_store_b64(v, r, voff, 0, 16); }
DI void st32_sc1(u32 v, u32 voff, rsrc_t r) { __builtin_amdgcn_raw_buffer_store_b32(v, r, voff, 0, 16); }

DI void probe(u32x2& v, u32 voff, rsrc_t ws) { asm volatile("buffer_load_dwordx2 %0, %1, %2, 0 offen sc1" : "=v"(v) : "v"(voff), "s"(ws) : "memory"); }
template <int N> DI void waitbar_pin(u32x2& v) { asm volatile("s_waitcnt vmcnt(%1) lgkmcnt(0)\n\ts_barrier" : "+v"(v) : "i"(N) : "memory"); }
DI void poll_flags(u32 voff, bool active, rsrc_t ws, u32 ta, u32 tb) {
  _Pragma("nounroll") for (int it = 0; it < SPIN_CAP; ++it) {
    u32x2 v;
    asm volatile("buffer_load_dwordx2 %0, %1, %2, 0 offen sc1\n\ts_waitcnt vmcnt(0)" : "=&v"(v) : "v"(voff), "s"(ws) : "memory");
    const bool ok = !active || (v.x == ta && v.y == tb);
    if (__all(ok)) break;
    __builtin_amdgcn_s_sleep(1);
  }
}
DI u32x4 poll_granule(u32 voff, bool active, rsrc_t ws, u32 ta, u32 tb) {
  u32x4 v;
  _Pragma("nounroll") for (int it = 0; it < SPIN_CAP; ++it) {
    asm volatile("buffer_load_dwordx4 %0, %1, %2, 0 offen sc1\n\ts_waitcnt vmcnt(0)" : "=&v"(v) : "v"(voff), "s"(ws) : "memory");
    const bool ok = !active || (v.y == ta && v.w == tb);
    if (__all(ok)) break;
    __builtin_amdgcn_s_sleep(1);
  }
  return v;
}

DI float asf(u32 x) { return __uint_as_float(x); }
DI u32 asu(float x) { return __float_as_uint(x); }
DI float bflo(u32 x) { return asf(x << 16); }
DI float bfhi(u32 x) { return asf(x & 0xffff0000u); }
DI u32 pk2(float a, float b) { f32x2 p = {a, b}; bf16x2 q = __builtin_convertvector(p, bf16x2); return __builtin_bit_cast(u32, q); }
DI float sum16(float x) { const u32 u = asu(x); auto r = __builtin_amdgcn_permlane16_swap(u, u, false, false); const u32 a = r[0], b = r[1]; return asf(a) + asf(b); }
DI float sum32(float x) { const u32 u = asu(x); auto r = __builtin_amdgcn_permlane32_swap(u, u, false, false); const u32 a = r[0], b = r[1]; return asf(a) + asf(b); }
template <int C> DI float dppadd(float x) { return x + asf(__builtin_amdgcn_update_dpp(0u, asu(x), C, 0xf, 0xf, true)); }
DI float wavesum(float x) { x = dppadd<0xB1>(x); x = dppadd<0x4E>(x); x = dppadd<0x141>(x); x = dppadd<0x140>(x); return sum32(sum16(x)); }
DI void gather4(float x, float (&e)[4]) {
  const u32 u = asu(x);
  auto p = __builtin_amdgcn_permlane16_swap(u, u, false, false);
  const u32 a = p[0], b = p[1];
  auto pa = __builtin_amdgcn_permlane32_swap(a, a, false, false);
  auto pb = __builtin_amdgcn_permlane32_swap(b, b, false, false);
  const u32 e0 = pa[0], e2 = pa[1], e1 = pb[0], e3 = pb[1];
  e[0] = asf(e0); e[1] = asf(e1); e[2] = asf(e2); e[3] = asf(e3);
}
typedef f32x4 GeluK;
DI GeluK gelu_k(float r, float ri) { return (f32x4){-2.302208198f * r, -0.1029432397f * r * r * r, ri, 0.f}; }
DI u32 gelu2(f32x2 t, GeluK k) {
  const f32x2 u = t * __builtin_elementwise_fma(t * t, (f32x2){k.y, k.y}, (f32x2){k.x, k.x});
  const f32x2 e = {__builtin_amdgcn_exp2f(u.x), __builtin_amdgcn_exp2f(u.y)};
  const f32x2 d = __builtin_elementwise_fma(e, (f32x2){k.z, k.z}, (f32x2){k.z, k.z});
  const f32x2 o = t * (f32x2){__builtin_amdgcn_rcpf(d.x), __builtin_amdgcn_rcpf(d.y)};
  return __builtin_bit_cast(u32, __builtin_convertvector(o, bf16x2));
}
DI u32x2 gelu4(f32x4 acc, f32x4 cs, float mu, GeluK k) {
  const f32x2 nm = {-mu, -mu};
  return (u32x2){gelu2(__builtin_elementwise_fma(nm, cs.xy, acc.xy), k), gelu2(__builtin_elementwise_fma(nm, cs.zw, acc.zw), k)};
}
DI f32x4 mfma(bf16x8 a, bf16x8 b, f32x4 c) { return __builtin_amdgcn_mfma_f32_16x16x32_bf16(a, b, c, 0, 0, 0); }
DI bf16x8 lds128(LAS char* p) { return *(LAS bf16x8*)p; }
DI bf16x8 ldstr(LAS char* p0, LAS char* p1) {
  const s16x4 a = __builtin_amdgcn_ds_read_tr16_b64_v4i16((LAS s16x4*)p0);
  const s16x4 b = __builtin_amdgcn_ds_read_tr16_b64_v4i16((LAS s16x4*)p1);
  const s16x8 w = __builtin_shufflevector(a, b, 0, 1, 2, 3, 4, 5, 6, 7);
  return __builtin_bit_cast(bf16x8, w);
}
DI u32x4 pack8(f32x4 a, f32x4 b) { u32x4 r; r.x = pk2(a.x, a.y); r.y = pk2(a.z, a.w); r.z = pk2(b.x, b.y); r.w = pk2(b.z, b.w); return r; }
DI f32x4 unpack4(u32 a, u32 b) { return (f32x4){bflo(a), bfhi(a), bflo(b), bfhi(b)}; }
DI int ssel_lt(int a, int b, int x, int y) { int r; asm("s_cmp_lt_i32 %1, %2\n\ts_cselect_b32 %0, %3, %4" : "=s"(r) : "s"(a), "s"(b), "s"(x), "s"(y) : "scc"); return r; }
DI int wrapk(int k, int n) { return k >= n ? k - n : k; }
#define SB() __builtin_amdgcn_sched_barrier(0)
#define MFMA_DONE() do { SB(); asm volatile("s_nop 11"); SB(); } while (0)
DI f32x4 keep(f32x4 v) { asm volatile("" : "+v"(v)); return v; }
DI void hold(u32& v) { asm volatile("" : "+v"(v)); }
DI LAS char* pin(LAS char* p) { asm volatile("" : "+v"(p)); return p; }
#endif

__global__ __launch_bounds__(768, 3) void s5_mixer_kernel(
    const void* __restrict__ x, const void* __restrict__ lam_re, const void* __restrict__ lam_im,
    const void* __restrict__ b_re, const void* __restrict__ b_im, const void* __restrict__ c_re,
    const void* __restrict__ c_im, const void* __restrict__ log_dt, const void* __restrict__ dskip,
    const void* __restrict__ w1, const void* __restrict__ w2, const void* __restrict__ w3,
    const void* __restrict__ w4, void* __restrict__ out, void* __restrict__ ws) {
#if defined(__HIP_DEVICE_COMPILE__) && __HIP_DEVICE_COMPILE__
  __shared__ __attribute__((aligned(16))) char smem_[163840];
  LAS char* const sm = (LAS char*)smem_;
  const u32 L0 = (u32)(size_t)sm;
  const int tid = threadIdx.x, l = tid & 63;
  const int w = __builtin_amdgcn_readfirstlane(tid >> 6);
  const int bid = blockIdx.x, xcd = bid & 7, rr = bid >> 3;
  const int m = (xcd >> 1) * 4 + (rr < 20 ? rr / 5 : (rr - 20) / 3);
  const int role = rr < 20 ? rr % 5 + 5 * (xcd & 1) : 10 + (rr - 20) % 3 + 3 * (xcd & 1);
  const int g = l >> 4, li = l & 15, r8 = l >> 3, tq = w & 3, cq = w >> 2, L = w - 8;
  const rsrc_t rws = mk_rsrc(ws, WS_BYTES);
  const rsrc_t rx = mk_rsrc(x, 2048 * 1280);
  const int fm = 5 * (m & 1) + (m >> 1);
  const int rot1 = (role + fm) % 10, rotA1 = (2 * role + fm) % 10;

  u32 ta, tb;
  const int stok = m * 128 + role * 8 + w;
  u32x2 ep; u32x4 ra; u32 rb_;
  if (w < 8) { ep = bld64_sc1(0, rws, 0); ra = bld128(l * 16, rx, stok * 1280); rb_ = bld32(1024 + l * 4, rx, stok * 1280); }
  asm volatile("" ::: "memory");
  auto row_stats = [&]() AINL {
    const u64 e = (((u64)__builtin_amdgcn_readfirstlane(ep.y) << 32) | (u32)__builtin_amdgcn_readfirstlane(ep.x)) + 0x9E3779B97F4A7C15ull;
    ta = (u32)e; tb = (u32)(e >> 32);
    float v[10] = {bflo(ra.x), bfhi(ra.x), bflo(ra.y), bfhi(ra.y), bflo(ra.z), bfhi(ra.z), bflo(ra.w), bfhi(ra.w), bflo(rb_), bfhi(rb_)};
    float s = 0.f;
#pragma unroll
    for (int i = 0; i < 10; ++i) s += v[i];
    const float mu = wavesum(s) * (1.0f / 640.0f);
    float q = 0.f;
#pragma unroll
    for (int i = 0; i < 10; ++i) { const float d = v[i] - mu; q = __builtin_fmaf(d, d, q); }
    const float rs = 1.0f / sqrtf(wavesum(q) * (1.0f / 640.0f) + LN_EPS);
    u32x4 gr = {asu(mu), ta, asu(rs), tb};
    st128_sc1(gr, l == 0 ? WS_XS + stok * 16 : OOB, rws);
  };
  auto put_tag = [&](u32 off) AINL { if (tid == 0) *(LAS u32x2*)(sm + off) = (u32x2){ta, tb}; };
  auto get_tag = [&](u32 off) AINL {
    const u32x2 t = *(volatile LAS u32x2*)(sm + off);
    ta = __builtin_amdgcn_readfirstlane(t.x); tb = __builtin_amdgcn_readfirstlane(t.y);
  };
  auto signal = [&](u32 off) AINL { st64_sc1((u32x2){ta, tb}, off, rws); };
  auto await = [&](auto nv, auto dly, u32 sig, u32 src, bool act, auto&& pre) AINL {
    waitbar<nv.v>();
    if (L == 0) { __builtin_amdgcn_s_sleep(dly.v); poll_flags(src, act, rws, ta, tb); }
    else { if (L == 1) signal(sig); pre(); }
    bar();
  };
  const u32 rb0 = li * 128 + ((g ^ (li >> 1)) << 4);
  const f32x4 zero4 = {0.f, 0.f, 0.f, 0.f};

  if (__builtin_expect(role < 10, 1)) {
    const int j = role;
    constexpr u32 ACT = 0, W = 65536;
    const int b0 = j < 9 ? 5 * j : 44, nb = j < 8 ? 5 : 4, kt0 = (b0 + 1) >> 1;
    if (__builtin_expect(w >= 8, 1)) {
      const rsrc_t rw1 = mk_rsrc(w1, 640 * 1280), rw2 = mk_rsrc(w2, 640 * 1280);
      const rsrc_t rw3 = mk_rsrc(w3, 640 * 3072), rw4 = mk_rsrc(w4, 1536 * 1280);
      const u32 vx0 = (m * 128 + L * 32 + r8) * 1280 + (((l & 7) ^ g) << 4);
      const u32 vx1 = (m * 128 + L * 32 + 8 + r8) * 1280 + (((l & 7) ^ (4 + g)) << 4) - 1024;
      const u32 wch = j * 128 + (((l & 7) ^ (2 * g)) << 4);
      const u32 vq0 = ((L >> 1) * 32 + (r8 >> 2) * 8 + (L & 1) * 4 + (r8 & 3)) * 1280 + wch, vq1 = vq0 + 16 * 1280 - 1024;
      const u32 ldsA = L0 + ACT + L * 4096, ldsW = L0 + W + L * 2048;
      auto issue_1 = [&](int s) AINL {
        const int kt = wrapk(s + rot1, 10);
        dma2<0>(ldsA + (s & 3) * 16384, vx0, vx1, rx, kt * 128);
        dma2<0>(ldsW + 65536 + (s & 3) * 8192, vq0, vq1, rw1, kt * 81920);
        dma2<0>(ldsA + (s & 3) * 16384 + 2048, vx0, vx1, rx, kt * 128 + 20480);
      };
      issue_1(0); issue_1(1); issue_1(2);
      const u32 vw0 = (L * 16 + r8) * 1280 + wch, vw1 = vw0 + 10240 - 1024;
      const u32 va = L * 4096 + l * 16, vl = L * 1024 + l * 16;
      auto k3 = [&](int s) AINL { return s == 0 ? j : ssel_lt(s - 1, j, s - 1, s); };
      auto k4 = [&](int s) AINL { return ssel_lt(s, 2, kt0 + s, ssel_lt(s - 2, kt0, s - 2, s)); };
      u32 v3[5];
#pragma unroll
      for (int i = 0; i < 5; ++i) {
        const int c = 64 * i + l, r = c / 20, pos = c - 20 * r;
        v3[i] = j == 9 && pos >= 16 ? OOB : (L * 16 + r) * 3072 + b0 * 64 + ((pos ^ (2 * ((r >> 2) & 1))) << 4) - 1024 * (i & 1);
      }
      auto issue_w2 = [&](int s) AINL { dma2<0>(ldsW + 16384 + s * 8192, vw0, vw1, rw2, s * 81920); };
      auto issue_w3 = [&](int s) AINL {
        const u32 d = L0 + W + (s & 3) * 20480 + L * 5120, so = k3(s) * 196608;
        dma2<0>(d, v3[0], v3[1], rw3, so); dma2<0>(d + 2048, v3[2], v3[3], rw3, so); dma1<0>(d + 4096, v3[4], rw3, so);
      };
      const u32 vz0 = r8 * 1280 + wch, vz1 = vz0 + 10240 - 1024;
      auto whole_w = [&](u32 d, rsrc_t r, u32 so) AINL {
#pragma unroll
        for (int i = 0; i < 4; ++i) dma2<0>(d + i * 2048, vz0, vz1, r, so + i * 20480);
      };
      auto issue_w4 = [&](int s) AINL { dma2<0>(ldsW + wrapk(s, 12) * 8192, vw0, vw1, rw4, k4(s) * 81920); };
      auto issue_a = [&](int s, u32 base) AINL { dma4<1>(ldsA + (s & 3) * 16384, va, rws, base); };
      auto issue_h1 = [&](int s) AINL {
#pragma unroll
        for (int ks = 0; ks < 2; ++ks) {
          const int b = 2 * s + ks, grp = b / 5;
          const int t = m * 128 + l - ((grp & 1) ? -(1 + (grp >> 1)) : (1 + (grp >> 1)));
          dma2<1>(L0 + ACT + (s & 3) * 16384 + ks * 8192 + L * 2048, t >= 0 ? t * 16 + 32 : OOB, t < 2048 - 64 ? t * 16 + 32 : OOB, rws, WS_H1 + (b * 4 + L) * 32768 - 32);
        }
      };
      const bf16x8 ones = __builtin_bit_cast(bf16x8, (u32x4){0x3F803F80u, 0x3F803F80u, 0x3F803F80u, 0x3F803F80u});
      LAS char* const wl = pin(sm + W + 65536 + (4 * g + (li >> 2)) * 128 + ((L ^ ((2 * g + (li >> 3)) & 3)) << 5) + 8 * (l & 3));
      f32x4 cs = zero4;
      sfor<0, 10>([&](auto ic) AINL {
        constexpr int s = ic.v;
        constexpr int gs[13] = {6, 6, 6, 6, 6, 6, 6, 6, 6, 6, 3, 2, 0};
        waitbar<(s == 9 ? 4 : gs[s + 1] + gs[s + 2])>();
        if constexpr (s + 3 < 10) issue_1(s + 3);
        if constexpr (s == 5) get_tag(W);
        const bf16x8 c0 = ldstr(wl + (s & 3) * 8192, wl + (s & 3) * 8192 + 2048), c1 = ldstr(wl + (s & 3) * 8192 + 4096, wl + (s & 3) * 8192 + 6144);
        cs = keep(mfma(c1, ones, mfma(c0, ones, cs)));
        if constexpr (s == 7) { dma1<1>(L0 + ACT + 2 * 16384 + L * 1024, vl, rws, WS_XS + m * 2048); issue_w2(0); }
        if constexpr (s == 8) issue_w2(1);
      });
      MFMA_DONE();
      if (li == 0) *(LAS f32x4*)(sm + ACT + 2 * 16384 + 4096 + L * 64 + g * 16) = cs;
      bar();
      issue_w2(2); issue_w2(3); issue_w2(4); issue_w2(5); issue_w2(6);
      const u32 ib = WS_IB + (m * 10 + j) * IB_B, ibt = WS_IB + (m * 10 + l) * IB_B;
      const int j5 = j % 5;
      await(IC<63>{}, IC<13>{},
            l < 10 ? ibt + j * 8 : l < 20 ? (j5 < 3 && m < 15 ? ibt + (10 + j5 + 3 * (j / 5)) * 8 : OOB)
                   : (l < 30 && j5 >= 2 && m > 0 ? ibt - 30 * IB_B + (14 + j5 + 3 * (j / 5)) * 8 : OOB),
            ib + l * 8, l < 10 || (l < 16 && m > 0) || (l >= 16 && l < 22 && m < 15),
            [&]() AINL { whole_w(L0 + W + 16384 + (6 + L) * 8192, rw2, (6 + L) * 81920); });
      issue_h1(0); issue_h1(1); issue_h1(2);
      sfor<0, 10>([&](auto ic) AINL {
        constexpr int s = ic.v;
        constexpr int gs[13] = {4, 4, 4, 4, 4, 4, 4, 4, 4, 4, 5, 5, 5};
        waitbar<gs[s + 1] + gs[s + 2]>();
        if constexpr (s + 3 < 10) issue_h1(s + 3);
        else issue_w3(s - 7);
      });
      issue_w3(3);
      await(IC<15>{}, IC<8>{}, l < 10 ? ibt + 256 + j * 8 : OOB, ib + 256 + l * 8, l < 10, [&]() AINL {});
      u32x2 fc;
      const u32 fco = WS_CF + ((b0 >> 2) + (j < 9 ? (l & 1) : 0)) * 128;
      sfor<1, 4>([&](auto ic) AINL { constexpr int s = ic.v; issue_a(s, WS_X1 + (m * 10 + k3(s)) * 16384); });
      sfor<1, 10>([&](auto ic) AINL {
        constexpr int s = ic.v;
        constexpr int gs[13] = {0, 4, 4, 4, 9, 9, 10, 9, 9, 9, 6, 0, 0};
        if constexpr (s == 6) waitbar_pin<gs[s + 1] + gs[s + 2]>(fc);
        else waitbar<(s == 9 ? 0 : gs[s + 1] + gs[s + 2])>();
        if constexpr (s == 3) probe(fc, fco, rws);
        if constexpr (s == 7) { if (!__all(fc.x == ta && fc.y == tb)) poll_flags(fco, true, rws, ta, tb); }
        if constexpr (s + 3 < 10) {
          issue_a(s + 3, WS_X1 + (m * 10 + k3(s + 3)) * 16384);
          issue_w3(s + 3);
        } else if constexpr (s == 7) {
          issue_a(2, WS_ST1 + m * 20480);
          dma1<1>(L0 + W + 81920 + L * 1024, vl, rws, WS_ST1 + m * 20480 + 16384);
          dma1<1>(L0 + W + 86016 + L * 1024, vl, rws, WS_CS3 + b0 * 128);
        }
      });
      {
        LAS char* const p = sm + ACT + 2 * 16384 + (l >> 5) * 8192 + (L * 32 + (l & 31)) * 8;
        LAS char* const pt = sm + W + 81920 + (l >> 5) * 2048 + (L * 32 + (l & 31)) * 8;
        f32x2 v = *(LAS f32x2*)pt + *(LAS f32x2*)(pt + 1024);
#pragma unroll
        for (int i = 0; i < 8; ++i) v += *(LAS f32x2*)(p + i * 1024);
        const float s1 = sum32(v.x) * (1.0f / 640.0f), s2 = sum32(v.y) * (1.0f / 640.0f);
        const float vq = fmaxf(__builtin_fmaf(-s1, s1, s2), 0.f) + LN_EPS, r1 = __builtin_amdgcn_rsqf(vq);
        const GeluK k = gelu_k(r1, vq * r1);
        if (l < 32) *(LAS f32x4*)(sm + ACT + 3 * 16384 + (L * 32 + l) * 16) = (f32x4){k.x, k.y, k.z, s1};
        if (w == 8) *(LAS f32x4*)(sm + ACT + 3 * 16384 + 4096 + l * 16) = *(LAS f32x4*)(sm + W + 86016 + l * 16);
      }
      bar();
      _Pragma("nounroll") for (int s = 0; s < 12; ++s) issue_w4(s);
      await(IC<20>{}, IC<8>{}, l < 10 ? ibt + 384 + j * 8 : OOB, ib + 384 + l * 8, l < 10, [&]() AINL {});
      sfor<2, 5>([&](auto ic) AINL { constexpr int s = ic.v; issue_a(s, WS_H2 + (m * 24 + k4(s)) * 16384); });
      u32x2 fs;
      const u32 fso = ib + 512;
      auto it4 = [&](auto nv, int s, bool wgt) AINL {
        waitbar<nv.v>();
        issue_a(s + 3, WS_H2 + (m * 24 + k4(s + 3)) * 16384);
        if (wgt) issue_w4(s + 10);
      };
      it4(IC<8>{}, 2, true); it4(IC<10>{}, 3, true);
      _Pragma("nounroll") for (int s = 4; s < 14; ++s) it4(IC<12>{}, s, true);
      it4(IC<12>{}, 14, false); it4(IC<10>{}, 15, false); it4(IC<8>{}, 16, false);
      waitbar<8>();
      probe(fs, fso, rws);
      issue_a(20, WS_H2 + (m * 24 + k4(20)) * 16384);
      it4(IC<9>{}, 18, false); it4(IC<9>{}, 19, false);
      waitbar_pin<8>(fs);
      issue_a(23, WS_H2 + (m * 24 + k4(23)) * 16384);
      waitbar<8>();
      if (!__all(fs.x == ta && fs.y == tb)) poll_flags(fso, true, rws, ta, tb);
      issue_a(0, WS_SS + (m * 10 + j) * 16384);
      waitbar<8>(); waitbar<4>();
      waitbar<0>();
    } else {
      const int q4 = li >> 2, p4 = l & 3;
      LAS char* const wb0 = pin(sm + W + (4 * g + q4) * 128 + ((((2 * cq) ^ ((2 * g + (q4 >> 1)) & 3))) << 5) + 8 * p4);
      LAS char* const wb1 = pin(sm + W + (4 * g + q4) * 128 + ((((2 * cq + 1) ^ ((2 * g + (q4 >> 1)) & 3))) << 5) + 8 * p4);
      LAS char* const wh0 = pin(wb0 + 65536);
      LAS char* const wh1 = pin(wb1 + 65536);
      LAS char* const ab = pin(sm + ACT + tq * 4096 + l * 16);
      struct F64 { bf16x8 a0, a1, b0, b1; };
      F64 F0, F1;
      f32x4 acc[2][2], cs[2];
      u32x2 xs[2][2];
      auto step64p = [&](auto mf, auto tts, F64& R, LAS char* pb, LAS char* p0, LAS char* p1, const F64& M) AINL {
        auto mm = [&](int i) AINL { if constexpr (mf.v) acc[i & 1][i >> 1] = mfma((i & 1) ? M.a1 : M.a0, (i >> 1) ? M.b1 : M.b0, acc[i & 1][i >> 1]); SB(); };
        mm(0); R.a0 = ldstr(p0, p0 + 2048); SB();
        mm(1); R.b0 = lds128(pb); R.b1 = lds128(pb + tts.v); SB();
        mm(2); R.a1 = ldstr(p1, p1 + 2048); SB();
        mm(3);
      };
      auto step64 = [&](auto mf, auto tts, F64& R, LAS char* pb, u32 wo, const F64& M) AINL {
        step64p(mf, tts, R, pb, wo >= 65536 ? wh0 + (wo - 65536) : wb0 + wo, wo >= 65536 ? wh1 + (wo - 65536) : wb1 + wo, M);
      };
      auto mm = [&](const F64& M) AINL {
        acc[0][0] = mfma(M.a0, M.b0, acc[0][0]); acc[1][0] = mfma(M.a1, M.b0, acc[1][0]);
        acc[0][1] = mfma(M.a0, M.b1, acc[0][1]); acc[1][1] = mfma(M.a1, M.b1, acc[1][1]);
      };
      float mu[2], rs[2];
      {
        LAS char* const xb0 = pin(sm + ACT + tq * 4096 + rb0);
        LAS char* const xb1 = pin(sm + ACT + tq * 4096 + (rb0 ^ 64));
#pragma unroll
        for (int a = 0; a < 2; ++a) { acc[a][0] = zero4; acc[a][1] = zero4; }
        const int s_own = wrapk(j - rot1 + 10, 10);
        LAS char* const xo0 = pin(sm + ACT + tq * 4096 + li * 128 + (((4 * cq + (g >> 1)) ^ (li >> 1)) << 4) + 8 * (g & 1));
        LAS char* const xo1 = pin(sm + ACT + tq * 4096 + li * 128 + (((4 * cq + 2 + (g >> 1)) ^ (li >> 1)) << 4) + 8 * (g & 1));
        sfor<0, 10>([&](auto ic) AINL {
          constexpr int s = ic.v;
          constexpr u32 so = (s & 3) * 16384, wo = 65536 + (s & 3) * 8192;
          bar();
          step64(IC<(s > 0)>{}, IC<2048>{}, F0, xb0 + so, wo, F1);
          step64(IC<1>{}, IC<2048>{}, F1, xb1 + so, wo + 4096, F0);
          if constexpr (s == 1) { row_stats(); put_tag(W); SB(); }
          if (s == s_own) {
#pragma unroll
            for (int tt = 0; tt < 2; ++tt)
#pragma unroll
              for (int ct = 0; ct < 2; ++ct) xs[ct][tt] = *(LAS u32x2*)((ct ? xo1 : xo0) + so + tt * 2048);
          }
        });
        mm(F1);
        MFMA_DONE();
        bar();
        u32x4 g0 = *(LAS u32x4*)(sm + ACT + 2 * 16384 + (tq * 32 + li) * 16);
        u32x4 g1 = *(LAS u32x4*)(sm + ACT + 2 * 16384 + (tq * 32 + 16 + li) * 16);
        cs[0] = *(LAS f32x4*)(sm + ACT + 2 * 16384 + 4096 + cq * 128 + g * 16);
        cs[1] = *(LAS f32x4*)(sm + ACT + 2 * 16384 + 4096 + cq * 128 + 64 + g * 16);
        if (!__all(g0.y == ta && g0.w == tb && g1.y == ta && g1.w == tb)) {
          g0 = poll_granule(WS_XS + (m * 128 + tq * 32 + li) * 16, true, rws, ta, tb);
          g1 = poll_granule(WS_XS + (m * 128 + tq * 32 + 16 + li) * 16, true, rws, ta, tb);
        }
        mu[0] = asf(g0.x); rs[0] = asf(g0.z); mu[1] = asf(g1.x); rs[1] = asf(g1.z);
#pragma unroll
        for (int tt = 0; tt < 2; ++tt) {
          const float q2 = 1.0f - LN_EPS * rs[tt] * rs[tt] + LN_EPS;
          const float r2 = rs[tt] * __builtin_amdgcn_rsqf(q2);
          const GeluK k = gelu_k(r2, __builtin_amdgcn_rcpf(r2));
          const u32x2 h0 = gelu4(acc[0][tt], cs[0], mu[tt], k), h1 = gelu4(acc[1][tt], cs[1], mu[tt], k);
          st128_sc1((u32x4){h0.x, h0.y, h1.x, h1.y}, WS_H1 + ((2 * j + cq) * 4 + g) * 32768 + (m * 128 + tq * 32 + tt * 16 + li) * 16, rws);
          SB();
        }
        waitbar<0>();
      }
      f32x4 xn[2][2];
#pragma unroll
      for (int i = 0; i < 4; ++i) xn[i >> 1][i & 1] = keep((unpack4(xs[i >> 1][i & 1].x, xs[i >> 1][i & 1].y) - mu[i & 1]) * rs[i & 1]);
      bar();
      LAS char* const abt = pin(sm + ACT + g * 2048 + (tq * 32 + li) * 16);
#pragma unroll
      for (int a = 0; a < 2; ++a) { acc[a][0] = zero4; acc[a][1] = zero4; }
      sfor<0, 10>([&](auto ic) AINL {
        constexpr int s = ic.v;
        bar();
        step64(IC<(s > 0)>{}, IC<256>{}, F0, abt + (s & 3) * 16384, 16384 + s * 8192, F1);
        step64(IC<1>{}, IC<256>{}, F1, abt + (s & 3) * 16384 + 8192, 16384 + s * 8192 + 4096, F0);
      });
      mm(F1);
      MFMA_DONE();
      u32x4 x1p[2];
      {
        float sm1[2], sq1[2];
#pragma unroll
        for (int tt = 0; tt < 2; ++tt) {
          const f32x4 y0 = xn[0][tt] + acc[0][tt], y1 = xn[1][tt] + acc[1][tt];
          x1p[tt] = pack8(y0, y1);
          *(LAS u32x4*)(sm + ACT + (2 * tq + tt) * 2048 + cq * 1024 + l * 16) = x1p[tt];
          st128_sc1(x1p[tt], WS_X1 + (m * 10 + j) * 16384 + (2 * tq + tt) * 2048 + cq * 1024 + l * 16, rws);
          const f32x4 a = y0 + y1, q = y0 * y0 + y1 * y1;
          sm1[tt] = (a.x + a.y) + (a.z + a.w); sq1[tt] = (q.x + q.y) + (q.z + q.w);
        }
        auto fold = [&](float v0, float v1) AINL {
          auto r = __builtin_amdgcn_permlane16_swap(asu(v0), asu(v1), false, false);
          const u32 a = r[0], b = r[1];
          return sum32(asf(a) + asf(b));
        };
        const u32x2 st = {asu(fold(sm1[0], sm1[1])), asu(fold(sq1[0], sq1[1]))};
        st64_sc1(st, l < 32 ? WS_ST1 + m * 20480 + (2 * j + cq) * 1024 + (tq * 32 + l) * 8 : OOB, rws);
      }
      asm volatile("" : "+v"(xs[0][0]), "+v"(xs[0][1]), "+v"(xs[1][0]), "+v"(xs[1][1]), "+v"(x1p[0]), "+v"(x1p[1]));
      waitbar<0>();
      {
        const int sg = (g & 1) * 32, rowo = (4 * g + q4) * 320 + 8 * p4;
        LAS char* const bA = pin(sm + W + rowo + 192 * cq + sg);
        LAS char* const bB = pin(sm + W + rowo + 192 * cq - sg);
        LAS char* const bC = pin(sm + W + rowo + (cq ? 160 - sg : 128 + sg));
        LAS char* const bA2 = pin(bA + 40960);
        LAS char* const bB2 = pin(bB + 40960);
        LAS char* const bC2 = pin(bC + 40960);
        LAS char* const at0 = pin(ab + cq * 2048);
        LAS char* const at1 = pin(ab + (cq ^ 1) * 2048);
        struct F3 { bf16x8 a[5], b0, b1; };
        F3 G0, G1;
        f32x4 a3[5][2];
        auto step3 = [&](auto mf, F3& R, u32 so, u32 wo, const F3& M) AINL {
          LAS char* const pA = wo >= 40960 ? bA2 + (wo - 40960) : bA + wo;
          LAS char* const pB = wo >= 40960 ? bB2 + (wo - 40960) : bB + wo;
          LAS char* const pC = wo >= 40960 ? bC2 + (wo - 40960) : bC + wo;
          auto mm = [&](int i) AINL { if constexpr (mf.v) a3[i >> 1][i & 1] = mfma(M.a[i >> 1], (i & 1) ? M.b1 : M.b0, a3[i >> 1][i & 1]); SB(); };
          auto ra = [&](int i) AINL { LAS char* const pw = i == 4 ? pC : ((i & 1) ? pB : pA) + 32 * i; R.a[i] = ldstr(pw, pw + 5120); SB(); };
          mm(0); ra(0); mm(1); R.b0 = lds128(at0 + so); SB(); mm(2); R.b1 = lds128(at1 + so); SB();
          mm(3); ra(1); mm(4); ra(2); mm(5); ra(3); mm(6); ra(4); mm(7); mm(8); mm(9);
        };
#pragma unroll
        for (int i = 0; i < 5; ++i) { a3[i][0] = zero4; a3[i][1] = zero4; }
        sfor<0, 10>([&](auto ic) AINL {
          constexpr int s = ic.v;
          constexpr u32 so = (s & 3) * 16384, wo = (s & 3) * 20480;
          if constexpr (s == 1) bar();
          if constexpr (s >= 1) bar();
          step3(IC<(s > 0)>{}, G0, so, wo, G1);
          step3(IC<1>{}, G1, so + 1024, wo + 10240, G0);
          if constexpr (s == 9) *(volatile LAS u32*)(sm + ACT + 3 * 16384 + 13312 + w * 4) = 0;
        });
#pragma unroll
        for (int i = 0; i < 5; ++i) { a3[i][0] = mfma(G1.a[i], G1.b0, a3[i][0]); a3[i][1] = mfma(G1.a[i], G1.b1, a3[i][1]); }
        MFMA_DONE();
        bar();
        GeluK k1[2];
#pragma unroll
        for (int tt = 0; tt < 2; ++tt) k1[tt] = *(LAS f32x4*)(sm + ACT + 3 * 16384 + (tq * 32 + (tt ^ cq) * 16 + li) * 16);
        f32x4 c3[5];
#pragma unroll
        for (int i = 0; i < 5; ++i) c3[i] = *(LAS f32x4*)(sm + ACT + 3 * 16384 + 4096 + ((i == 4 ? 64 + 16 * cq : 96 * cq + 16 * i) + 4 * g) * 4);
        auto act4 = [&](int i, int tt) AINL { return gelu4(a3[i][tt], c3[i], k1[tt].w, k1[tt]); };
        const u32 vh = WS_H2 + m * 24 * 16384 + tq * 4096 + l * 16, vhl = cq && nb == 4 ? OOB : vh;
        auto put = [&](u32x4 v, int b, int t, u32 vo) AINL {
          const int ob = b0 + b - 2 * kt0, to = (t ^ cq) * 2048;
          __builtin_amdgcn_raw_buffer_store_b128(v, rws, vo, ((b0 + b) >> 1) * 16384 + ((b0 + b) & 1) * 1024 + to, 16);
          const int junk = 3 * 16384 + 12288 - tq * 4096;
          *(LAS u32x4*)(ab + ssel_lt(ob, 0, junk, ssel_lt(ob, 4, (ob >> 1) * 16384 + (ob & 1) * 1024 + to, junk))) = v;
          SB();
        };
        auto two = [&](u32x2 lo, u32x2 hi) AINL { return (u32x4){lo.x, lo.y, hi.x, hi.y}; };
        volatile LAS u32* const flg = (volatile LAS u32*)(sm + ACT + 3 * 16384 + 13312);
        *(volatile LAS u32x2*)(sm + ACT + 3 * 16384 + 8192 + w * 512 + l * 8) = act4(4, 1);
        flg[w] = 1;
        put(two(act4(0, 0), act4(1, 0)), 3 * cq, 0, vh); put(two(act4(2, 0), act4(3, 0)), 3 * cq + 1, 0, vhl);
        _Pragma("nounroll") for (int it = 0; it < SPIN_CAP; ++it)
          if (__builtin_amdgcn_readfirstlane(flg[w ^ 4])) break;
        asm volatile("" ::: "memory");
        const u32x2 oth = *(LAS u32x2*)(sm + ACT + 3 * 16384 + 8192 + (w ^ 4) * 512 + l * 8), own = act4(4, 0);
        put(cq ? two(oth, own) : two(own, oth), 2, 0, vh);
        put(two(act4(0, 1), act4(1, 1)), 3 * cq, 1, vh); put(two(act4(2, 1), act4(3, 1)), 3 * cq + 1, 1, vhl);
      }
      waitbar<0>();
      const rsrc_t rdk = mk_rsrc(dskip, 1280);
      const u32x2 dk0 = bld64((cq * 32 + 4 * g) * 2, rdk, j * 128), dk1 = bld64((cq * 32 + 16 + 4 * g) * 2, rdk, j * 128);
      f32x4 xf[2][2], xd[2][2], x1f[2][2];
      auto pre4 = [&]() AINL {
#pragma unroll
        for (int i = 0; i < 4; ++i) {
          const int ct = i >> 1, tt = i & 1;
          const u32x2 dk = ct ? dk1 : dk0;
          xf[ct][tt] = keep(unpack4(xs[ct][tt].x, xs[ct][tt].y));
          xd[ct][tt] = keep((xf[ct][tt] - mu[tt]) * rs[tt] * unpack4(dk.x, dk.y));
          x1f[ct][tt] = keep(ct ? unpack4(x1p[tt].z, x1p[tt].w) : unpack4(x1p[tt].x, x1p[tt].y));
        }
      };
#pragma unroll
      for (int a = 0; a < 2; ++a) { acc[a][0] = zero4; acc[a][1] = zero4; }
      sfor<0, 4>([&](auto ic) AINL {
        constexpr int s = ic.v;
        if constexpr (s == 2) { pre4(); bar(); }
        if constexpr (s >= 2) bar();
        step64(IC<(s > 0)>{}, IC<2048>{}, F0, ab + s * 16384, s * 8192, F1);
        step64(IC<1>{}, IC<2048>{}, F1, ab + s * 16384 + 1024, s * 8192 + 4096, F0);
      });
      _Pragma("nounroll") for (int it = 1; it < 6; ++it) {
        const int wo = wrapk(it, 3) * 32768;
        LAS char* const q0 = pin(wb0 + wo);
        LAS char* const q1 = pin(wb1 + wo);
        sfor<0, 4>([&](auto ic) AINL {
          constexpr int s = ic.v;
          bar();
          step64p(IC<1>{}, IC<2048>{}, F0, ab + s * 16384, q0 + s * 8192, q1 + s * 8192, F1);
          step64p(IC<1>{}, IC<2048>{}, F1, ab + s * 16384 + 1024, q0 + s * 8192 + 4096, q1 + s * 8192 + 4096, F0);
        });
      }
      mm(F1);
      MFMA_DONE();
      bar();
      const rsrc_t rout = mk_rsrc(out, 2048 * 2560);
#pragma unroll
      for (int tt = 0; tt < 2; ++tt) {
        const u32x4 sv = *(LAS u32x4*)(sm + ACT + w * 2048 + tt * 1024 + l * 16);
        u32x4 o[2];
#pragma unroll
        for (int ct = 0; ct < 2; ++ct) {
          const f32x4 sf = (ct ? unpack4(sv.z, sv.w) : unpack4(sv.x, sv.y)) + xd[ct][tt];
          const f32x4 z = x1f[ct][tt] + acc[ct][tt], u = z * -1.4426950409f;
          const f32x4 d = (f32x4){__builtin_amdgcn_exp2f(u.x), __builtin_amdgcn_exp2f(u.y), __builtin_amdgcn_exp2f(u.z), __builtin_amdgcn_exp2f(u.w)} + 1.0f;
          const f32x4 r = {__builtin_amdgcn_rcpf(d.x), __builtin_amdgcn_rcpf(d.y), __builtin_amdgcn_rcpf(d.z), __builtin_amdgcn_rcpf(d.w)};
          o[ct] = __builtin_bit_cast(u32x4, __builtin_elementwise_fma(sf * z, r, xf[ct][tt]));
        }
        u32x4 t = li < 8 ? o[1] : o[0];
#pragma unroll
        for (int e = 0; e < 4; ++e) { const u32 v = t[e]; t[e] = __builtin_amdgcn_update_dpp(0u, v, 0x128, 0xf, 0xf, true); }
        const u32 vo = (m * 128 + tq * 32 + tt * 16 + (li & 7)) * 2560 + (j * 64 + cq * 32 + (li >> 3) * 16 + 4 * g) * 4;
        __builtin_amdgcn_raw_buffer_store_b128(li < 8 ? o[0] : t, rout, vo, 0, 2);
        __builtin_amdgcn_raw_buffer_store_b128(li < 8 ? t : o[1], rout, vo, 8 * 2560, 2);
      }
    }
  } else if (role < 15) {
    const int a = role - 10;
    constexpr u32 MISC = 147456, CST = 157696, TAG = 159744, AS3 = 131072;
    if (w >= 8) {
      const rsrc_t rbr = mk_rsrc(b_re, 640 * 1280), rbi = mk_rsrc(b_im, 640 * 1280);
      const u32 vx0 = (m * 128 + 32 * (r8 >> 2) + 8 * L + (r8 & 3)) * 1280 + (((l & 7) ^ g) << 4);
      const u32 vx1 = (m * 128 + 32 * (2 + (r8 >> 2)) + 8 * L + (r8 & 3)) * 1280 + (((l & 7) ^ (4 + g)) << 4) - 1024;
      const u32 vb0 = (a * 128 + L * 32 + r8) * 1280 + (((l & 7) ^ g) << 4);
      const u32 vb1 = (a * 128 + L * 32 + 8 + r8) * 1280 + (((l & 7) ^ (4 + g)) << 4) - 1024;
      const u32 ldsw = L0 + L * 4096;
      auto issue_nk = [&](u32 d, rsrc_t r, u32 so) AINL { dma2<0>(d, vb0, vb1, r, so); dma2<0>(d + 2048, vb0, vb1, r, so + 20480); };
      auto issue1 = [&](int s) AINL {
        const int kt = wrapk(s + rotA1, 10);
        const u32 d = ldsw + (s % 3) * 49152;
        dma2<0>(d, vx0, vx1, rx, kt * 128); dma2<0>(d + 2048, vx0, vx1, rx, kt * 128 + 5120);
        issue_nk(d + 16384, rbr, kt * 128); issue_nk(d + 32768, rbi, kt * 128);
      };
      auto ktc = [&](int s) AINL { return ssel_lt(s, 4, (s >> 1) * 10 + 2 * a + (s & 1), ((s - 4) >> 3) * 10 + ssel_lt((s - 4) & 7, 2 * a, (s - 4) & 7, ((s - 4) & 7) + 2)); };
      auto issue_c = [&](int s) AINL {
        const int kt = ktc(s);
        issue_nk(ldsw + 49152 + (s % 5) * 16384, mk_rsrc(kt < 10 ? c_re : c_im, 640 * 1280), wrapk(kt, 10) * 128);
      };
      const u32 va = L * 4096 + l * 16;
      auto issue_h = [&](int s) AINL { dma4<1>(ldsw + ((s & 3) == 3 ? AS3 : (s & 3) * 16384), va, rws, WS_HH + (m * 20 + ktc(s)) * 16384); };
      u32 ldum = 0;
      if (L < 3) touch(ldum, ((m * 5 + a) * 192 + L * 64 + l) * 128, mk_rsrc(w3, 640 * 3072));
      __builtin_amdgcn_s_sleep(16);
      issue1(0); issue1(1);
      sfor<0, 10>([&](auto ic) AINL {
        constexpr int s = ic.v;
        waitbar<12>();
        if constexpr (s == 7) dma1<1>(L0 + MISC + L * 1024, (L & 1) * 1024 + l * 16, rws, L < 2 ? WS_XS + m * 2048 : WS_RS + a * 2048);
        if constexpr (s + 2 < 10) issue1(s + 2);
        else if constexpr (s == 8) { issue_c(0); issue_c(1); issue_c(2); }
        else {
          if (L < 2) {
            const int t = L * 64 + l;
            u32x4 g0 = *(LAS u32x4*)(sm + MISC + t * 16);
            if (!__all(g0.y == ta && g0.w == tb)) g0 = poll_granule(WS_XS + (m * 128 + t) * 16, true, rws, ta, tb);
            const float rs = asf(g0.z);
            *(LAS float*)(sm + CST + (t >> 5) * 144 + (t & 31) * 4) = rs;
            *(LAS float*)(sm + CST + 576 + (t >> 5) * 144 + (t & 31) * 4) = -rs * asf(g0.x);
          }
        }
        if constexpr (s == 0) { hold(ldum); get_tag(TAG); }
      });
      bar();
      issue_c(3); issue_c(4);
      bar();
      await(IC<0>{}, IC<8>{}, l < 5 ? WS_IB + IB_A + (m * 5 + l) * 128 + a * 8 : OOB, WS_IB + IB_A + (m * 5 + a) * 128 + l * 8, l < 5, [&]() AINL {});
      issue_h(4); issue_h(5); issue_c(5); issue_c(6);
      waitbar<12>();
      issue_h(6); issue_h(7); issue_c(7);
      waitbar<12>();
      issue_h(8); issue_c(8);
      _Pragma("nounroll") for (int s = 6; s < 17; ++s) { waitbar<16>(); issue_h(s + 3); issue_c(s + 3); }
      waitbar<16>(); waitbar<8>(); waitbar<0>();
    } else {
      const int p = a * 128 + w * 16 + li;
      const u32 plr = bld16(p * 2, mk_rsrc(lam_re, 1280)), pli = bld16(p * 2, mk_rsrc(lam_im, 1280)), pdt = bld16(p * 2, mk_rsrc(log_dt, 1280));
      row_stats();
      put_tag(TAG);
      float lbr, lbi, kr, ki;
      {
        const float Lr = bflo(plr), Li = bflo(pli);
        const float dt = bflo(pk2(expf(bflo(pdt)), 0.f));
        const float ea = expf(dt * Lr);
        float sn, cn; sincosf(dt * Li, &sn, &cn);
        lbr = ea * cn; lbi = ea * sn;
        const float inv = 2.0f / (Lr * Lr + Li * Li);
        kr = ((lbr - 1.0f) * Lr + lbi * Li) * inv;
        ki = (lbi * Lr - (lbr - 1.0f) * Li) * inv;
        asm volatile("" : "+v"(lbr), "+v"(lbi), "+v"(kr), "+v"(ki));
      }
      f32x4 are[8], aim[8];
      u32 wdum = 0;
      if (w >= 2 && w < 4) touch(wdum, l < 40 ? ((m * 5 + a) * 80 + (w & 1) * 40 + l) * 128 : OOB, mk_rsrc(w2, 640 * 1280));
      if (w >= 4) touch(wdum, l < 40 ? ((m * 5 + a) * 80 + (w & 1) * 40 + l) * 128 : OOB, mk_rsrc(w >= 6 ? c_im : c_re, 640 * 1280));
#pragma unroll
      for (int i = 0; i < 8; ++i) { are[i] = zero4; aim[i] = zero4; }
      {
        struct FW { bf16x8 br, bi; };
        struct FX { bf16x8 x[4]; };
        FW W0, W1; FX Xa, Xb;
        const int pc = a * 128 + m * 8 + w;
        u32x4 t0, t1; u32 u0, u1;
        auto half = [&](auto mf, auto rw, auto hh, FW& RW, LAS char* pw, FX& RX, LAS char* px, const FW& MW, const FX& MX) AINL {
          constexpr int h = hh.v;
          auto mm = [&](int i) AINL {
            if constexpr (mf.v) { if (i & 1) aim[4 * h + (i >> 1)] = mfma(MX.x[i >> 1], MW.bi, aim[4 * h + (i >> 1)]); else are[4 * h + (i >> 1)] = mfma(MX.x[i >> 1], MW.br, are[4 * h + (i >> 1)]); }
            SB();
          };
          auto rx_ = [&](int i) AINL { RX.x[i] = lds128(px + i * 2048); SB(); };
          if constexpr (rw.v) {
            mm(0); RW.br = lds128(pw); SB(); mm(1); rx_(0); mm(2); RW.bi = lds128(pw + 16384); SB();
            mm(3); rx_(1); mm(4); rx_(2); mm(5); rx_(3); mm(6); mm(7);
          } else {
            mm(0); rx_(0); mm(1); rx_(1); mm(2); rx_(2); mm(3); rx_(3); mm(4); mm(5); mm(6); mm(7);
          }
        };
        sfor<0, 10>([&](auto ic) AINL {
          constexpr int s = ic.v;
          LAS char* const x0 = pin(sm + rb0 + (s % 3) * 49152);
          LAS char* const x1 = pin(sm + (rb0 ^ 64) + (s % 3) * 49152);
          LAS char* const w0 = pin(x0 + w * 2048 + 16384);
          LAS char* const w1_ = pin(x1 + w * 2048 + 16384);
          bar();
          half(IC<(s > 0)>{}, IC<1>{}, IC<1>{}, W0, w0, Xa, x0, W1, Xb);
          half(IC<1>{}, IC<0>{}, IC<0>{}, W0, w0, Xb, x0 + 8192, W0, Xa);
          half(IC<1>{}, IC<1>{}, IC<1>{}, W1, w1_, Xa, x1, W0, Xb);
          half(IC<1>{}, IC<0>{}, IC<0>{}, W1, w1_, Xb, x1 + 8192, W1, Xa);
          if constexpr (s == 2) {
            const rsrc_t rbr = mk_rsrc(b_re, 640 * 1280), rbi = mk_rsrc(b_im, 640 * 1280);
            t0 = bld128(l * 16, rbr, pc * 1280); t1 = bld128(l * 16, rbi, pc * 1280);
            u0 = bld32(1024 + l * 4, rbr, pc * 1280); u1 = bld32(1024 + l * 4, rbi, pc * 1280);
          }
          if constexpr (s == 4) {
            auto sum10 = [&](u32x4 t, u32 u) AINL {
              return wavesum(((bflo(t.x) + bfhi(t.x)) + (bflo(t.y) + bfhi(t.y))) + ((bflo(t.z) + bfhi(t.z)) + (bflo(t.w) + bfhi(t.w))) + (bflo(u) + bfhi(u)));
            };
            st128_sc1((u32x4){asu(sum10(t0, u0)), ta, asu(sum10(t1, u1)), tb}, l == 0 ? WS_RS + pc * 16 : OOB, rws);
          }
        });
#pragma unroll
        for (int i = 0; i < 4; ++i) { are[4 + i] = mfma(Xb.x[i], W1.br, are[4 + i]); aim[4 + i] = mfma(Xb.x[i], W1.bi, aim[4 + i]); }
        MFMA_DONE();
      }
      bar();
      u32x4 rg = *(LAS u32x4*)(sm + MISC + 2048 + (w * 16 + li) * 16);
      if (!__all(rg.y == ta && rg.w == tb)) rg = poll_granule(WS_RS + p * 16, true, rws, ta, tb);
      const float sre = asf(rg.x), sim = asf(rg.z);
      asm volatile("s_waitcnt vmcnt(0)" : "+v"(wdum)::"memory");
      LAS char* const cst = pin(sm + CST + g * 144);
#pragma unroll
      for (int mt = 0; mt < 8; ++mt) {
        const f32x4 rv = *(LAS f32x4*)(cst + mt * 16), nv = *(LAS f32x4*)(cst + 576 + mt * 16);
        are[mt] = rv * are[mt] + nv * sre;
        aim[mt] = rv * aim[mt] + nv * sim;
      }
      float hr = 0.f, hi = 0.f;
#pragma unroll
      for (int mt = 0; mt < 8; ++mt)
#pragma unroll
        for (int e = 0; e < 4; ++e) {
          const float nr = __builtin_fmaf(-lbi, hi, __builtin_fmaf(lbr, hr, are[mt][e]));
          const float ni = __builtin_fmaf(lbi, hr, __builtin_fmaf(lbr, hi, aim[mt][e]));
          hr = nr; hi = ni;
        }
      SB();
      float er[4], ei[4];
      gather4(hr, er); gather4(hi, ei);
      float p32r = lbr, p32i = lbi;
#pragma unroll
      for (int i = 0; i < 5; ++i) { const float t = p32r * p32r - p32i * p32i; p32i = 2.0f * p32r * p32i; p32r = t; }
      float cr[5], ci[5];
      cr[0] = 0.f; ci[0] = 0.f;
#pragma unroll
      for (int q = 0; q < 4; ++q) {
        cr[q + 1] = __builtin_fmaf(-p32i, ci[q], __builtin_fmaf(p32r, cr[q], er[q]));
        ci[q + 1] = __builtin_fmaf(p32i, cr[q], __builtin_fmaf(p32r, ci[q], ei[q]));
      }
      st128_sc1((u32x4){asu(cr[4]), ta, asu(ci[4]), tb}, l < 16 ? WS_EG + (m * 640 + p) * 16 : OOB, rws);
      SB();
      const float p64r = p32r * p32r - p32i * p32i, p64i = 2.0f * p32r * p32i;
      const float Pr = p64r * p64r - p64i * p64i, Pi = 2.0f * p64r * p64i;
#pragma unroll
      for (int mt = 0; mt < 8; ++mt) {
#pragma unroll
        for (int e = 0; e < 4; ++e) {
          const float ur = are[mt][e], ui = aim[mt][e];
          are[mt][e] = __builtin_fmaf(kr, ur, -ki * ui);
          aim[mt][e] = __builtin_fmaf(-kr, ui, -ki * ur);
        }
        are[mt] = keep(are[mt]); aim[mt] = keep(aim[mt]);
      }
      SB();
      float Hr = 0.f, Hi = 0.f;
      if (m > 0) {
        __builtin_amdgcn_s_sleep(10);
        u32x4 e0, e1, e2, e3;
        const int m0 = m - 1 - 4 * g;
        const u32 o0 = WS_EG + (m0 * 640 + p) * 16;
        const bool v0 = m0 >= 0, v1 = m0 >= 1, v2 = m0 >= 2, v3 = m0 >= 3;
        _Pragma("nounroll") for (int it = 0; it < SPIN_CAP; ++it) {
          asm volatile("buffer_load_dwordx4 %0, %4, %8, 0 offen sc1\n\tbuffer_load_dwordx4 %1, %5, %8, 0 offen sc1\n\t"
                       "buffer_load_dwordx4 %2, %6, %8, 0 offen sc1\n\tbuffer_load_dwordx4 %3, %7, %8, 0 offen sc1\n\ts_waitcnt vmcnt(0)"
                       : "=&v"(e0), "=&v"(e1), "=&v"(e2), "=&v"(e3)
                       : "v"(v0 ? o0 : OOB), "v"(v1 ? o0 - 10240 : OOB), "v"(v2 ? o0 - 20480 : OOB), "v"(v3 ? o0 - 30720 : OOB), "s"(rws) : "memory");
          const bool ok = (!v0 || (e0.y == ta && e0.w == tb)) && (!v1 || (e1.y == ta && e1.w == tb)) &&
                          (!v2 || (e2.y == ta && e2.w == tb)) && (!v3 || (e3.y == ta && e3.w == tb));
          if (__all(ok)) break;
          __builtin_amdgcn_s_sleep(6);
        }
        float qr = asf(e3.x), qi = asf(e3.z), t;
        t = __builtin_fmaf(-Pi, qi, __builtin_fmaf(Pr, qr, asf(e2.x))); qi = __builtin_fmaf(Pi, qr, __builtin_fmaf(Pr, qi, asf(e2.z))); qr = t;
        t = __builtin_fmaf(-Pi, qi, __builtin_fmaf(Pr, qr, asf(e1.x))); qi = __builtin_fmaf(Pi, qr, __builtin_fmaf(Pr, qi, asf(e1.z))); qr = t;
        t = __builtin_fmaf(-Pi, qi, __builtin_fmaf(Pr, qr, asf(e0.x))); qi = __builtin_fmaf(Pi, qr, __builtin_fmaf(Pr, qi, asf(e0.z))); qr = t;
        float gr[4], gi[4];
        gather4(qr, gr); gather4(qi, gi);
        const float P2r = Pr * Pr - Pi * Pi, P2i = 2.0f * Pr * Pi;
        const float P4r = P2r * P2r - P2i * P2i, P4i = 2.0f * P2r * P2i;
        Hr = gr[3]; Hi = gi[3];
#pragma unroll
        for (int q = 2; q >= 0; --q) {
          t = __builtin_fmaf(-P4i, Hi, __builtin_fmaf(P4r, Hr, gr[q])); Hi = __builtin_fmaf(P4i, Hr, __builtin_fmaf(P4r, Hi, gi[q])); Hr = t;
        }
      }
      u32 wdum4 = 0;
      if (w >= 5) touch(wdum4, ((m * 5 + a) * 192 + (w - 5) * 64 + l) * 128, mk_rsrc(w4, 1536 * 1280));
      {
        const float p96r = p64r * p32r - p64i * p32i, p96i = p64r * p32i + p64i * p32r;
        const float fr = g == 0 ? 1.f : g == 1 ? p32r : g == 2 ? p64r : p96r;
        const float fi = g == 0 ? 0.f : g == 1 ? p32i : g == 2 ? p64i : p96i;
        const float c0r = g == 0 ? cr[0] : g == 1 ? cr[1] : g == 2 ? cr[2] : cr[3];
        const float c0i = g == 0 ? ci[0] : g == 1 ? ci[1] : g == 2 ? ci[2] : ci[3];
        const float sr = __builtin_fmaf(-fi, Hi, __builtin_fmaf(fr, Hr, c0r)), si = __builtin_fmaf(fi, Hr, __builtin_fmaf(fr, Hi, c0i));
        hr = __builtin_fmaf(kr, sr, -ki * si);
        hi = __builtin_fmaf(-kr, si, -ki * sr);
      }
      {
        LAS char* const tb_ = sm + (w < 6 ? w * 8192 : AS3 + (w - 6) * 8192);
        u32x4 hv[2][2][2];
        const int swc = ((li & 3) << 1) | ((li >> 2) & 1) | ((li >> 3) << 3);
        const int q4 = li >> 2, p4 = l & 3;
        const int c0 = 8 * (g & 1) + q4, c1 = c0 + 4;
        const int sw0 = ((c0 & 3) << 1) | ((c0 >> 3) << 3), sw1 = sw0 | 1;
        const u32 ob = WS_HH + (m * 20 + 2 * a + (w >> 2)) * 16384 + ((w >> 1) & 1) * 1024 + ((2 * (w & 1) + (g & 1)) * 16 + li) * 16;
#pragma unroll
        for (int hf = 0; hf < 2; ++hf) {
#pragma unroll
          for (int mt = 4 * hf; mt < 4 * hf + 4; ++mt)
#pragma unroll
            for (int e = 0; e < 4; ++e) {
              const float nr = __builtin_fmaf(lbi, hi, __builtin_fmaf(lbr, hr, are[mt][e]));
              const float ni = __builtin_fmaf(-lbi, hr, __builtin_fmaf(lbr, hi, aim[mt][e]));
              hr = nr; hi = ni;
              are[mt][e] = hr; aim[mt][e] = hi;
            }
#pragma unroll
          for (int i = 2 * hf; i < 2 * hf + 2; ++i) {
            *(LAS u32x4*)(tb_ + li * 256 + (((4 * g + i) ^ swc) << 4)) = pack8(are[2 * i], are[2 * i + 1]);
            *(LAS u32x4*)(tb_ + 4096 + li * 256 + (((4 * g + i) ^ swc) << 4)) = pack8(aim[2 * i], aim[2 * i + 1]);
          }
          asm volatile("s_waitcnt lgkmcnt(0)" ::: "memory");
#pragma unroll
          for (int part = 0; part < 2; ++part)
#pragma unroll
            for (int u = 0; u < 2; ++u) {
              const int tt = 4 * u + 2 * (g >> 1) + hf, ch = 2 * tt + (p4 >> 1);
              const bf16x8 v = ldstr(tb_ + part * 4096 + c0 * 256 + ((ch ^ sw0) << 4) + 8 * (p4 & 1),
                                     tb_ + part * 4096 + c1 * 256 + ((ch ^ sw1) << 4) + 8 * (p4 & 1));
              hv[hf][part][u] = __builtin_bit_cast(u32x4, v);
              st128_sc1(hv[hf][part][u], ob + part * 163840 + tt * 2048, rws);
            }
          SB();
        }
        bar();
        LAS char* const hd = sm + (w >> 2) * 16384 + (g >> 1) * 4096 + ((w >> 1) & 1) * 1024 + ((2 * (w & 1) + (g & 1)) * 16 + li) * 16;
        LAS char* const hd1 = hd + ((w >> 2) ? AS3 - 16384 : 32768);
#pragma unroll
        for (int hf = 0; hf < 2; ++hf)
#pragma unroll
          for (int u = 0; u < 2; ++u) {
            *(LAS u32x4*)(hd + u * 8192 + hf * 2048) = hv[hf][0][u];
            *(LAS u32x4*)(hd1 + u * 8192 + hf * 2048) = hv[hf][1][u];
          }
      }
      asm volatile("s_waitcnt vmcnt(0) lgkmcnt(0)\n\ts_barrier" : "+v"(wdum4)::"memory");
      f32x4 ac[4][2];
#pragma unroll
      for (int i = 0; i < 4; ++i) { ac[i][0] = zero4; ac[i][1] = zero4; }
      LAS char* const ab = pin(sm + tq * 4096 + l * 16);
      LAS char* const ab3 = pin(ab + AS3);
      LAS char* const cb0 = pin(sm + 49152 + cq * 8192 + rb0);
      LAS char* const cb1 = pin(sm + 49152 + cq * 8192 + (rb0 ^ 64));
      struct FC { bf16x8 c[4], b0, b1; };
      FC G0, G1;
      auto stepC = [&](auto mf, FC& R, int ks, int s, LAS char* pc, const FC& M) AINL {
        LAS char* const pa = ((s & 3) == 3 ? ab3 : ab + (s & 3) * 16384) + ks * 1024;
        auto mm = [&](int i) AINL { if constexpr (mf.v) ac[i >> 1][i & 1] = mfma(M.c[i >> 1], (i & 1) ? M.b1 : M.b0, ac[i >> 1][i & 1]); SB(); };
        auto rc = [&](int i) AINL { R.c[i] = lds128(pc + i * 2048); SB(); };
        mm(0); R.b0 = lds128(pa); SB(); mm(1); rc(0); mm(2); R.b1 = lds128(pa + 2048); SB(); mm(3); rc(1); mm(4); rc(2); mm(5); rc(3); mm(6); mm(7);
      };
      sfor<0, 4>([&](auto ic) AINL {
        constexpr int s = ic.v;
        if constexpr (s == 2) bar();
        stepC(IC<(s > 0)>{}, G0, 0, s, cb0 + s * 16384, G1);
        stepC(IC<1>{}, G1, 1, s, cb1 + s * 16384, G0);
      });
      int cslot = 4;
      _Pragma("nounroll") for (int it = 1; it < 5; ++it) {
        sfor<0, 4>([&](auto ic) AINL {
          constexpr int s = ic.v;
          LAS char* const q0 = pin(cb0 + cslot * 16384);
          LAS char* const q1 = pin(cb1 + cslot * 16384);
          cslot = wrapk(cslot + 1, 5);
          bar();
          stepC(IC<1>{}, G0, 0, s, q0, G1);
          stepC(IC<1>{}, G1, 1, s, q1, G0);
        });
      }
#pragma unroll
      for (int ct = 0; ct < 4; ++ct) { ac[ct][0] = mfma(G1.c[ct], G1.b0, ac[ct][0]); ac[ct][1] = mfma(G1.c[ct], G1.b1, ac[ct][1]); }
      MFMA_DONE();
#pragma unroll
      for (int cb_ = 0; cb_ < 2; ++cb_)
#pragma unroll
        for (int tt = 0; tt < 2; ++tt)
          st128_sc1(pack8(ac[2 * cb_][tt], ac[2 * cb_ + 1][tt]), WS_SS + ((m * 10 + 2 * a + cq) * 8 + tq + 4 * cb_) * 2048 + tt * 1024 + l * 16, rws);
      waitbar<0>();
      if (w == 0) signal(l < 2 ? WS_IB + (m * 10 + 2 * a + l) * IB_B + 512 : OOB);
    }
  } else if (w < 8) {
    u32 d0 = 0;
    if (w == 7) touch(d0, l < 50 ? WS_IB + m * 10 * IB_B + l * 128 : l < 55 ? WS_IB + IB_A + (m * 5 + l - 50) * 128 : OOB, rws);
    row_stats();
    asm volatile("s_waitcnt vmcnt(0)" : "+v"(d0)::"memory");
    auto add8 = [&](float (&c)[8], u32x4 t) AINL {
      c[0] += bflo(t.x); c[1] += bfhi(t.x); c[2] += bflo(t.y); c[3] += bfhi(t.y); c[4] += bflo(t.z); c[5] += bfhi(t.z); c[6] += bflo(t.w); c[7] += bfhi(t.w);
    };
    if (m < 12) {
      const int c = m;
      const rsrc_t rw3 = mk_rsrc(w3, 640 * 3072);
      float cs[8] = {0.f, 0.f, 0.f, 0.f, 0.f, 0.f, 0.f, 0.f};
      const u32 vo = (tid >> 4) * 3072 + c * 256 + (tid & 15) * 16;
      u32x4 t[20];
#pragma unroll
      for (int i = 0; i < 20; ++i) t[i] = bld128(vo, rw3, i * 32 * 3072);
      SB();
#pragma unroll
      for (int i = 0; i < 20; ++i) add8(cs, t[i]);
#pragma unroll
      for (int e = 0; e < 8; ++e) cs[e] = sum32(sum16(cs[e]));
      if (l < 16) {
        *(LAS f32x4*)(sm + w * 512 + l * 32) = (f32x4){cs[0], cs[1], cs[2], cs[3]};
        *(LAS f32x4*)(sm + w * 512 + l * 32 + 16) = (f32x4){cs[4], cs[5], cs[6], cs[7]};
      }
      bar();
      if (w < 2) {
        float s = 0.f;
#pragma unroll
        for (int i = 0; i < 8; ++i) s += *(LAS float*)(sm + i * 512 + tid * 4);
        st32_sc1(asu(s), WS_CS3 + (c * 128 + tid) * 4, rws);
      }
      waitbar<0>();
      if (w == 0) signal(l < 16 ? WS_CF + c * 128 + l * 8 : OOB);
    }
    {
      const rsrc_t rw4 = mk_rsrc(w4, 1536 * 1280);
      u32 d = 0;
      touch(d, (m * 960 + tid) * 128, rw4); touch(d, tid < 448 ? (m * 960 + 512 + tid) * 128 : OOB, rw4);
      asm volatile("s_waitcnt vmcnt(0)" : "+v"(d)::"memory");
    }
    if (m == 0) {
#pragma unroll
      for (int i = 0; i < 4; ++i) (void)poll_granule(WS_XS + (i * 512 + tid) * 16, true, rws, ta, tb);
      bar();
      if (tid == 0) st64_sc1((u32x2){ta, tb}, WS_EPOCH, rws);
    }
  }
#endif
}

extern "C" void kernel_launch(void* const* d_in, const int* in_sizes, int n_in, void* d_out, int out_size,
                              void* d_ws, size_t ws_size, hipStream_t stream) {
  (void)in_sizes; (void)n_in; (void)out_size;
  s5_mixer_kernel<<<256, 768, 0, stream>>>(d_in[0], d_in[1], d_in[2], d_in[3], d_in[4], d_in[5], d_in[6], d_in[7],
                                           d_in[8], d_in[10], d_in[11], d_in[12], d_in[13], d_out,
                                           (char*)d_ws + ((ws_size - WS_BYTES) & ~(size_t)0xFFFF));
}
```
